```python
import torch

def _softmax(x, axis=-1):
    return torch.softmax(x, dim=axis)
def _gelu(x):
    return torch.nn.functional.gelu(x, approximate="tanh")
def _silu(x):
    return torch.nn.functional.silu(x)
def _sigmoid(x):
    return torch.sigmoid(x)
def _layernorm(x, axis=-1, eps=1e-5):
    m = x.mean(dim=axis, keepdim=True)
    v = x.var(dim=axis, keepdim=True, unbiased=False)
    return (x - m) / torch.sqrt(v + eps)
def _scatter_add(tgt, idx, src, axis=0):
    return tgt.clone().index_add_(axis, idx, src)
def _segment_sum(src, seg, n):
    out = torch.zeros((n,) + src.shape[1:], dtype=src.dtype)
    return out.index_add_(0, seg, src)
def _pad(x, pw):
    flat = tuple(v for pair in reversed(pw) for v in pair)
    return torch.nn.functional.pad(x, flat)

kernel_name = 'standalone__mha_block_cached__near_ridge'

def setup_inputs():

    _w = (lambda a: (lambda t: t.to(torch.bfloat16) if t.is_floating_point() else t)(torch.as_tensor(a)))
    B, Sq, D = 8, 224, 448
    H, Sk, Dh = 16, 1024, 256
    return {
        "x": _w((lambda *s: torch.randn(*s, dtype=torch.bfloat16) * 0.8)(B, Sq, D)),
        "k_cache": _w((lambda *s: torch.randn(*s, dtype=torch.bfloat16) * 0.8)(B, H, Sk, Dh)),
        "v_cache": _w((lambda *s: torch.randn(*s, dtype=torch.bfloat16) * 0.05)(B, H, Sk, Dh)),
        "wq": _w((lambda *s: torch.randn(*s, dtype=torch.bfloat16) * 0.8)(D, H * Dh) * (D ** -0.5)),
        "wo": _w((lambda *s: torch.randn(*s, dtype=torch.bfloat16) * 0.05)(H * Dh, D) * ((H * Dh) ** -0.5)),
    }

def reference(x, k_cache, v_cache, wq, wo):
    B, Sq, D = x.shape
    H, Dh = 16, 256
    q = (lambda x, *d: x.permute(*d))((x @ wq).reshape(B, Sq, H, Dh), 0, 2, 1, 3)
    scores = torch.einsum("bhqd,bhkd->bhqk", q, k_cache) * (Dh ** -0.5)
    p = _softmax(scores, axis=-1)
    o = torch.einsum("bhqk,bhkd->bhqd", p, v_cache)
    return (lambda x, *d: x.permute(*d))(o, 0, 2, 1, 3).reshape(B, Sq, H * Dh) @ wo
```

```cpp
#include <hip/hip_runtime.h>
#include <hip/hip_bf16.h>

typedef __bf16 bf16x8 __attribute__((ext_vector_type(8)));
typedef __bf16 bf16x4 __attribute__((ext_vector_type(4)));
typedef __bf16 bf16x2 __attribute__((ext_vector_type(2)));
typedef float f32x16 __attribute__((ext_vector_type(16)));
typedef float f32x2 __attribute__((ext_vector_type(2)));
typedef unsigned u32x4 __attribute__((ext_vector_type(4)));
#define LDSP(T) __attribute__((address_space(3))) T*
#define DI __device__ __forceinline__
#define SRD3 0x00020000

#define KV_POL "nt"
#define W_POL ""

constexpr int NB = 8, SQ = 224, DM = 448, NH = 16, SK = 1024, DH = 256;
constexpr int NS = 10, SLOT = 16384;
constexpr int N_Q = 21, N_ATT = 64, N_O = 14, N_ALL = N_Q + N_ATT + N_O;

DI unsigned rfl(unsigned x) { return __builtin_amdgcn_readfirstlane(x); }
DI void dma_kv(unsigned m0v, unsigned voff, __amdgpu_buffer_rsrc_t srd, unsigned soff) {
  asm volatile("s_mov_b32 m0, %0\n\ts_nop 0\n\tbuffer_load_dwordx4 %1, %2, %3 offen " KV_POL " lds" :: "s"(m0v), "v"(voff), "s"(srd), "s"(soff) : "memory");
}
DI void dma_w(unsigned m0v, unsigned voff, __amdgpu_buffer_rsrc_t srd, unsigned soff) {
  asm volatile("s_mov_b32 m0, %0\n\ts_nop 0\n\tbuffer_load_dwordx4 %1, %2, %3 offen " W_POL " lds" :: "s"(m0v), "v"(voff), "s"(srd), "s"(soff) : "memory");
}
DI unsigned cvtpk(float lo, float hi) { f32x2 v = {lo, hi}; bf16x2 b = __builtin_convertvector(v, bf16x2); return __builtin_bit_cast(unsigned, b); }
DI bf16x8 pack8(const f32x16& a, int base, float sc) {
  u32x4 w;
  #pragma unroll
  for (int e = 0; e < 4; ++e) w[e] = cvtpk(a[base + 2 * e] * sc, a[base + 2 * e + 1] * sc);
  return __builtin_bit_cast(bf16x8, w);
}
DI bf16x8 ldrow(const char* lds, unsigned off) { return *(const bf16x8*)(lds + off); }
DI bf16x8 ldtr(const char* lds, unsigned off0, unsigned off1) {
  bf16x4 a = __builtin_amdgcn_ds_read_tr16_b64_v4bf16((LDSP(bf16x4))(lds + off0));
  bf16x4 b = __builtin_amdgcn_ds_read_tr16_b64_v4bf16((LDSP(bf16x4))(lds + off1));
  return __builtin_shufflevector(a, b, 0, 1, 2, 3, 4, 5, 6, 7);
}
#define MFMA(a, b, c) __builtin_amdgcn_mfma_f32_32x32x16_bf16(a, b, c, 0, 0, 0)

__global__ __launch_bounds__(256, 1) void mha_main(const char* __restrict__ x, const char* __restrict__ kc, const char* __restrict__ vc,
                                                   const char* __restrict__ wq, const char* __restrict__ wo, char* __restrict__ part) {
  extern __shared__ __attribute__((aligned(16))) char lds[];
  const unsigned tid = threadIdx.x, l = tid & 63, w = rfl(tid >> 6);
  const unsigned bid = blockIdx.x, xcd = bid & 7, idx = bid >> 3, p_ = idx >> 1, half = idx & 1;
  const unsigned b = (xcd >> 2) * 4 + (p_ >> 2), hd = (xcd & 3) * 4 + (p_ & 3);
  const unsigned qt = min(4 * half + w, 6u);
  const bool active = (4 * half + w) < 7;
  const unsigned ldsb = (unsigned)(size_t)lds;

  const __amdgpu_buffer_rsrc_t sK = __builtin_amdgcn_make_buffer_rsrc((void*)(kc + (size_t)(b * NH + hd) * (SK * DH * 2)), (short)0, SK * DH * 2, SRD3);
  const __amdgpu_buffer_rsrc_t sV = __builtin_amdgcn_make_buffer_rsrc((void*)(vc + (size_t)(b * NH + hd) * (SK * DH * 2)), (short)0, SK * DH * 2, SRD3);
  const __amdgpu_buffer_rsrc_t sX = __builtin_amdgcn_make_buffer_rsrc((void*)(x + (size_t)b * (SQ * DM * 2)), (short)0, SQ * DM * 2, SRD3);
  const __amdgpu_buffer_rsrc_t sWq = __builtin_amdgcn_make_buffer_rsrc((void*)(wq + hd * DH * 2), (short)0, DM * NH * DH * 2 - hd * DH * 2, SRD3);
  const __amdgpu_buffer_rsrc_t sWo = __builtin_amdgcn_make_buffer_rsrc((void*)(wo + (size_t)hd * DH * DM * 2), (short)0, DH * DM * 2, SRD3);

  const unsigned r8 = l >> 3, c8 = l & 7;
  const unsigned swc = c8 ^ (4 * (w & 1) + (l >> 4));
  const unsigned swt = c8 ^ (((l >> 4) & 1) << 2);
  const unsigned vK = (8 * w + r8) * 512 + 16 * swc;
  const unsigned vV = (8 * w + r8) * 512 + 16 * swt;
  const unsigned vW = (8 * w + r8) * 8192 + 16 * swt;
  const unsigned vX0 = (32 * qt + r8) * 896 + 16 * (c8 ^ (l >> 4));
  const unsigned vX1 = (32 * qt + r8) * 896 + 16 * (c8 ^ (4 + (l >> 4)));
  const unsigned vO = r8 * 896 + 16 * swt;

  auto issue = [&](int n) {
    const unsigned dst = ldsb + (unsigned)(n % NS) * SLOT + w * 4096;
    if (n < N_Q) {
      const int c = n / 3, k = n % 3;
      if (k == 0) {
        #pragma unroll
        for (int i = 0; i < 4; ++i) dma_w(rfl(dst + 1024 * i), (i & 1) ? vX1 : vX0, sX, rfl(8 * i * 896 + 128 * c));
      } else {
        #pragma unroll
        for (int i = 0; i < 4; ++i) dma_w(rfl(dst + 1024 * i), vW, sWq, rfl((64 * c + 32 * (k - 1)) * 8192 + 128 * i));
      }
    } else if (n < N_Q + N_ATT) {
      const int m = n - N_Q, t = m >> 1;
      if (m & 1) {
        #pragma unroll
        for (int i = 0; i < 4; ++i) dma_kv(rfl(dst + 1024 * i), vV, sV, rfl(t * 16384 + 128 * i));
      } else {
        #pragma unroll
        for (int i = 0; i < 4; ++i) dma_kv(rfl(dst + 1024 * i), vK, sK, rfl(t * 16384 + 128 * i));
      }
    } else {
      const int m = n - N_Q - N_ATT, j = m >> 1, dh = m & 1;
      #pragma unroll
      for (int i = 0; i < 4; ++i) dma_w(rfl(dst + 1024 * i), vO, sWo, rfl(m < N_O ? (128 * dh + 8 * (4 * w + i)) * 896 + 128 * j : 0x7ff00000u));
    }
  };
  auto slot = [&](int n) { return (unsigned)(n % NS) * SLOT; };

  const unsigned r = l & 31, h = l >> 5, bb = (l >> 4) & 1, q = (l & 15) >> 2, p = l & 3;
  unsigned aK[4], aX[4];
  #pragma unroll
  for (int s = 0; s < 4; ++s) {
    aK[s] = 4096 * (r >> 3) + 128 * (r & 7) + 16 * ((2 * s + h) ^ ((r >> 1) & 7));
    aX[s] = 4096 * w + 1024 * (r >> 3) + 128 * (r & 7) + 16 * ((2 * s + h) ^ ((r >> 1) & 7));
  }
  unsigned aV[2], aW[2], aO[2];
  #pragma unroll
  for (int d1 = 0; d1 < 2; ++d1) {
    aV[d1] = 128 * (4 * h + q) + 16 * ((4 * d1 + 2 * bb + (p >> 1)) ^ ((q >> 1) << 2)) + 8 * (p & 1);
    aW[d1] = 4096 * h + 128 * q + 16 * ((4 * d1 + 2 * bb + (p & 1)) ^ ((q >> 1) << 2)) + 8 * (p >> 1);
    aO[d1] = 128 * (4 * h + q) + 16 * ((4 * d1 + 2 * bb + (p & 1)) ^ ((q >> 1) << 2)) + 8 * (p >> 1);
  }

  #pragma unroll
  for (int n = 0; n < NS; ++n) issue(n);
  int nissued = NS;

  f32x16 acc[8];
  #pragma unroll
  for (int i = 0; i < 8; ++i) acc[i] = (f32x16){};
  for (int c = 0; c < 7; ++c) {
    asm volatile("s_waitcnt vmcnt(%0)" :: "i"(4 * (NS - 3)) : "memory");
    __builtin_amdgcn_s_barrier();
    const unsigned sx = slot(3 * c);
    #pragma unroll
    for (int hf = 0; hf < 2; ++hf) {
      const unsigned sw = slot(3 * c + 1 + hf);
      #pragma unroll
      for (int s = 0; s < 2; ++s) {
        const bf16x8 xf = ldrow(lds, sx + aX[2 * hf + s]);
        #pragma unroll
        for (int dt = 0; dt < 8; ++dt) {
          const unsigned o = sw + aW[dt & 1] + 8192 * s + 1024 * (dt >> 1);
          acc[dt] = MFMA(ldtr(lds, o, o + 512), xf, acc[dt]);
        }
      }
    }
    asm volatile("s_waitcnt lgkmcnt(0)" ::: "memory");
    __builtin_amdgcn_s_barrier();
    issue(nissued); issue(nissued + 1); issue(nissued + 2); nissued += 3;
  }
  const float CS = 0.09016844005556021f;
  bf16x8 Qf[16];
  #pragma unroll
  for (int dt = 0; dt < 8; ++dt) { Qf[2 * dt] = pack8(acc[dt], 0, CS); Qf[2 * dt + 1] = pack8(acc[dt], 8, CS); }

  #pragma unroll
  for (int i = 0; i < 8; ++i) acc[i] = (f32x16){};
  float m = -1e30f, lsum = 0.f;
  for (int t = 0; t < 32; ++t) {
    asm volatile("s_waitcnt vmcnt(%0)" :: "i"(4 * (NS - 2)) : "memory");
    __builtin_amdgcn_s_barrier();
    const unsigned sk = slot(N_Q + 2 * t), sv = slot(N_Q + 2 * t + 1);
    f32x16 S = (f32x16){};
    #pragma unroll
    for (int s = 0; s < 16; ++s) S = MFMA(ldrow(lds, sk + aK[s & 3] + 1024 * (s >> 2)), Qf[s], S);
    float mx = S[0];
    #pragma unroll
    for (int e = 1; e < 16; ++e) mx = fmaxf(mx, S[e]);
    mx = fmaxf(mx, __shfl_xor(mx, 32));
    const float mn = fmaxf(m, mx);
    const float al = __builtin_amdgcn_exp2f(m - mn);
    m = mn;
    float rs = 0.f;
    #pragma unroll
    for (int e = 0; e < 16; ++e) { S[e] = __builtin_amdgcn_exp2f(S[e] - mn); rs += S[e]; }
    lsum = lsum * al + rs;
    if (__any(al != 1.f)) {
      #pragma unroll
      for (int dt = 0; dt < 8; ++dt) acc[dt] *= al;
    }
    const bf16x8 P0 = pack8(S, 0, 1.f), P1 = pack8(S, 8, 1.f);
    #pragma unroll
    for (int dt = 0; dt < 8; ++dt) {
      const unsigned o = sv + aV[dt & 1] + 1024 * (dt >> 1);
      acc[dt] = MFMA(ldtr(lds, o, o + 4096), P0, acc[dt]);
      acc[dt] = MFMA(ldtr(lds, o + 8192, o + 12288), P1, acc[dt]);
    }
    asm volatile("s_waitcnt lgkmcnt(0)" ::: "memory");
    __builtin_amdgcn_s_barrier();
    issue(nissued); issue(nissued + 1); nissued += 2;
  }
  lsum += __shfl_xor(lsum, 32);
  const float inv = 1.f / lsum;
  bf16x8 Of[16];
  #pragma unroll
  for (int dt = 0; dt < 8; ++dt) { Of[2 * dt] = pack8(acc[dt], 0, inv); Of[2 * dt + 1] = pack8(acc[dt], 8, inv); }

  char* pw = part + ((size_t)bid * 4 + w) * (7 * 4 * 1024) + l * 16;
  for (int j = 0; j < 7; ++j) {
    asm volatile("s_waitcnt vmcnt(0)" ::: "memory");
    __builtin_amdgcn_s_barrier();
    f32x16 o2[2] = {(f32x16){}, (f32x16){}};
    #pragma unroll
    for (int dh = 0; dh < 2; ++dh) {
      const unsigned so = slot(N_Q + N_ATT + 2 * j + dh);
      #pragma unroll
      for (int d4 = 0; d4 < 4; ++d4)
        #pragma unroll
        for (int u = 0; u < 2; ++u)
          #pragma unroll
          for (int nt = 0; nt < 2; ++nt) {
            const unsigned o = so + aO[nt] + 1024 * (4 * d4 + 2 * u);
            o2[nt] = MFMA(ldtr(lds, o, o + 1024), Of[2 * (4 * dh + d4) + u], o2[nt]);
          }
    }
    asm volatile("s_waitcnt lgkmcnt(0)" ::: "memory");
    __builtin_amdgcn_s_barrier();
    issue(nissued); issue(nissued + 1); nissued += 2;
    if (active) {
      #pragma unroll
      for (int nt = 0; nt < 2; ++nt)
        #pragma unroll
        for (int u = 0; u < 2; ++u)
          *(bf16x8*)(pw + ((j * 2 + nt) * 2 + u) * 1024) = pack8(o2[nt], 8 * u, 1.f);
    }
  }
}

__global__ __launch_bounds__(256) void mha_reduce(const char* __restrict__ part, char* __restrict__ out) {
  const unsigned gw = (blockIdx.x * 256 + threadIdx.x) >> 6, l = threadIdx.x & 63;
  const unsigned j = gw % 7, rg = gw / 7;
  const unsigned b = rg / 28, row = (rg % 28) * 8 + (l >> 3), cn = l & 7;
  const unsigned qt = row >> 5, half = qt >> 2, w = qt & 3, qc = row & 31;
  const unsigned nt = cn >> 2, u = (cn >> 1) & 1, h = cn & 1;
  const unsigned inner = ((j * 2 + nt) * 2 + u) * 1024 + (h * 32 + qc) * 16;
  float a[8] = {};
  u32x4 v[16];
  #pragma unroll
  for (int hd = 0; hd < 16; ++hd) {
    const unsigned xcd = (b >> 2) * 4 + (hd >> 2), p_ = (b & 3) * 4 + (hd & 3);
    const unsigned bid = (p_ * 2 + half) * 8 + xcd;
    v[hd] = *(const u32x4*)(part + ((size_t)bid * 4 + w) * (7 * 4 * 1024) + inner);
  }
  #pragma unroll
  for (int hd = 0; hd < 16; ++hd)
    #pragma unroll
    for (int e = 0; e < 4; ++e) { a[2 * e] += __uint_as_float(v[hd][e] << 16); a[2 * e + 1] += __uint_as_float(v[hd][e] & 0xffff0000u); }
  u32x4 o;
  #pragma unroll
  for (int e = 0; e < 4; ++e) o[e] = cvtpk(a[2 * e], a[2 * e + 1]);
  *(u32x4*)(out + ((size_t)(b * SQ + row) * DM + 64 * j + 8 * cn) * 2) = o;
}

extern "C" void kernel_launch(void* const* d_in, const int* in_sizes, int n_in, void* d_out, int out_size, void* d_ws, size_t ws_size, hipStream_t stream) {
  (void)hipFuncSetAttribute((const void*)mha_main, hipFuncAttributeMaxDynamicSharedMemorySize, NS * SLOT);
  mha_main<<<256, 256, NS * SLOT, stream>>>((const char*)d_in[0], (const char*)d_in[1], (const char*)d_in[2], (const char*)d_in[3], (const char*)d_in[4], (char*)d_ws);
  mha_reduce<<<392, 256, 0, stream>>>((const char*)d_ws, (char*)d_out);
}
```

```cpp
#include <hip/hip_runtime.h>
#include <hip/hip_bf16.h>

typedef __bf16 bf16x2 __attribute__((ext_vector_type(2)));
typedef float f32x2 __attribute__((ext_vector_type(2)));
typedef unsigned u32x4 __attribute__((ext_vector_type(4)));
typedef __attribute__((address_space(1))) unsigned gu32;
#define SRD3 0x00020000

constexpr int NB = 8, SQ = 224, DM = 448, NH = 16, SK = 1024, DH = 256;
constexpr int LDS_BYTES = 163840;
constexpr unsigned PART_WAVE = 7 * 4 * 1024;

__device__ __forceinline__ unsigned rfl(unsigned x) { return __builtin_amdgcn_readfirstlane(x); }
__device__ __forceinline__ unsigned cvtpk(float lo, float hi) {
  f32x2 v = {lo, hi}; bf16x2 b = __builtin_convertvector(v, bf16x2); return __builtin_bit_cast(unsigned, b);
}

__global__ __launch_bounds__(256, 1) void mha_main(const char* __restrict__ x, const char* __restrict__ kc, const char* __restrict__ vc,
                                                   const char* __restrict__ wq, const char* __restrict__ wo, char* __restrict__ part,
                                                   unsigned* __restrict__ flags, char* __restrict__ out) {
  extern __shared__ __attribute__((aligned(16))) char lds[];
  const unsigned tid = threadIdx.x, l = tid & 63, w = rfl(tid >> 6);
  const unsigned bid = blockIdx.x, xcd = bid & 7, idx = bid >> 3, pr = idx >> 1, half = idx & 1;
  const unsigned b = (xcd >> 2) * 4 + (pr >> 2), hd = (xcd & 3) * 4 + (pr & 3);
  const unsigned qt = 4 * half + w;
  const unsigned ldsb = (unsigned)(size_t)lds;

  const __amdgpu_buffer_rsrc_t sX = __builtin_amdgcn_make_buffer_rsrc((void*)(x + (size_t)b * (SQ * DM * 2)), (short)0, SQ * DM * 2, SRD3);
  const __amdgpu_buffer_rsrc_t sWq = __builtin_amdgcn_make_buffer_rsrc((void*)(wq + hd * DH * 2), (short)0, DM * NH * DH * 2 - hd * DH * 2, SRD3);
  const unsigned r8 = l >> 3, c8 = l & 7;
  const unsigned swt = c8 ^ (((l >> 4) & 1) << 2);
  const unsigned vW = (8 * w + r8) * 8192 + 16 * swt;
  const unsigned vX0 = (32 * qt + r8) * 896 + 16 * (c8 ^ (l >> 4));
  const unsigned mA = rfl(ldsb + w * 4096);
  const unsigned g8 = (b & 3) * 2 + half;
  const unsigned c0 = rfl((g8 + 2 * (hd & 3)) % 7);
  asm volatile(
      ".Lmha_begin:\n"
      "s_getpc_b64 s[56:57]\n"
      "v_mbcnt_lo_u32_b32 v235, -1, 0\n"
      "v_mbcnt_hi_u32_b32 v235, -1, v235\n"
      "v_and_b32 v235, 3, v235\n"
      "s_lshl_b32 s58, %6, 2\n"
      "v_add_u32 v235, s58, v235\n"
      "v_lshlrev_b32 v235, 7, v235\n"
      "s_mov_b32 s58, .Lmha_end-.Lmha_begin-8\n"
      "v_min_u32 v235, s58, v235\n"
      "v_and_b32 v235, -4, v235\n"
      "s_add_u32 m0, %2, 49152\n"
      "s_nop 0\n"
      "global_load_lds_dword v235, s[56:57]\n"
      "s_add_u32 s41, %2, 65536\n"
      "s_add_u32 s42, %2, 131072\n"
      "s_lshl_b32 s45, %3, 19\n"
      "s_lshl_b32 s46, %3, 7\n"
      "v_mov_b32 v218, %5\n"
      "v_mov_b32 v219, %4\n"
      "v_xor_b32 v220, 64, %4\n"
      "v_add_u32 v220, 7168, v220\n"
      "v_add_u32 v221, 14336, %4\n"
      "v_xor_b32 v222, 64, %4\n"
      "v_add_u32 v222, 21504, v222\n"
      "s_add_u32 m0, s42, 0\n"
      "s_nop 0\n"
      "buffer_load_dwordx4 v219, %0, s46 offen lds\n"
      "s_add_u32 m0, s42, 1024\n"
      "s_nop 0\n"
      "buffer_load_dwordx4 v220, %0, s46 offen lds\n"
      "s_add_u32 m0, s42, 2048\n"
      "s_nop 0\n"
      "buffer_load_dwordx4 v221, %0, s46 offen lds\n"
      "s_add_u32 m0, s42, 3072\n"
      "s_nop 0\n"
      "buffer_load_dwordx4 v222, %0, s46 offen lds\n"
      "s_add_u32 s46, s46, 128\n"
      "s_cmp_eq_u32 s46, 896\n"
      "s_cselect_b32 s46, 0, s46\n"
      "s_add_u32 m0, s41, 0\n"
      "s_nop 0\n"
      "buffer_load_dwordx4 v218, %1, s45 offen offset:0 lds\n"
      "s_add_u32 m0, s41, 896\n"
      "s_nop 0\n"
      "buffer_load_dwordx4 v218, %1, s45 offen offset:128 lds\n"
      "s_add_u32 m0, s41, 1792\n"
      "s_nop 0\n"
      "buffer_load_dwordx4 v218, %1, s45 offen offset:256 lds\n"
      "s_add_u32 m0, s41, 2688\n"
      "s_nop 0\n"
      "buffer_load_dwordx4 v218, %1, s45 offen offset:384 lds\n"
      "s_add_u32 s45, s45, 0x40000\n"
      "s_cmp_eq_u32 s45, 0x380000\n"
      "s_cselect_b32 s45, 0, s45\n"
      "s_add_u32 m0, s41, 16384\n"
      "s_nop 0\n"
      "buffer_load_dwordx4 v218, %1, s45 offen offset:0 lds\n"
      "s_add_u32 m0, s41, 17280\n"
      "s_nop 0\n"
      "buffer_load_dwordx4 v218, %1, s45 offen offset:128 lds\n"
      "s_add_u32 m0, s41, 18176\n"
      "s_nop 0\n"
      "buffer_load_dwordx4 v218, %1, s45 offen offset:256 lds\n"
      "s_add_u32 m0, s41, 19072\n"
      "s_nop 0\n"
      "buffer_load_dwordx4 v218, %1, s45 offen offset:384 lds\n"
      "s_add_u32 s45, s45, 0x40000\n"
      "s_cmp_eq_u32 s45, 0x380000\n"
      "s_cselect_b32 s45, 0, s45\n"
      "s_add_u32 m0, s42, 16384\n"
      "s_nop 0\n"
      "buffer_load_dwordx4 v219, %0, s46 offen lds\n"
      "s_add_u32 m0, s42, 17408\n"
      "s_nop 0\n"
      "buffer_load_dwordx4 v220, %0, s46 offen lds\n"
      "s_add_u32 m0, s42, 18432\n"
      "s_nop 0\n"
      "buffer_load_dwordx4 v221, %0, s46 offen lds\n"
      "s_add_u32 m0, s42, 19456\n"
      "s_nop 0\n"
      "buffer_load_dwordx4 v222, %0, s46 offen lds\n"
      "s_add_u32 s46, s46, 128\n"
      "s_cmp_eq_u32 s46, 896\n"
      "s_cselect_b32 s46, 0, s46\n"
      :
      : "s"(sX), "s"(sWq), "s"(mA), "s"(c0), "v"(vX0), "v"(vW), "s"(rfl(idx * 4 + w))
      : "s41", "s42", "s45", "s46", "s56", "s57", "s58", "v218", "v219", "v220", "v221", "v222", "v235", "scc", "memory");
  __builtin_amdgcn_sched_barrier(0);

  const __amdgpu_buffer_rsrc_t sK = __builtin_amdgcn_make_buffer_rsrc((void*)(kc + (size_t)(b * NH + hd) * (SK * DH * 2)), (short)0, SK * DH * 2, SRD3);
  const __amdgpu_buffer_rsrc_t sV = __builtin_amdgcn_make_buffer_rsrc((void*)(vc + (size_t)(b * NH + hd) * (SK * DH * 2)), (short)0, SK * DH * 2, SRD3);
  const __amdgpu_buffer_rsrc_t sWo = __builtin_amdgcn_make_buffer_rsrc((void*)(wo + (size_t)hd * DH * DM * 2), (short)0, DH * DM * 2, SRD3);
  const __amdgpu_buffer_rsrc_t sP = __builtin_amdgcn_make_buffer_rsrc((void*)(part + ((size_t)bid * 4 + w) * PART_WAVE), (short)0, PART_WAVE, SRD3);

  const unsigned regK = 0u, regV = 65536u;
  const unsigned vK = (8 * w + r8) * 512 + 16 * (c8 ^ (4 * (w & 1) + (l >> 4)));
  const unsigned vV = (8 * w + r8) * 512 + 16 * swt;
  const unsigned vO0 = (32 * w + r8) * 896 + 16 * swt;
  const unsigned r = l & 31, h = l >> 5, bb = (l >> 4) & 1, q = (l & 15) >> 2, p = l & 3;
  const unsigned aK0 = ldsb + regK + 4096 * (r >> 3) + 128 * (r & 7) + 16 * (h ^ ((r >> 1) & 7));
  const unsigned aX0 = ldsb + 131072 + 4096 * w + 1024 * (r >> 3) + 128 * (r & 7) + 16 * (h ^ ((r >> 1) & 7));
  const unsigned aV0 = ldsb + regV + 128 * (4 * h + q) + 16 * ((2 * bb + (p >> 1)) ^ ((q >> 1) << 2)) + 8 * (p & 1);
  const unsigned aW0 = ldsb + 65536 + 4096 * h + 128 * q + 16 * ((2 * bb + (p & 1)) ^ ((q >> 1) << 2)) + 8 * (p >> 1);
  const unsigned aO0 = ldsb + 128 * (4 * h + q) + 16 * ((2 * bb + (p & 1)) ^ ((q >> 1) << 2)) + 8 * (p >> 1);
  const unsigned vSt = l * 16;

  const unsigned kv0 = rfl(((4 * pr) & 31) * 16384);
  const unsigned j0 = rfl((g8 + 3) % 7);
  const unsigned prog = (qt + 1) >> 3;

  gu32* const gflag = (gu32*)(flags + (b * 2 + half) * 128);
  unsigned eb;

  asm volatile(
      ".Lmha_main:\n"
      "s_getpc_b64 s[56:57]\n"
      "s_add_u32 s70, s56, .Lmha_late-.Lmha_main-4\n"
      "s_addc_u32 s71, s57, 0\n"
      "s_mov_b32 s74, s56\n"
      "s_mov_b32 s75, s57\n"
      "v_mbcnt_lo_u32_b32 v229, -1, 0\n"
      "v_mbcnt_hi_u32_b32 v229, -1, v229\n"
      "v_and_b32 v229, 3, v229\n"
      "s_lshl_b32 s60, %13, 2\n"
      "v_add_u32 v229, s60, v229\n"
      "v_lshlrev_b32 v229, 7, v229\n"
      "s_mov_b32 s60, .Lmha_late-.Lmha_main-8\n"
      "v_min_u32 v229, s60, v229\n"
      "v_and_b32 v229, -4, v229\n"
      "v_mbcnt_lo_u32_b32 v231, -1, 0\n"
      "v_mbcnt_hi_u32_b32 v231, -1, v231\n"
      "v_and_b32 v231, 3, v231\n"
      "s_lshl_b32 s60, %13, 2\n"
      "v_add_u32 v231, s60, v231\n"
      "v_lshlrev_b32 v231, 7, v231\n"
      "s_mov_b32 s60, .Lmha_end-.Lmha_late-8\n"
      "v_min_u32 v231, s60, v231\n"
      "v_and_b32 v231, -4, v231\n"
      "s_cmp_eq_u32 %11, 1\n"
      "s_cbranch_scc1 Lp1_%=\n"
      "s_mov_b32 s40, %7\n"
      "s_add_u32 s41, %7, 65536\n"
      "s_add_u32 s42, %7, 131072\n"
      "s_mov_b32 s43, %8\n"
      "s_mov_b32 s44, %8\n"
      "s_lshl_b32 s45, %9, 19\n"
      "s_lshl_b32 s46, %9, 7\n"
      "s_lshl_b32 s47, %10, 7\n"
      "s_add_u32 s48, s47, 114688\n"
      "s_lshl_b32 s49, %10, 12\n"
      "s_mov_b32 s50, 0\n"
      "s_mov_b32 s52, 0x3db8aa3b\n"
      "s_mov_b32 s53, 0x3db8aa3b\n"
      "s_mov_b32 s72, 0x9e3779b1\n"
      "v_mov_b32 v218, %18\n"
      "v_mov_b32 v219, %19\n"
      "v_xor_b32 v220, 64, %19\n"
      "v_add_u32 v220, 7168, v220\n"
      "v_mov_b32 v221, %19\n"
      "v_add_u32 v221, 14336, v221\n"
      "v_xor_b32 v222, 64, %19\n"
      "v_add_u32 v222, 21504, v222\n"
      "s_add_u32 s46, s46, 128\n"
      "s_cmp_eq_u32 s46, 896\n"
      "s_cselect_b32 s46, 0, s46\n"
      "s_add_u32 s45, s45, 0x40000\n"
      "s_cmp_eq_u32 s45, 0x380000\n"
      "s_cselect_b32 s45, 0, s45\n"
      "s_add_u32 s45, s45, 0x40000\n"
      "s_cmp_eq_u32 s45, 0x380000\n"
      "s_cselect_b32 s45, 0, s45\n"
      "s_add_u32 s46, s46, 128\n"
      "s_cmp_eq_u32 s46, 896\n"
      "s_cselect_b32 s46, 0, s46\n"
      "v_mov_b32 v233, 0\n"
      "v_mov_b32 v216, %16\n"
      "v_mov_b32 v217, %17\n"
      "v_mov_b32 v223, %20\n"
      "v_add_u32 v224, 7168, %20\n"
      "v_add_u32 v225, 14336, %20\n"
      "v_add_u32 v226, 21504, %20\n"
      "v_xor_b32 v200, 0, %21\n"
      "v_xor_b32 v208, 0, %22\n"
      "v_xor_b32 v201, 32, %21\n"
      "v_xor_b32 v209, 32, %22\n"
      "v_xor_b32 v202, 64, %21\n"
      "v_xor_b32 v210, 64, %22\n"
      "v_xor_b32 v203, 96, %21\n"
      "v_xor_b32 v211, 96, %22\n"
      "v_xor_b32 v204, 0, %23\n"
      "v_xor_b32 v206, 0, %24\n"
      "v_xor_b32 v212, 0, %25\n"
      "v_add_u32 v214, 65536, v212\n"
      "v_xor_b32 v205, 64, %23\n"
      "v_xor_b32 v207, 64, %24\n"
      "v_xor_b32 v213, 64, %25\n"
      "v_add_u32 v215, 65536, v213\n"
      "v_mov_b32 v227, %26\n"
      "s_waitcnt vmcnt(8) lgkmcnt(0)\n"
      "s_barrier\n"
      "ds_read_b128 a[192:195], v208 offset:0\n"
      "ds_read_b128 a[196:199], v209 offset:0\n"
      "ds_read_b128 a[200:203], v210 offset:0\n"
      "ds_read_b128 a[204:207], v211 offset:0\n"
      "ds_read_b64_tr_b16 a[64:65], v206 offset:0\n"
      "ds_read_b64_tr_b16 a[66:67], v206 offset:512\n"
      "ds_read_b64_tr_b16 a[68:69], v207 offset:0\n"
      "ds_read_b64_tr_b16 a[70:71], v207 offset:512\n"
      "ds_read_b64_tr_b16 a[72:73], v206 offset:1024\n"
      "ds_read_b64_tr_b16 a[74:75], v206 offset:1536\n"
      "ds_read_b64_tr_b16 a[76:77], v207 offset:1024\n"
      "ds_read_b64_tr_b16 a[78:79], v207 offset:1536\n"
      "ds_read_b64_tr_b16 a[80:81], v206 offset:2048\n"
      "ds_read_b64_tr_b16 a[82:83], v206 offset:2560\n"
      "ds_read_b64_tr_b16 a[84:85], v207 offset:2048\n"
      "ds_read_b64_tr_b16 a[86:87], v207 offset:2560\n"
      "ds_read_b64_tr_b16 a[88:89], v206 offset:3072\n"
      "ds_read_b64_tr_b16 a[90:91], v206 offset:3584\n"
      "ds_read_b64_tr_b16 a[92:93], v207 offset:3072\n"
      "ds_read_b64_tr_b16 a[94:95], v207 offset:3584\n"
      "ds_read_b64_tr_b16 a[96:97], v206 offset:8192\n"
      "ds_read_b64_tr_b16 a[98:99], v206 offset:8704\n"
      "ds_read_b64_tr_b16 a[100:101], v207 offset:8192\n"
      "ds_read_b64_tr_b16 a[102:103], v207 offset:8704\n"
      "ds_read_b64_tr_b16 a[104:105], v206 offset:9216\n"
      "ds_read_b64_tr_b16 a[106:107], v206 offset:9728\n"
      "ds_read_b64_tr_b16 a[108:109], v207 offset:9216\n"
      "ds_read_b64_tr_b16 a[110:111], v207 offset:9728\n"
      "ds_read_b64_tr_b16 a[112:113], v206 offset:10240\n"
      "ds_read_b64_tr_b16 a[114:115], v206 offset:10752\n"
      "ds_read_b64_tr_b16 a[116:117], v207 offset:10240\n"
      "ds_read_b64_tr_b16 a[118:119], v207 offset:10752\n"
      "ds_read_b64_tr_b16 a[120:121], v206 offset:11264\n"
      "ds_read_b64_tr_b16 a[122:123], v206 offset:11776\n"
      "ds_read_b64_tr_b16 a[124:125], v207 offset:11264\n"
      "ds_read_b64_tr_b16 a[126:127], v207 offset:11776\n"
      "s_waitcnt vmcnt(4) lgkmcnt(0)\n"
      "s_barrier\n"
      "v_mfma_f32_32x32x16_bf16 v[0:15], a[64:67], a[192:195], 0\n"
      "s_add_u32 m0, s41, 32768\n"
      "ds_read_b64_tr_b16 a[128:129], v206 offset:16384\n"
      "buffer_load_dwordx4 v218, %4, s45 offen offset:0 lds\n"
      "ds_read_b64_tr_b16 a[144:145], v206 offset:18432\n"
      "ds_read_b64_tr_b16 a[146:147], v206 offset:18944\n"
      "v_mfma_f32_32x32x16_bf16 v[16:31], a[68:71], a[192:195], 0\n"
      "ds_read_b64_tr_b16 a[148:149], v207 offset:18432\n"
      "ds_read_b64_tr_b16 a[150:151], v207 offset:18944\n"
      "v_mfma_f32_32x32x16_bf16 v[32:47], a[72:75], a[192:195], 0\n"
      "s_add_u32 m0, s41, 33664\n"
      "ds_read_b64_tr_b16 a[130:131], v206 offset:16896\n"
      "buffer_load_dwordx4 v218, %4, s45 offen offset:128 lds\n"
      "ds_read_b64_tr_b16 a[152:153], v206 offset:19456\n"
      "ds_read_b64_tr_b16 a[154:155], v206 offset:19968\n"
      "v_mfma_f32_32x32x16_bf16 v[48:63], a[76:79], a[192:195], 0\n"
      "ds_read_b64_tr_b16 a[156:157], v207 offset:19456\n"
      "ds_read_b64_tr_b16 a[158:159], v207 offset:19968\n"
      "v_mfma_f32_32x32x16_bf16 v[64:79], a[80:83], a[192:195], 0\n"
      "s_add_u32 m0, s41, 34560\n"
      "ds_read_b64_tr_b16 a[132:133], v207 offset:16384\n"
      "buffer_load_dwordx4 v218, %4, s45 offen offset:256 lds\n"
      "ds_read_b64_tr_b16 a[160:161], v206 offset:24576\n"
      "ds_read_b64_tr_b16 a[162:163], v206 offset:25088\n"
      "v_mfma_f32_32x32x16_bf16 v[80:95], a[84:87], a[192:195], 0\n"
      "ds_read_b64_tr_b16 a[164:165], v207 offset:24576\n"
      "ds_read_b64_tr_b16 a[166:167], v207 offset:25088\n"
      "v_mfma_f32_32x32x16_bf16 v[96:111], a[88:91], a[192:195], 0\n"
      "s_add_u32 m0, s41, 35456\n"
      "ds_read_b64_tr_b16 a[134:135], v207 offset:16896\n"
      "buffer_load_dwordx4 v218, %4, s45 offen offset:384 lds\n"
      "s_add_u32 s45, s45, 0x40000\n"
      "s_cmp_eq_u32 s45, 0x380000\n"
      "s_cselect_b32 s45, 0, s45\n"
      "ds_read_b64_tr_b16 a[168:169], v206 offset:25600\n"
      "ds_read_b64_tr_b16 a[170:171], v206 offset:26112\n"
      "v_mfma_f32_32x32x16_bf16 v[112:127], a[92:95], a[192:195], 0\n"
      "ds_read_b64_tr_b16 a[172:173], v207 offset:25600\n"
      "ds_read_b64_tr_b16 a[174:175], v207 offset:26112\n"
      "v_mfma_f32_32x32x16_bf16 v[0:15], a[96:99], a[196:199], v[0:15]\n"
      "s_add_u32 m0, s41, 49152\n"
      "ds_read_b64_tr_b16 a[136:137], v206 offset:17408\n"
      "buffer_load_dwordx4 v218, %4, s45 offen offset:0 lds\n"
      "ds_read_b64_tr_b16 a[176:177], v206 offset:26624\n"
      "ds_read_b64_tr_b16 a[178:179], v206 offset:27136\n"
      "v_mfma_f32_32x32x16_bf16 v[16:31], a[100:103], a[196:199], v[16:31]\n"
      "ds_read_b64_tr_b16 a[180:181], v207 offset:26624\n"
      "ds_read_b64_tr_b16 a[182:183], v207 offset:27136\n"
      "v_mfma_f32_32x32x16_bf16 v[32:47], a[104:107], a[196:199], v[32:47]\n"
      "s_add_u32 m0, s41, 50048\n"
      "ds_read_b64_tr_b16 a[138:139], v206 offset:17920\n"
      "buffer_load_dwordx4 v218, %4, s45 offen offset:128 lds\n"
      "ds_read_b64_tr_b16 a[184:185], v206 offset:27648\n"
      "ds_read_b64_tr_b16 a[186:187], v206 offset:28160\n"
      "v_mfma_f32_32x32x16_bf16 v[48:63], a[108:111], a[196:199], v[48:63]\n"
      "ds_read_b64_tr_b16 a[188:189], v207 offset:27648\n"
      "ds_read_b64_tr_b16 a[190:191], v207 offset:28160\n"
      "v_mfma_f32_32x32x16_bf16 v[64:79], a[112:115], a[196:199], v[64:79]\n"
      "s_add_u32 m0, s41, 50944\n"
      "ds_read_b64_tr_b16 a[140:141], v207 offset:17408\n"
      "buffer_load_dwordx4 v218, %4, s45 offen offset:256 lds\n"
      "v_mfma_f32_32x32x16_bf16 v[80:95], a[116:119], a[196:199], v[80:95]\n"
      "v_mfma_f32_32x32x16_bf16 v[96:111], a[120:123], a[196:199], v[96:111]\n"
      "s_add_u32 m0, s41, 51840\n"
      "ds_read_b64_tr_b16 a[142:143], v207 offset:17920\n"
      "buffer_load_dwordx4 v218, %4, s45 offen offset:384 lds\n"
      "s_add_u32 s45, s45, 0x40000\n"
      "s_cmp_eq_u32 s45, 0x380000\n"
      "s_cselect_b32 s45, 0, s45\n"
      "v_mfma_f32_32x32x16_bf16 v[112:127], a[124:127], a[196:199], v[112:127]\n"
      "s_waitcnt vmcnt(4) lgkmcnt(0)\n"
      "s_barrier\n"
      "v_mfma_f32_32x32x16_bf16 v[0:15], a[128:131], a[200:203], v[0:15]\n"
      "s_add_u32 m0, s41, 0\n"
      "ds_read_b128 a[208:211], v208 offset:16384\n"
      "buffer_load_dwordx4 v218, %4, s45 offen offset:0 lds\n"
      "ds_read_b64_tr_b16 a[64:65], v206 offset:32768\n"
      "ds_read_b64_tr_b16 a[66:67], v206 offset:33280\n"
      "v_mfma_f32_32x32x16_bf16 v[16:31], a[132:135], a[200:203], v[16:31]\n"
      "ds_read_b64_tr_b16 a[68:69], v207 offset:32768\n"
      "ds_read_b64_tr_b16 a[70:71], v207 offset:33280\n"
      "ds_read_b64_tr_b16 a[72:73], v206 offset:33792\n"
      "v_mfma_f32_32x32x16_bf16 v[32:47], a[136:139], a[200:203], v[32:47]\n"
      "s_add_u32 m0, s41, 896\n"
      "ds_read_b128 a[212:215], v209 offset:16384\n"
      "buffer_load_dwordx4 v218, %4, s45 offen offset:128 lds\n"
      "ds_read_b64_tr_b16 a[74:75], v206 offset:34304\n"
      "ds_read_b64_tr_b16 a[76:77], v207 offset:33792\n"
      "v_mfma_f32_32x32x16_bf16 v[48:63], a[140:143], a[200:203], v[48:63]\n"
      "ds_read_b64_tr_b16 a[78:79], v207 offset:34304\n"
      "ds_read_b64_tr_b16 a[80:81], v206 offset:34816\n"
      "ds_read_b64_tr_b16 a[82:83], v206 offset:35328\n"
      "v_mfma_f32_32x32x16_bf16 v[64:79], a[144:147], a[200:203], v[64:79]\n"
      "s_add_u32 m0, s41, 1792\n"
      "ds_read_b128 a[216:219], v210 offset:16384\n"
      "buffer_load_dwordx4 v218, %4, s45 offen offset:256 lds\n"
      "ds_read_b64_tr_b16 a[84:85], v207 offset:34816\n"
      "ds_read_b64_tr_b16 a[86:87], v207 offset:35328\n"
      "ds_read_b64_tr_b16 a[88:89], v206 offset:35840\n"
      "v_mfma_f32_32x32x16_bf16 v[80:95], a[148:151], a[200:203], v[80:95]\n"
      "ds_read_b64_tr_b16 a[90:91], v206 offset:36352\n"
      "ds_read_b64_tr_b16 a[92:93], v207 offset:35840\n"
      "v_mfma_f32_32x32x16_bf16 v[96:111], a[152:155], a[200:203], v[96:111]\n"
      "s_add_u32 m0, s41, 2688\n"
      "ds_read_b128 a[220:223], v211 offset:16384\n"
      "buffer_load_dwordx4 v218, %4, s45 offen offset:384 lds\n"
      "s_add_u32 s45, s45, 0x40000\n"
      "s_cmp_eq_u32 s45, 0x380000\n"
      "s_cselect_b32 s45, 0, s45\n"
      "ds_read_b64_tr_b16 a[94:95], v207 offset:36352\n"
      "ds_read_b64_tr_b16 a[96:97], v206 offset:40960\n"
      "ds_read_b64_tr_b16 a[98:99], v206 offset:41472\n"
      "v_mfma_f32_32x32x16_bf16 v[112:127], a[156:159], a[200:203], v[112:127]\n"
      "ds_read_b64_tr_b16 a[100:101], v207 offset:40960\n"
      "ds_read_b64_tr_b16 a[102:103], v207 offset:41472\n"
      "v_mfma_f32_32x32x16_bf16 v[0:15], a[160:163], a[204:207], v[0:15]\n"
      "s_add_u32 m0, s42, 0\n"
      "s_nop 0\n"
      "buffer_load_dwordx4 v219, %3, s46 offen lds\n"
      "ds_read_b64_tr_b16 a[104:105], v206 offset:41984\n"
      "ds_read_b64_tr_b16 a[106:107], v206 offset:42496\n"
      "ds_read_b64_tr_b16 a[108:109], v207 offset:41984\n"
      "v_mfma_f32_32x32x16_bf16 v[16:31], a[164:167], a[204:207], v[16:31]\n"
      "ds_read_b64_tr_b16 a[110:111], v207 offset:42496\n"
      "ds_read_b64_tr_b16 a[112:113], v206 offset:43008\n"
      "ds_read_b64_tr_b16 a[114:115], v206 offset:43520\n"
      "v_mfma_f32_32x32x16_bf16 v[32:47], a[168:171], a[204:207], v[32:47]\n"
      "s_add_u32 m0, s42, 1024\n"
      "s_nop 0\n"
      "buffer_load_dwordx4 v220, %3, s46 offen lds\n"
      "ds_read_b64_tr_b16 a[116:117], v207 offset:43008\n"
      "ds_read_b64_tr_b16 a[118:119], v207 offset:43520\n"
      "v_mfma_f32_32x32x16_bf16 v[48:63], a[172:175], a[204:207], v[48:63]\n"
      "ds_read_b64_tr_b16 a[120:121], v206 offset:44032\n"
      "ds_read_b64_tr_b16 a[122:123], v206 offset:44544\n"
      "ds_read_b64_tr_b16 a[124:125], v207 offset:44032\n"
      "v_mfma_f32_32x32x16_bf16 v[64:79], a[176:179], a[204:207], v[64:79]\n"
      "s_add_u32 m0, s42, 2048\n"
      "s_nop 0\n"
      "buffer_load_dwordx4 v221, %3, s46 offen lds\n"
      "ds_read_b64_tr_b16 a[126:127], v207 offset:44544\n"
      "v_mfma_f32_32x32x16_bf16 v[80:95], a[180:183], a[204:207], v[80:95]\n"
      "v_mfma_f32_32x32x16_bf16 v[96:111], a[184:187], a[204:207], v[96:111]\n"
      "s_add_u32 m0, s42, 3072\n"
      "s_nop 0\n"
      "buffer_load_dwordx4 v222, %3, s46 offen lds\n"
      "s_add_u32 s46, s46, 128\n"
      "s_cmp_eq_u32 s46, 896\n"
      "s_cselect_b32 s46, 0, s46\n"
      "v_mfma_f32_32x32x16_bf16 v[112:127], a[188:191], a[204:207], v[112:127]\n"
      "s_waitcnt vmcnt(8) lgkmcnt(0)\n"
      "s_barrier\n"
      "v_mfma_f32_32x32x16_bf16 v[0:15], a[64:67], a[208:211], v[0:15]\n"
      "s_add_u32 m0, s41, 16384\n"
      "ds_read_b64_tr_b16 a[128:129], v206 offset:49152\n"
      "buffer_load_dwordx4 v218, %4, s45 offen offset:0 lds\n"
      "ds_read_b64_tr_b16 a[152:153], v206 offset:52224\n"
      "ds_read_b64_tr_b16 a[154:155], v206 offset:52736\n"
      "v_mfma_f32_32x32x16_bf16 v[16:31], a[68:71], a[208:211], v[16:31]\n"
      "s_add_u32 m0, s41, 17280\n"
      "ds_read_b64_tr_b16 a[130:131], v206 offset:49664\n"
      "buffer_load_dwordx4 v218, %4, s45 offen offset:128 lds\n"
      "ds_read_b64_tr_b16 a[156:157], v207 offset:52224\n"
      "ds_read_b64_tr_b16 a[158:159], v207 offset:52736\n"
      "v_mfma_f32_32x32x16_bf16 v[32:47], a[72:75], a[208:211], v[32:47]\n"
      "s_add_u32 m0, s41, 18176\n"
      "ds_read_b64_tr_b16 a[132:133], v207 offset:49152\n"
      "buffer_load_dwordx4 v218, %4, s45 offen offset:256 lds\n"
      "ds_read_b64_tr_b16 a[160:161], v206 offset:57344\n"
      "v_mfma_f32_32x32x16_bf16 v[48:63], a[76:79], a[208:211], v[48:63]\n"
      "ds_read_b64_tr_b16 a[162:163], v206 offset:57856\n"
      "ds_read_b64_tr_b16 a[164:165], v207 offset:57344\n"
      "v_mfma_f32_32x32x16_bf16 v[64:79], a[80:83], a[208:211], v[64:79]\n"
      "s_add_u32 m0, s41, 19072\n"
      "ds_read_b64_tr_b16 a[134:135], v207 offset:49664\n"
      "buffer_load_dwordx4 v218, %4, s45 offen offset:384 lds\n"
      "s_add_u32 s45, s45, 0x40000\n"
      "s_cmp_eq_u32 s45, 0x380000\n"
      "s_cselect_b32 s45, 0, s45\n"
      "ds_read_b64_tr_b16 a[166:167], v207 offset:57856\n"
      "ds_read_b64_tr_b16 a[168:169], v206 offset:58368\n"
      "v_mfma_f32_32x32x16_bf16 v[80:95], a[84:87], a[208:211], v[80:95]\n"
      "s_add_u32 m0, s41, 32768\n"
      "ds_read_b64_tr_b16 a[136:137], v206 offset:50176\n"
      "buffer_load_dwordx4 v218, %4, s45 offen offset:0 lds\n"
      "ds_read_b64_tr_b16 a[170:171], v206 offset:58880\n"
      "v_mfma_f32_32x32x16_bf16 v[96:111], a[88:91], a[208:211], v[96:111]\n"
      "s_add_u32 m0, s41, 33664\n"
      "ds_read_b64_tr_b16 a[138:139], v206 offset:50688\n"
      "buffer_load_dwordx4 v218, %4, s45 offen offset:128 lds\n"
      "ds_read_b64_tr_b16 a[172:173], v207 offset:58368\n"
      "ds_read_b64_tr_b16 a[174:175], v207 offset:58880\n"
      "v_mfma_f32_32x32x16_bf16 v[112:127], a[92:95], a[208:211], v[112:127]\n"
      "ds_read_b64_tr_b16 a[176:177], v206 offset:59392\n"
      "v_mfma_f32_32x32x16_bf16 v[0:15], a[96:99], a[212:215], v[0:15]\n"
      "s_add_u32 m0, s41, 34560\n"
      "ds_read_b64_tr_b16 a[140:141], v207 offset:50176\n"
      "buffer_load_dwordx4 v218, %4, s45 offen offset:256 lds\n"
      "ds_read_b64_tr_b16 a[178:179], v206 offset:59904\n"
      "ds_read_b64_tr_b16 a[180:181], v207 offset:59392\n"
      "v_mfma_f32_32x32x16_bf16 v[16:31], a[100:103], a[212:215], v[16:31]\n"
      "s_add_u32 m0, s41, 35456\n"
      "ds_read_b64_tr_b16 a[142:143], v207 offset:50688\n"
      "buffer_load_dwordx4 v218, %4, s45 offen offset:384 lds\n"
      "s_add_u32 s45, s45, 0x40000\n"
      "s_cmp_eq_u32 s45, 0x380000\n"
      "s_cselect_b32 s45, 0, s45\n"
      "ds_read_b64_tr_b16 a[182:183], v207 offset:59904\n"
      "ds_read_b64_tr_b16 a[184:185], v206 offset:60416\n"
      "v_mfma_f32_32x32x16_bf16 v[32:47], a[104:107], a[212:215], v[32:47]\n"
      "s_add_u32 m0, s40, 0\n"
      "ds_read_b64_tr_b16 a[144:145], v206 offset:51200\n"
      "buffer_load_dwordx4 v216, %1, s43 offen offset:0 lds\n"
      "ds_read_b64_tr_b16 a[186:187], v206 offset:60928\n"
      "v_mfma_f32_32x32x16_bf16 v[48:63], a[108:111], a[212:215], v[48:63]\n"
      "ds_read_b64_tr_b16 a[188:189], v207 offset:60416\n"
      "ds_read_b64_tr_b16 a[190:191], v207 offset:60928\n"
      "v_mfma_f32_32x32x16_bf16 v[64:79], a[112:115], a[212:215], v[64:79]\n"
      "s_add_u32 m0, s40, 896\n"
      "ds_read_b64_tr_b16 a[146:147], v206 offset:51712\n"
      "buffer_load_dwordx4 v216, %1, s43 offen offset:128 lds\n"
      "v_mfma_f32_32x32x16_bf16 v[80:95], a[116:119], a[212:215], v[80:95]\n"
      "s_add_u32 m0, s40, 1792\n"
      "ds_read_b64_tr_b16 a[148:149], v207 offset:51200\n"
      "buffer_load_dwordx4 v216, %1, s43 offen offset:256 lds\n"
      "v_mfma_f32_32x32x16_bf16 v[96:111], a[120:123], a[212:215], v[96:111]\n"
      "s_add_u32 m0, s40, 2688\n"
      "ds_read_b64_tr_b16 a[150:151], v207 offset:51712\n"
      "buffer_load_dwordx4 v216, %1, s43 offen offset:384 lds\n"
      "s_add_u32 s43, s43, 0x4000\n"
      "v_mfma_f32_32x32x16_bf16 v[112:127], a[124:127], a[212:215], v[112:127]\n"
      "s_waitcnt vmcnt(12) lgkmcnt(0)\n"
      "s_barrier\n"
      "v_mfma_f32_32x32x16_bf16 v[0:15], a[128:131], a[216:219], v[0:15]\n"
      "s_add_u32 m0, s41, 49152\n"
      "ds_read_b128 a[192:195], v208 offset:0\n"
      "buffer_load_dwordx4 v218, %4, s45 offen offset:0 lds\n"
      "ds_read_b64_tr_b16 a[64:65], v206 offset:0\n"
      "ds_read_b64_tr_b16 a[66:67], v206 offset:512\n"
      "v_mfma_f32_32x32x16_bf16 v[16:31], a[132:135], a[216:219], v[16:31]\n"
      "ds_read_b64_tr_b16 a[68:69], v207 offset:0\n"
      "ds_read_b64_tr_b16 a[70:71], v207 offset:512\n"
      "ds_read_b64_tr_b16 a[72:73], v206 offset:1024\n"
      "v_mfma_f32_32x32x16_bf16 v[32:47], a[136:139], a[216:219], v[32:47]\n"
      "s_add_u32 m0, s41, 50048\n"
      "ds_read_b128 a[196:199], v209 offset:0\n"
      "buffer_load_dwordx4 v218, %4, s45 offen offset:128 lds\n"
      "ds_read_b64_tr_b16 a[74:75], v206 offset:1536\n"
      "ds_read_b64_tr_b16 a[76:77], v207 offset:1024\n"
      "v_mfma_f32_32x32x16_bf16 v[48:63], a[140:143], a[216:219], v[48:63]\n"
      "ds_read_b64_tr_b16 a[78:79], v207 offset:1536\n"
      "ds_read_b64_tr_b16 a[80:81], v206 offset:2048\n"
      "ds_read_b64_tr_b16 a[82:83], v206 offset:2560\n"
      "v_mfma_f32_32x32x16_bf16 v[64:79], a[144:147], a[216:219], v[64:79]\n"
      "s_add_u32 m0, s41, 50944\n"
      "ds_read_b128 a[200:203], v210 offset:0\n"
      "buffer_load_dwordx4 v218, %4, s45 offen offset:256 lds\n"
      "ds_read_b64_tr_b16 a[84:85], v207 offset:2048\n"
      "ds_read_b64_tr_b16 a[86:87], v207 offset:2560\n"
      "ds_read_b64_tr_b16 a[88:89], v206 offset:3072\n"
      "v_mfma_f32_32x32x16_bf16 v[80:95], a[148:151], a[216:219], v[80:95]\n"
      "ds_read_b64_tr_b16 a[90:91], v206 offset:3584\n"
      "ds_read_b64_tr_b16 a[92:93], v207 offset:3072\n"
      "v_mfma_f32_32x32x16_bf16 v[96:111], a[152:155], a[216:219], v[96:111]\n"
      "s_add_u32 m0, s41, 51840\n"
      "ds_read_b128 a[204:207], v211 offset:0\n"
      "buffer_load_dwordx4 v218, %4, s45 offen offset:384 lds\n"
      "s_add_u32 s45, s45, 0x40000\n"
      "s_cmp_eq_u32 s45, 0x380000\n"
      "s_cselect_b32 s45, 0, s45\n"
      "ds_read_b64_tr_b16 a[94:95], v207 offset:3584\n"
      "ds_read_b64_tr_b16 a[96:97], v206 offset:8192\n"
      "ds_read_b64_tr_b16 a[98:99], v206 offset:8704\n"
      "v_mfma_f32_32x32x16_bf16 v[112:127], a[156:159], a[216:219], v[112:127]\n"
      "ds_read_b64_tr_b16 a[100:101], v207 offset:8192\n"
      "ds_read_b64_tr_b16 a[102:103], v207 offset:8704\n"
      "v_mfma_f32_32x32x16_bf16 v[0:15], a[160:163], a[220:223], v[0:15]\n"
      "s_add_u32 m0, s42, 16384\n"
      "s_nop 0\n"
      "buffer_load_dwordx4 v219, %3, s46 offen lds\n"
      "ds_read_b64_tr_b16 a[104:105], v206 offset:9216\n"
      "ds_read_b64_tr_b16 a[106:107], v206 offset:9728\n"
      "ds_read_b64_tr_b16 a[108:109], v207 offset:9216\n"
      "v_mfma_f32_32x32x16_bf16 v[16:31], a[164:167], a[220:223], v[16:31]\n"
      "ds_read_b64_tr_b16 a[110:111], v207 offset:9728\n"
      "ds_read_b64_tr_b16 a[112:113], v206 offset:10240\n"
      "ds_read_b64_tr_b16 a[114:115], v206 offset:10752\n"
      "v_mfma_f32_32x32x16_bf16 v[32:47], a[168:171], a[220:223], v[32:47]\n"
      "s_add_u32 m0, s42, 17408\n"
      "s_nop 0\n"
      "buffer_load_dwordx4 v220, %3, s46 offen lds\n"
      "ds_read_b64_tr_b16 a[116:117], v207 offset:10240\n"
      "ds_read_b64_tr_b16 a[118:119], v207 offset:10752\n"
      "v_mfma_f32_32x32x16_bf16 v[48:63], a[172:175], a[220:223], v[48:63]\n"
      "ds_read_b64_tr_b16 a[120:121], v206 offset:11264\n"
      "ds_read_b64_tr_b16 a[122:123], v206 offset:11776\n"
      "ds_read_b64_tr_b16 a[124:125], v207 offset:11264\n"
      "v_mfma_f32_32x32x16_bf16 v[64:79], a[176:179], a[220:223], v[64:79]\n"
      "s_add_u32 m0, s42, 18432\n"
      "s_nop 0\n"
      "buffer_load_dwordx4 v221, %3, s46 offen lds\n"
      "ds_read_b64_tr_b16 a[126:127], v207 offset:11776\n"
      "v_mfma_f32_32x32x16_bf16 v[80:95], a[180:183], a[220:223], v[80:95]\n"
      "v_mfma_f32_32x32x16_bf16 v[96:111], a[184:187], a[220:223], v[96:111]\n"
      "s_add_u32 m0, s42, 19456\n"
      "s_nop 0\n"
      "buffer_load_dwordx4 v222, %3, s46 offen lds\n"
      "s_add_u32 s46, s46, 128\n"
      "s_cmp_eq_u32 s46, 896\n"
      "s_cselect_b32 s46, 0, s46\n"
      "global_load_dword %0, v233, %12 sc1\n"
      "v_mfma_f32_32x32x16_bf16 v[112:127], a[188:191], a[220:223], v[112:127]\n"
      "s_waitcnt vmcnt(17) lgkmcnt(0)\n"
      "s_barrier\n"
      "v_mfma_f32_32x32x16_bf16 v[0:15], a[64:67], a[192:195], v[0:15]\n"
      "s_add_u32 m0, s41, 0\n"
      "ds_read_b64_tr_b16 a[128:129], v206 offset:16384\n"
      "buffer_load_dwordx4 v218, %4, s45 offen offset:0 lds\n"
      "ds_read_b64_tr_b16 a[144:145], v206 offset:18432\n"
      "ds_read_b64_tr_b16 a[146:147], v206 offset:18944\n"
      "v_mfma_f32_32x32x16_bf16 v[16:31], a[68:71], a[192:195], v[16:31]\n"
      "ds_read_b64_tr_b16 a[148:149], v207 offset:18432\n"
      "ds_read_b64_tr_b16 a[150:151], v207 offset:18944\n"
      "v_mfma_f32_32x32x16_bf16 v[32:47], a[72:75], a[192:195], v[32:47]\n"
      "s_add_u32 m0, s41, 896\n"
      "ds_read_b64_tr_b16 a[130:131], v206 offset:16896\n"
      "buffer_load_dwordx4 v218, %4, s45 offen offset:128 lds\n"
      "ds_read_b64_tr_b16 a[152:153], v206 offset:19456\n"
      "ds_read_b64_tr_b16 a[154:155], v206 offset:19968\n"
      "v_mfma_f32_32x32x16_bf16 v[48:63], a[76:79], a[192:195], v[48:63]\n"
      "ds_read_b64_tr_b16 a[156:157], v207 offset:19456\n"
      "ds_read_b64_tr_b16 a[158:159], v207 offset:19968\n"
      "v_mfma_f32_32x32x16_bf16 v[64:79], a[80:83], a[192:195], v[64:79]\n"
      "s_add_u32 m0, s41, 1792\n"
      "ds_read_b64_tr_b16 a[132:133], v207 offset:16384\n"
      "buffer_load_dwordx4 v218, %4, s45 offen offset:256 lds\n"
      "ds_read_b64_tr_b16 a[160:161], v206 offset:24576\n"
      "ds_read_b64_tr_b16 a[162:163], v206 offset:25088\n"
      "v_mfma_f32_32x32x16_bf16 v[80:95], a[84:87], a[192:195], v[80:95]\n"
      "ds_read_b64_tr_b16 a[164:165], v207 offset:24576\n"
      "ds_read_b64_tr_b16 a[166:167], v207 offset:25088\n"
      "v_mfma_f32_32x32x16_bf16 v[96:111], a[88:91], a[192:195], v[96:111]\n"
      "s_add_u32 m0, s41, 2688\n"
      "ds_read_b64_tr_b16 a[134:135], v207 offset:16896\n"
      "buffer_load_dwordx4 v218, %4, s45 offen offset:384 lds\n"
      "s_add_u32 s45, s45, 0x40000\n"
      "s_cmp_eq_u32 s45, 0x380000\n"
      "s_cselect_b32 s45, 0, s45\n"
      "ds_read_b64_tr_b16 a[168:169], v206 offset:25600\n"
      "ds_read_b64_tr_b16 a[170:171], v206 offset:26112\n"
      "v_mfma_f32_32x32x16_bf16 v[112:127], a[92:95], a[192:195], v[112:127]\n"
      "ds_read_b64_tr_b16 a[172:173], v207 offset:25600\n"
      "ds_read_b64_tr_b16 a[174:175], v207 offset:26112\n"
      "v_mfma_f32_32x32x16_bf16 v[0:15], a[96:99], a[196:199], v[0:15]\n"
      "s_add_u32 m0, s40, 16384\n"
      "ds_read_b64_tr_b16 a[136:137], v206 offset:17408\n"
      "buffer_load_dwordx4 v216, %1, s43 offen offset:0 lds\n"
      "ds_read_b64_tr_b16 a[176:177], v206 offset:26624\n"
      "ds_read_b64_tr_b16 a[178:179], v206 offset:27136\n"
      "v_mfma_f32_32x32x16_bf16 v[16:31], a[100:103], a[196:199], v[16:31]\n"
      "ds_read_b64_tr_b16 a[180:181], v207 offset:26624\n"
      "ds_read_b64_tr_b16 a[182:183], v207 offset:27136\n"
      "v_mfma_f32_32x32x16_bf16 v[32:47], a[104:107], a[196:199], v[32:47]\n"
      "s_add_u32 m0, s40, 17280\n"
      "ds_read_b64_tr_b16 a[138:139], v206 offset:17920\n"
      "buffer_load_dwordx4 v216, %1, s43 offen offset:128 lds\n"
      "ds_read_b64_tr_b16 a[184:185], v206 offset:27648\n"
      "ds_read_b64_tr_b16 a[186:187], v206 offset:28160\n"
      "v_mfma_f32_32x32x16_bf16 v[48:63], a[108:111], a[196:199], v[48:63]\n"
      "ds_read_b64_tr_b16 a[188:189], v207 offset:27648\n"
      "ds_read_b64_tr_b16 a[190:191], v207 offset:28160\n"
      "v_mfma_f32_32x32x16_bf16 v[64:79], a[112:115], a[196:199], v[64:79]\n"
      "s_add_u32 m0, s40, 18176\n"
      "ds_read_b64_tr_b16 a[140:141], v207 offset:17408\n"
      "buffer_load_dwordx4 v216, %1, s43 offen offset:256 lds\n"
      "v_mfma_f32_32x32x16_bf16 v[80:95], a[116:119], a[196:199], v[80:95]\n"
      "v_mfma_f32_32x32x16_bf16 v[96:111], a[120:123], a[196:199], v[96:111]\n"
      "s_add_u32 m0, s40, 19072\n"
      "ds_read_b64_tr_b16 a[142:143], v207 offset:17920\n"
      "buffer_load_dwordx4 v216, %1, s43 offen offset:384 lds\n"
      "s_add_u32 s43, s43, 0x4000\n"
      "v_mfma_f32_32x32x16_bf16 v[112:127], a[124:127], a[196:199], v[112:127]\n"
      "s_waitcnt vmcnt(9) lgkmcnt(0)\n"
      "s_barrier\n"
      "v_mfma_f32_32x32x16_bf16 v[0:15], a[128:131], a[200:203], v[0:15]\n"
      "s_add_u32 m0, s41, 16384\n"
      "ds_read_b128 a[208:211], v208 offset:16384\n"
      "buffer_load_dwordx4 v218, %4, s45 offen offset:0 lds\n"
      "ds_read_b64_tr_b16 a[64:65], v206 offset:32768\n"
      "ds_read_b64_tr_b16 a[66:67], v206 offset:33280\n"
      "v_mfma_f32_32x32x16_bf16 v[16:31], a[132:135], a[200:203], v[16:31]\n"
      "ds_read_b64_tr_b16 a[68:69], v207 offset:32768\n"
      "ds_read_b64_tr_b16 a[70:71], v207 offset:33280\n"
      "ds_read_b64_tr_b16 a[72:73], v206 offset:33792\n"
      "v_mfma_f32_32x32x16_bf16 v[32:47], a[136:139], a[200:203], v[32:47]\n"
      "s_add_u32 m0, s41, 17280\n"
      "ds_read_b128 a[212:215], v209 offset:16384\n"
      "buffer_load_dwordx4 v218, %4, s45 offen offset:128 lds\n"
      "ds_read_b64_tr_b16 a[74:75], v206 offset:34304\n"
      "ds_read_b64_tr_b16 a[76:77], v207 offset:33792\n"
      "v_mfma_f32_32x32x16_bf16 v[48:63], a[140:143], a[200:203], v[48:63]\n"
      "ds_read_b64_tr_b16 a[78:79], v207 offset:34304\n"
      "ds_read_b64_tr_b16 a[80:81], v206 offset:34816\n"
      "ds_read_b64_tr_b16 a[82:83], v206 offset:35328\n"
      "v_mfma_f32_32x32x16_bf16 v[64:79], a[144:147], a[200:203], v[64:79]\n"
      "s_add_u32 m0, s41, 18176\n"
      "ds_read_b128 a[216:219], v210 offset:16384\n"
      "buffer_load_dwordx4 v218, %4, s45 offen offset:256 lds\n"
      "ds_read_b64_tr_b16 a[84:85], v207 offset:34816\n"
      "ds_read_b64_tr_b16 a[86:87], v207 offset:35328\n"
      "ds_read_b64_tr_b16 a[88:89], v206 offset:35840\n"
      "v_mfma_f32_32x32x16_bf16 v[80:95], a[148:151], a[200:203], v[80:95]\n"
      "ds_read_b64_tr_b16 a[90:91], v206 offset:36352\n"
      "ds_read_b64_tr_b16 a[92:93], v207 offset:35840\n"
      "v_mfma_f32_32x32x16_bf16 v[96:111], a[152:155], a[200:203], v[96:111]\n"
      "s_add_u32 m0, s41, 19072\n"
      "ds_read_b128 a[220:223], v211 offset:16384\n"
      "buffer_load_dwordx4 v218, %4, s45 offen offset:384 lds\n"
      "s_add_u32 s45, s45, 0x40000\n"
      "s_cmp_eq_u32 s45, 0x380000\n"
      "s_cselect_b32 s45, 0, s45\n"
      "ds_read_b64_tr_b16 a[94:95], v207 offset:36352\n"
      "ds_read_b64_tr_b16 a[96:97], v206 offset:40960\n"
      "ds_read_b64_tr_b16 a[98:99], v206 offset:41472\n"
      "v_mfma_f32_32x32x16_bf16 v[112:127], a[156:159], a[200:203], v[112:127]\n"
      "ds_read_b64_tr_b16 a[100:101], v207 offset:40960\n"
      "ds_read_b64_tr_b16 a[102:103], v207 offset:41472\n"
      "v_mfma_f32_32x32x16_bf16 v[0:15], a[160:163], a[204:207], v[0:15]\n"
      "s_add_u32 m0, s42, 0\n"
      "s_nop 0\n"
      "buffer_load_dwordx4 v219, %3, s46 offen lds\n"
      "ds_read_b64_tr_b16 a[104:105], v206 offset:41984\n"
      "ds_read_b64_tr_b16 a[106:107], v206 offset:42496\n"
      "ds_read_b64_tr_b16 a[108:109], v207 offset:41984\n"
      "v_mfma_f32_32x32x16_bf16 v[16:31], a[164:167], a[204:207], v[16:31]\n"
      "ds_read_b64_tr_b16 a[110:111], v207 offset:42496\n"
      "ds_read_b64_tr_b16 a[112:113], v206 offset:43008\n"
      "ds_read_b64_tr_b16 a[114:115], v206 offset:43520\n"
      "v_mfma_f32_32x32x16_bf16 v[32:47], a[168:171], a[204:207], v[32:47]\n"
      "s_add_u32 m0, s42, 1024\n"
      "s_nop 0\n"
      "buffer_load_dwordx4 v220, %3, s46 offen lds\n"
      "ds_read_b64_tr_b16 a[116:117], v207 offset:43008\n"
      "ds_read_b64_tr_b16 a[118:119], v207 offset:43520\n"
      "v_mfma_f32_32x32x16_bf16 v[48:63], a[172:175], a[204:207], v[48:63]\n"
      "ds_read_b64_tr_b16 a[120:121], v206 offset:44032\n"
      "ds_read_b64_tr_b16 a[122:123], v206 offset:44544\n"
      "ds_read_b64_tr_b16 a[124:125], v207 offset:44032\n"
      "v_mfma_f32_32x32x16_bf16 v[64:79], a[176:179], a[204:207], v[64:79]\n"
      "s_add_u32 m0, s42, 2048\n"
      "s_nop 0\n"
      "buffer_load_dwordx4 v221, %3, s46 offen lds\n"
      "ds_read_b64_tr_b16 a[126:127], v207 offset:44544\n"
      "v_mfma_f32_32x32x16_bf16 v[80:95], a[180:183], a[204:207], v[80:95]\n"
      "v_mfma_f32_32x32x16_bf16 v[96:111], a[184:187], a[204:207], v[96:111]\n"
      "s_add_u32 m0, s42, 3072\n"
      "s_nop 0\n"
      "buffer_load_dwordx4 v222, %3, s46 offen lds\n"
      "s_add_u32 s46, s46, 128\n"
      "s_cmp_eq_u32 s46, 896\n"
      "s_cselect_b32 s46, 0, s46\n"
      "v_mfma_f32_32x32x16_bf16 v[112:127], a[188:191], a[204:207], v[112:127]\n"
      "s_waitcnt vmcnt(17) lgkmcnt(0)\n"
      "s_barrier\n"
      "v_mfma_f32_32x32x16_bf16 v[0:15], a[64:67], a[208:211], v[0:15]\n"
      "s_add_u32 m0, s41, 32768\n"
      "ds_read_b64_tr_b16 a[128:129], v206 offset:49152\n"
      "buffer_load_dwordx4 v218, %4, s45 offen offset:0 lds\n"
      "ds_read_b64_tr_b16 a[144:145], v206 offset:51200\n"
      "ds_read_b64_tr_b16 a[146:147], v206 offset:51712\n"
      "v_mfma_f32_32x32x16_bf16 v[16:31], a[68:71], a[208:211], v[16:31]\n"
      "ds_read_b64_tr_b16 a[148:149], v207 offset:51200\n"
      "ds_read_b64_tr_b16 a[150:151], v207 offset:51712\n"
      "v_mfma_f32_32x32x16_bf16 v[32:47], a[72:75], a[208:211], v[32:47]\n"
      "s_add_u32 m0, s41, 33664\n"
      "ds_read_b64_tr_b16 a[130:131], v206 offset:49664\n"
      "buffer_load_dwordx4 v218, %4, s45 offen offset:128 lds\n"
      "ds_read_b64_tr_b16 a[152:153], v206 offset:52224\n"
      "ds_read_b64_tr_b16 a[154:155], v206 offset:52736\n"
      "v_mfma_f32_32x32x16_bf16 v[48:63], a[76:79], a[208:211], v[48:63]\n"
      "ds_read_b64_tr_b16 a[156:157], v207 offset:52224\n"
      "ds_read_b64_tr_b16 a[158:159], v207 offset:52736\n"
      "v_mfma_f32_32x32x16_bf16 v[64:79], a[80:83], a[208:211], v[64:79]\n"
      "s_add_u32 m0, s41, 34560\n"
      "ds_read_b64_tr_b16 a[132:133], v207 offset:49152\n"
      "buffer_load_dwordx4 v218, %4, s45 offen offset:256 lds\n"
      "ds_read_b64_tr_b16 a[160:161], v206 offset:57344\n"
      "ds_read_b64_tr_b16 a[162:163], v206 offset:57856\n"
      "v_mfma_f32_32x32x16_bf16 v[80:95], a[84:87], a[208:211], v[80:95]\n"
      "ds_read_b64_tr_b16 a[164:165], v207 offset:57344\n"
      "ds_read_b64_tr_b16 a[166:167], v207 offset:57856\n"
      "v_mfma_f32_32x32x16_bf16 v[96:111], a[88:91], a[208:211], v[96:111]\n"
      "s_add_u32 m0, s41, 35456\n"
      "ds_read_b64_tr_b16 a[134:135], v207 offset:49664\n"
      "buffer_load_dwordx4 v218, %4, s45 offen offset:384 lds\n"
      "s_add_u32 s45, s45, 0x40000\n"
      "s_cmp_eq_u32 s45, 0x380000\n"
      "s_cselect_b32 s45, 0, s45\n"
      "ds_read_b64_tr_b16 a[168:169], v206 offset:58368\n"
      "ds_read_b64_tr_b16 a[170:171], v206 offset:58880\n"
      "v_mfma_f32_32x32x16_bf16 v[112:127], a[92:95], a[208:211], v[112:127]\n"
      "ds_read_b64_tr_b16 a[172:173], v207 offset:58368\n"
      "ds_read_b64_tr_b16 a[174:175], v207 offset:58880\n"
      "v_mfma_f32_32x32x16_bf16 v[0:15], a[96:99], a[212:215], v[0:15]\n"
      "s_add_u32 m0, s40, 32768\n"
      "ds_read_b64_tr_b16 a[136:137], v206 offset:50176\n"
      "buffer_load_dwordx4 v216, %1, s43 offen offset:0 lds\n"
      "ds_read_b64_tr_b16 a[176:177], v206 offset:59392\n"
      "ds_read_b64_tr_b16 a[178:179], v206 offset:59904\n"
      "v_mfma_f32_32x32x16_bf16 v[16:31], a[100:103], a[212:215], v[16:31]\n"
      "ds_read_b64_tr_b16 a[180:181], v207 offset:59392\n"
      "ds_read_b64_tr_b16 a[182:183], v207 offset:59904\n"
      "v_mfma_f32_32x32x16_bf16 v[32:47], a[104:107], a[212:215], v[32:47]\n"
      "s_add_u32 m0, s40, 33664\n"
      "ds_read_b64_tr_b16 a[138:139], v206 offset:50688\n"
      "buffer_load_dwordx4 v216, %1, s43 offen offset:128 lds\n"
      "ds_read_b64_tr_b16 a[184:185], v206 offset:60416\n"
      "ds_read_b64_tr_b16 a[186:187], v206 offset:60928\n"
      "v_mfma_f32_32x32x16_bf16 v[48:63], a[108:111], a[212:215], v[48:63]\n"
      "ds_read_b64_tr_b16 a[188:189], v207 offset:60416\n"
      "ds_read_b64_tr_b16 a[190:191], v207 offset:60928\n"
      "v_mfma_f32_32x32x16_bf16 v[64:79], a[112:115], a[212:215], v[64:79]\n"
      "s_add_u32 m0, s40, 34560\n"
      "ds_read_b64_tr_b16 a[140:141], v207 offset:50176\n"
      "buffer_load_dwordx4 v216, %1, s43 offen offset:256 lds\n"
      "v_mfma_f32_32x32x16_bf16 v[80:95], a[116:119], a[212:215], v[80:95]\n"
      "v_mfma_f32_32x32x16_bf16 v[96:111], a[120:123], a[212:215], v[96:111]\n"
      "s_add_u32 m0, s40, 35456\n"
      "ds_read_b64_tr_b16 a[142:143], v207 offset:50688\n"
      "buffer_load_dwordx4 v216, %1, s43 offen offset:384 lds\n"
      "s_add_u32 s43, s43, 0x4000\n"
      "v_mfma_f32_32x32x16_bf16 v[112:127], a[124:127], a[212:215], v[112:127]\n"
      "s_waitcnt vmcnt(8) lgkmcnt(0)\n"
      "s_barrier\n"
      "v_mfma_f32_32x32x16_bf16 v[0:15], a[128:131], a[216:219], v[0:15]\n"
      "s_add_u32 m0, s41, 49152\n"
      "ds_read_b128 a[192:195], v208 offset:0\n"
      "buffer_load_dwordx4 v218, %4, s45 offen offset:0 lds\n"
      "ds_read_b64_tr_b16 a[64:65], v206 offset:0\n"
      "ds_read_b64_tr_b16 a[66:67], v206 offset:512\n"
      "v_mfma_f32_32x32x16_bf16 v[16:31], a[132:135], a[216:219], v[16:31]\n"
      "ds_read_b64_tr_b16 a[68:69], v207 offset:0\n"
      "ds_read_b64_tr_b16 a[70:71], v207 offset:512\n"
      "ds_read_b64_tr_b16 a[72:73], v206 offset:1024\n"
      "v_mfma_f32_32x32x16_bf16 v[32:47], a[136:139], a[216:219], v[32:47]\n"
      "s_add_u32 m0, s41, 50048\n"
      "ds_read_b128 a[196:199], v209 offset:0\n"
      "buffer_load_dwordx4 v218, %4, s45 offen offset:128 lds\n"
      "ds_read_b64_tr_b16 a[74:75], v206 offset:1536\n"
      "ds_read_b64_tr_b16 a[76:77], v207 offset:1024\n"
      "v_mfma_f32_32x32x16_bf16 v[48:63], a[140:143], a[216:219], v[48:63]\n"
      "ds_read_b64_tr_b16 a[78:79], v207 offset:1536\n"
      "ds_read_b64_tr_b16 a[80:81], v206 offset:2048\n"
      "ds_read_b64_tr_b16 a[82:83], v206 offset:2560\n"
      "v_mfma_f32_32x32x16_bf16 v[64:79], a[144:147], a[216:219], v[64:79]\n"
      "s_add_u32 m0, s41, 50944\n"
      "ds_read_b128 a[200:203], v210 offset:0\n"
      "buffer_load_dwordx4 v218, %4, s45 offen offset:256 lds\n"
      "ds_read_b64_tr_b16 a[84:85], v207 offset:2048\n"
      "ds_read_b64_tr_b16 a[86:87], v207 offset:2560\n"
      "ds_read_b64_tr_b16 a[88:89], v206 offset:3072\n"
      "v_mfma_f32_32x32x16_bf16 v[80:95], a[148:151], a[216:219], v[80:95]\n"
      "ds_read_b64_tr_b16 a[90:91], v206 offset:3584\n"
      "ds_read_b64_tr_b16 a[92:93], v207 offset:3072\n"
      "v_mfma_f32_32x32x16_bf16 v[96:111], a[152:155], a[216:219], v[96:111]\n"
      "s_add_u32 m0, s41, 51840\n"
      "ds_read_b128 a[204:207], v211 offset:0\n"
      "buffer_load_dwordx4 v218, %4, s45 offen offset:384 lds\n"
      "s_add_u32 s45, s45, 0x40000\n"
      "s_cmp_eq_u32 s45, 0x380000\n"
      "s_cselect_b32 s45, 0, s45\n"
      "ds_read_b64_tr_b16 a[94:95], v207 offset:3584\n"
      "ds_read_b64_tr_b16 a[96:97], v206 offset:8192\n"
      "ds_read_b64_tr_b16 a[98:99], v206 offset:8704\n"
      "v_mfma_f32_32x32x16_bf16 v[112:127], a[156:159], a[216:219], v[112:127]\n"
      "ds_read_b64_tr_b16 a[100:101], v207 offset:8192\n"
      "ds_read_b64_tr_b16 a[102:103], v207 offset:8704\n"
      "v_mfma_f32_32x32x16_bf16 v[0:15], a[160:163], a[220:223], v[0:15]\n"
      "s_add_u32 m0, s42, 16384\n"
      "s_nop 0\n"
      "buffer_load_dwordx4 v219, %3, s46 offen lds\n"
      "ds_read_b64_tr_b16 a[104:105], v206 offset:9216\n"
      "ds_read_b64_tr_b16 a[106:107], v206 offset:9728\n"
      "ds_read_b64_tr_b16 a[108:109], v207 offset:9216\n"
      "v_mfma_f32_32x32x16_bf16 v[16:31], a[164:167], a[220:223], v[16:31]\n"
      "ds_read_b64_tr_b16 a[110:111], v207 offset:9728\n"
      "ds_read_b64_tr_b16 a[112:113], v206 offset:10240\n"
      "ds_read_b64_tr_b16 a[114:115], v206 offset:10752\n"
      "v_mfma_f32_32x32x16_bf16 v[32:47], a[168:171], a[220:223], v[32:47]\n"
      "s_add_u32 m0, s42, 17408\n"
      "s_nop 0\n"
      "buffer_load_dwordx4 v220, %3, s46 offen lds\n"
      "ds_read_b64_tr_b16 a[116:117], v207 offset:10240\n"
      "ds_read_b64_tr_b16 a[118:119], v207 offset:10752\n"
      "v_mfma_f32_32x32x16_bf16 v[48:63], a[172:175], a[220:223], v[48:63]\n"
      "ds_read_b64_tr_b16 a[120:121], v206 offset:11264\n"
      "ds_read_b64_tr_b16 a[122:123], v206 offset:11776\n"
      "ds_read_b64_tr_b16 a[124:125], v207 offset:11264\n"
      "v_mfma_f32_32x32x16_bf16 v[64:79], a[176:179], a[220:223], v[64:79]\n"
      "s_add_u32 m0, s42, 18432\n"
      "s_nop 0\n"
      "buffer_load_dwordx4 v221, %3, s46 offen lds\n"
      "ds_read_b64_tr_b16 a[126:127], v207 offset:11776\n"
      "v_mfma_f32_32x32x16_bf16 v[80:95], a[180:183], a[220:223], v[80:95]\n"
      "v_mfma_f32_32x32x16_bf16 v[96:111], a[184:187], a[220:223], v[96:111]\n"
      "s_add_u32 m0, s42, 19456\n"
      "s_nop 0\n"
      "buffer_load_dwordx4 v222, %3, s46 offen lds\n"
      "s_add_u32 s46, s46, 128\n"
      "s_cmp_eq_u32 s46, 896\n"
      "s_cselect_b32 s46, 0, s46\n"
      "v_mfma_f32_32x32x16_bf16 v[112:127], a[188:191], a[220:223], v[112:127]\n"
      "s_waitcnt vmcnt(16) lgkmcnt(0)\n"
      "s_barrier\n"
      "v_mfma_f32_32x32x16_bf16 v[0:15], a[64:67], a[192:195], v[0:15]\n"
      "s_add_u32 m0, s41, 0\n"
      "ds_read_b64_tr_b16 a[128:129], v206 offset:16384\n"
      "buffer_load_dwordx4 v218, %4, s45 offen offset:0 lds\n"
      "ds_read_b64_tr_b16 a[144:145], v206 offset:18432\n"
      "ds_read_b64_tr_b16 a[146:147], v206 offset:18944\n"
      "v_mfma_f32_32x32x16_bf16 v[16:31], a[68:71], a[192:195], v[16:31]\n"
      "ds_read_b64_tr_b16 a[148:149], v207 offset:18432\n"
      "ds_read_b64_tr_b16 a[150:151], v207 offset:18944\n"
      "v_mfma_f32_32x32x16_bf16 v[32:47], a[72:75], a[192:195], v[32:47]\n"
      "s_add_u32 m0, s41, 896\n"
      "ds_read_b64_tr_b16 a[130:131], v206 offset:16896\n"
      "buffer_load_dwordx4 v218, %4, s45 offen offset:128 lds\n"
      "ds_read_b64_tr_b16 a[152:153], v206 offset:19456\n"
      "ds_read_b64_tr_b16 a[154:155], v206 offset:19968\n"
      "v_mfma_f32_32x32x16_bf16 v[48:63], a[76:79], a[192:195], v[48:63]\n"
      "ds_read_b64_tr_b16 a[156:157], v207 offset:19456\n"
      "ds_read_b64_tr_b16 a[158:159], v207 offset:19968\n"
      "v_mfma_f32_32x32x16_bf16 v[64:79], a[80:83], a[192:195], v[64:79]\n"
      "s_add_u32 m0, s41, 1792\n"
      "ds_read_b64_tr_b16 a[132:133], v207 offset:16384\n"
      "buffer_load_dwordx4 v218, %4, s45 offen offset:256 lds\n"
      "ds_read_b64_tr_b16 a[160:161], v206 offset:24576\n"
      "ds_read_b64_tr_b16 a[162:163], v206 offset:25088\n"
      "v_mfma_f32_32x32x16_bf16 v[80:95], a[84:87], a[192:195], v[80:95]\n"
      "ds_read_b64_tr_b16 a[164:165], v207 offset:24576\n"
      "ds_read_b64_tr_b16 a[166:167], v207 offset:25088\n"
      "v_mfma_f32_32x32x16_bf16 v[96:111], a[88:91], a[192:195], v[96:111]\n"
      "s_add_u32 m0, s41, 2688\n"
      "ds_read_b64_tr_b16 a[134:135], v207 offset:16896\n"
      "buffer_load_dwordx4 v218, %4, s45 offen offset:384 lds\n"
      "s_add_u32 s45, s45, 0x40000\n"
      "s_cmp_eq_u32 s45, 0x380000\n"
      "s_cselect_b32 s45, 0, s45\n"
      "ds_read_b64_tr_b16 a[168:169], v206 offset:25600\n"
      "ds_read_b64_tr_b16 a[170:171], v206 offset:26112\n"
      "v_mfma_f32_32x32x16_bf16 v[112:127], a[92:95], a[192:195], v[112:127]\n"
      "ds_read_b64_tr_b16 a[172:173], v207 offset:25600\n"
      "ds_read_b64_tr_b16 a[174:175], v207 offset:26112\n"
      "v_mfma_f32_32x32x16_bf16 v[0:15], a[96:99], a[196:199], v[0:15]\n"
      "s_add_u32 m0, s40, 49152\n"
      "ds_read_b64_tr_b16 a[136:137], v206 offset:17408\n"
      "buffer_load_dwordx4 v216, %1, s43 offen offset:0 lds\n"
      "ds_read_b64_tr_b16 a[176:177], v206 offset:26624\n"
      "ds_read_b64_tr_b16 a[178:179], v206 offset:27136\n"
      "v_mfma_f32_32x32x16_bf16 v[16:31], a[100:103], a[196:199], v[16:31]\n"
      "ds_read_b64_tr_b16 a[180:181], v207 offset:26624\n"
      "ds_read_b64_tr_b16 a[182:183], v207 offset:27136\n"
      "v_mfma_f32_32x32x16_bf16 v[32:47], a[104:107], a[196:199], v[32:47]\n"
      "s_add_u32 m0, s40, 50048\n"
      "ds_read_b64_tr_b16 a[138:139], v206 offset:17920\n"
      "buffer_load_dwordx4 v216, %1, s43 offen offset:128 lds\n"
      "ds_read_b64_tr_b16 a[184:185], v206 offset:27648\n"
      "ds_read_b64_tr_b16 a[186:187], v206 offset:28160\n"
      "v_mfma_f32_32x32x16_bf16 v[48:63], a[108:111], a[196:199], v[48:63]\n"
      "ds_read_b64_tr_b16 a[188:189], v207 offset:27648\n"
      "ds_read_b64_tr_b16 a[190:191], v207 offset:28160\n"
      "v_mfma_f32_32x32x16_bf16 v[64:79], a[112:115], a[196:199], v[64:79]\n"
      "s_add_u32 m0, s40, 50944\n"
      "ds_read_b64_tr_b16 a[140:141], v207 offset:17408\n"
      "buffer_load_dwordx4 v216, %1, s43 offen offset:256 lds\n"
      "v_mfma_f32_32x32x16_bf16 v[80:95], a[116:119], a[196:199], v[80:95]\n"
      "v_mfma_f32_32x32x16_bf16 v[96:111], a[120:123], a[196:199], v[96:111]\n"
      "s_add_u32 m0, s40, 51840\n"
      "ds_read_b64_tr_b16 a[142:143], v207 offset:17920\n"
      "buffer_load_dwordx4 v216, %1, s43 offen offset:384 lds\n"
      "s_add_u32 s43, s43, 0x4000\n"
      "s_and_b32 s43, s43, 0x7ffff\n"
      "v_mfma_f32_32x32x16_bf16 v[112:127], a[124:127], a[196:199], v[112:127]\n"
      "global_load_dword v235, v229, s[74:75]\n"
      "s_waitcnt vmcnt(9) lgkmcnt(0)\n"
      "s_barrier\n"
      "v_mfma_f32_32x32x16_bf16 v[0:15], a[128:131], a[200:203], v[0:15]\n"
      "s_add_u32 m0, s41, 16384\n"
      "ds_read_b128 a[208:211], v208 offset:16384\n"
      "buffer_load_dwordx4 v218, %4, s45 offen offset:0 lds\n"
      "ds_read_b64_tr_b16 a[64:65], v206 offset:32768\n"
      "ds_read_b64_tr_b16 a[66:67], v206 offset:33280\n"
      "v_mfma_f32_32x32x16_bf16 v[16:31], a[132:135], a[200:203], v[16:31]\n"
      "ds_read_b64_tr_b16 a[68:69], v207 offset:32768\n"
      "ds_read_b64_tr_b16 a[70:71], v207 offset:33280\n"
      "ds_read_b64_tr_b16 a[72:73], v206 offset:33792\n"
      "v_mfma_f32_32x32x16_bf16 v[32:47], a[136:139], a[200:203], v[32:47]\n"
      "s_add_u32 m0, s41, 17280\n"
      "ds_read_b128 a[212:215], v209 offset:16384\n"
      "buffer_load_dwordx4 v218, %4, s45 offen offset:128 lds\n"
      "ds_read_b64_tr_b16 a[74:75], v206 offset:34304\n"
      "ds_read_b64_tr_b16 a[76:77], v207 offset:33792\n"
      "v_mfma_f32_32x32x16_bf16 v[48:63], a[140:143], a[200:203], v[48:63]\n"
      "ds_read_b64_tr_b16 a[78:79], v207 offset:34304\n"
      "ds_read_b64_tr_b16 a[80:81], v206 offset:34816\n"
      "ds_read_b64_tr_b16 a[82:83], v206 offset:35328\n"
      "v_mfma_f32_32x32x16_bf16 v[64:79], a[144:147], a[200:203], v[64:79]\n"
      "s_add_u32 m0, s41, 18176\n"
      "ds_read_b128 a[216:219], v210 offset:16384\n"
      "buffer_load_dwordx4 v218, %4, s45 offen offset:256 lds\n"
      "ds_read_b64_tr_b16 a[84:85], v207 offset:34816\n"
      "ds_read_b64_tr_b16 a[86:87], v207 offset:35328\n"
      "ds_read_b64_tr_b16 a[88:89], v206 offset:35840\n"
      "v_mfma_f32_32x32x16_bf16 v[80:95], a[148:151], a[200:203], v[80:95]\n"
      "ds_read_b64_tr_b16 a[90:91], v206 offset:36352\n"
      "ds_read_b64_tr_b16 a[92:93], v207 offset:35840\n"
      "v_mfma_f32_32x32x16_bf16 v[96:111], a[152:155], a[200:203], v[96:111]\n"
      "s_add_u32 m0, s41, 19072\n"
      "ds_read_b128 a[220:223], v211 offset:16384\n"
      "buffer_load_dwordx4 v218, %4, s45 offen offset:384 lds\n"
      "s_add_u32 s45, s45, 0x40000\n"
      "s_cmp_eq_u32 s45, 0x380000\n"
      "s_cselect_b32 s45, 0, s45\n"
      "ds_read_b64_tr_b16 a[94:95], v207 offset:36352\n"
      "ds_read_b64_tr_b16 a[96:97], v206 offset:40960\n"
      "ds_read_b64_tr_b16 a[98:99], v206 offset:41472\n"
      "v_mfma_f32_32x32x16_bf16 v[112:127], a[156:159], a[200:203], v[112:127]\n"
      "ds_read_b64_tr_b16 a[100:101], v207 offset:40960\n"
      "ds_read_b64_tr_b16 a[102:103], v207 offset:41472\n"
      "v_mfma_f32_32x32x16_bf16 v[0:15], a[160:163], a[204:207], v[0:15]\n"
      "s_add_u32 m0, s42, 0\n"
      "s_nop 0\n"
      "buffer_load_dwordx4 v219, %3, s46 offen lds\n"
      "ds_read_b64_tr_b16 a[104:105], v206 offset:41984\n"
      "ds_read_b64_tr_b16 a[106:107], v206 offset:42496\n"
      "ds_read_b64_tr_b16 a[108:109], v207 offset:41984\n"
      "v_mfma_f32_32x32x16_bf16 v[16:31], a[164:167], a[204:207], v[16:31]\n"
      "ds_read_b64_tr_b16 a[110:111], v207 offset:42496\n"
      "ds_read_b64_tr_b16 a[112:113], v206 offset:43008\n"
      "ds_read_b64_tr_b16 a[114:115], v206 offset:43520\n"
      "v_mfma_f32_32x32x16_bf16 v[32:47], a[168:171], a[204:207], v[32:47]\n"
      "s_add_u32 m0, s42, 1024\n"
      "s_nop 0\n"
      "buffer_load_dwordx4 v220, %3, s46 offen lds\n"
      "ds_read_b64_tr_b16 a[116:117], v207 offset:43008\n"
      "ds_read_b64_tr_b16 a[118:119], v207 offset:43520\n"
      "v_mfma_f32_32x32x16_bf16 v[48:63], a[172:175], a[204:207], v[48:63]\n"
      "ds_read_b64_tr_b16 a[120:121], v206 offset:44032\n"
      "ds_read_b64_tr_b16 a[122:123], v206 offset:44544\n"
      "ds_read_b64_tr_b16 a[124:125], v207 offset:44032\n"
      "v_mfma_f32_32x32x16_bf16 v[64:79], a[176:179], a[204:207], v[64:79]\n"
      "s_add_u32 m0, s42, 2048\n"
      "s_nop 0\n"
      "buffer_load_dwordx4 v221, %3, s46 offen lds\n"
      "ds_read_b64_tr_b16 a[126:127], v207 offset:44544\n"
      "v_mfma_f32_32x32x16_bf16 v[80:95], a[180:183], a[204:207], v[80:95]\n"
      "v_mfma_f32_32x32x16_bf16 v[96:111], a[184:187], a[204:207], v[96:111]\n"
      "s_add_u32 m0, s42, 3072\n"
      "s_nop 0\n"
      "buffer_load_dwordx4 v222, %3, s46 offen lds\n"
      "s_add_u32 s46, s46, 128\n"
      "s_cmp_eq_u32 s46, 896\n"
      "s_cselect_b32 s46, 0, s46\n"
      "v_mfma_f32_32x32x16_bf16 v[112:127], a[188:191], a[204:207], v[112:127]\n"
      "s_waitcnt vmcnt(17) lgkmcnt(0)\n"
      "s_barrier\n"
      "v_mfma_f32_32x32x16_bf16 v[0:15], a[64:67], a[208:211], v[0:15]\n"
      "s_add_u32 m0, s41, 32768\n"
      "ds_read_b64_tr_b16 a[128:129], v206 offset:49152\n"
      "buffer_load_dwordx4 v217, %2, s44 offen offset:0 lds\n"
      "ds_read_b64_tr_b16 a[136:137], v206 offset:50176\n"
      "ds_read_b64_tr_b16 a[138:139], v206 offset:50688\n"
      "ds_read_b64_tr_b16 a[140:141], v207 offset:50176\n"
      "v_mfma_f32_32x32x16_bf16 v[16:31], a[68:71], a[208:211], v[16:31]\n"
      "ds_read_b64_tr_b16 a[142:143], v207 offset:50688\n"
      "ds_read_b64_tr_b16 a[144:145], v206 offset:51200\n"
      "v_mfma_f32_32x32x16_bf16 v[32:47], a[72:75], a[208:211], v[32:47]\n"
      "ds_read_b64_tr_b16 a[146:147], v206 offset:51712\n"
      "ds_read_b64_tr_b16 a[148:149], v207 offset:51200\n"
      "v_mfma_f32_32x32x16_bf16 v[48:63], a[76:79], a[208:211], v[48:63]\n"
      "ds_read_b64_tr_b16 a[150:151], v207 offset:51712\n"
      "ds_read_b64_tr_b16 a[152:153], v206 offset:52224\n"
      "v_mfma_f32_32x32x16_bf16 v[64:79], a[80:83], a[208:211], v[64:79]\n"
      "s_add_u32 m0, s41, 33664\n"
      "ds_read_b64_tr_b16 a[130:131], v206 offset:49664\n"
      "buffer_load_dwordx4 v217, %2, s44 offen offset:128 lds\n"
      "ds_read_b64_tr_b16 a[154:155], v206 offset:52736\n"
      "ds_read_b64_tr_b16 a[156:157], v207 offset:52224\n"
      "ds_read_b64_tr_b16 a[158:159], v207 offset:52736\n"
      "v_mfma_f32_32x32x16_bf16 v[80:95], a[84:87], a[208:211], v[80:95]\n"
      "ds_read_b64_tr_b16 a[160:161], v206 offset:57344\n"
      "ds_read_b64_tr_b16 a[162:163], v206 offset:57856\n"
      "v_mfma_f32_32x32x16_bf16 v[96:111], a[88:91], a[208:211], v[96:111]\n"
      "ds_read_b64_tr_b16 a[164:165], v207 offset:57344\n"
      "ds_read_b64_tr_b16 a[166:167], v207 offset:57856\n"
      "v_mfma_f32_32x32x16_bf16 v[112:127], a[92:95], a[208:211], v[112:127]\n"
      "ds_read_b64_tr_b16 a[168:169], v206 offset:58368\n"
      "ds_read_b64_tr_b16 a[170:171], v206 offset:58880\n"
      "v_mfma_f32_32x32x16_bf16 v[0:15], a[96:99], a[212:215], v[0:15]\n"
      "s_add_u32 m0, s41, 34560\n"
      "ds_read_b64_tr_b16 a[132:133], v207 offset:49152\n"
      "buffer_load_dwordx4 v217, %2, s44 offen offset:256 lds\n"
      "ds_read_b64_tr_b16 a[172:173], v207 offset:58368\n"
      "ds_read_b64_tr_b16 a[174:175], v207 offset:58880\n"
      "ds_read_b64_tr_b16 a[176:177], v206 offset:59392\n"
      "v_mfma_f32_32x32x16_bf16 v[16:31], a[100:103], a[212:215], v[16:31]\n"
      "ds_read_b64_tr_b16 a[178:179], v206 offset:59904\n"
      "ds_read_b64_tr_b16 a[180:181], v207 offset:59392\n"
      "v_mfma_f32_32x32x16_bf16 v[32:47], a[104:107], a[212:215], v[32:47]\n"
      "ds_read_b64_tr_b16 a[182:183], v207 offset:59904\n"
      "ds_read_b64_tr_b16 a[184:185], v206 offset:60416\n"
      "v_mfma_f32_32x32x16_bf16 v[48:63], a[108:111], a[212:215], v[48:63]\n"
      "ds_read_b64_tr_b16 a[186:187], v206 offset:60928\n"
      "ds_read_b64_tr_b16 a[188:189], v207 offset:60416\n"
      "v_mfma_f32_32x32x16_bf16 v[64:79], a[112:115], a[212:215], v[64:79]\n"
      "s_add_u32 m0, s41, 35456\n"
      "ds_read_b64_tr_b16 a[134:135], v207 offset:49664\n"
      "buffer_load_dwordx4 v217, %2, s44 offen offset:384 lds\n"
      "s_add_u32 s44, s44, 0x4000\n"
      "ds_read_b64_tr_b16 a[190:191], v207 offset:60928\n"
      "v_mfma_f32_32x32x16_bf16 v[80:95], a[116:119], a[212:215], v[80:95]\n"
      "v_mfma_f32_32x32x16_bf16 v[96:111], a[120:123], a[212:215], v[96:111]\n"
      "v_mfma_f32_32x32x16_bf16 v[112:127], a[124:127], a[212:215], v[112:127]\n"
      "s_waitcnt vmcnt(4) lgkmcnt(0)\n"
      "s_barrier\n"
      "v_mfma_f32_32x32x16_bf16 v[0:15], a[128:131], a[216:219], v[0:15]\n"
      "s_add_u32 m0, s41, 49152\n"
      "ds_read_b128 a[192:195], v208 offset:0\n"
      "buffer_load_dwordx4 v217, %2, s44 offen offset:0 lds\n"
      "ds_read_b64_tr_b16 a[64:65], v206 offset:0\n"
      "ds_read_b64_tr_b16 a[66:67], v206 offset:512\n"
      "v_mfma_f32_32x32x16_bf16 v[16:31], a[132:135], a[216:219], v[16:31]\n"
      "ds_read_b64_tr_b16 a[68:69], v207 offset:0\n"
      "ds_read_b64_tr_b16 a[70:71], v207 offset:512\n"
      "ds_read_b64_tr_b16 a[72:73], v206 offset:1024\n"
      "v_mfma_f32_32x32x16_bf16 v[32:47], a[136:139], a[216:219], v[32:47]\n"
      "ds_read_b64_tr_b16 a[74:75], v206 offset:1536\n"
      "ds_read_b64_tr_b16 a[76:77], v207 offset:1024\n"
      "v_mfma_f32_32x32x16_bf16 v[48:63], a[140:143], a[216:219], v[48:63]\n"
      "ds_read_b64_tr_b16 a[78:79], v207 offset:1536\n"
      "ds_read_b64_tr_b16 a[80:81], v206 offset:2048\n"
      "ds_read_b64_tr_b16 a[82:83], v206 offset:2560\n"
      "v_mfma_f32_32x32x16_bf16 v[64:79], a[144:147], a[216:219], v[64:79]\n"
      "s_add_u32 m0, s41, 50048\n"
      "ds_read_b128 a[196:199], v209 offset:0\n"
      "buffer_load_dwordx4 v217, %2, s44 offen offset:128 lds\n"
      "ds_read_b64_tr_b16 a[84:85], v207 offset:2048\n"
      "ds_read_b64_tr_b16 a[86:87], v207 offset:2560\n"
      "ds_read_b64_tr_b16 a[88:89], v206 offset:3072\n"
      "v_mfma_f32_32x32x16_bf16 v[80:95], a[148:151], a[216:219], v[80:95]\n"
      "ds_read_b64_tr_b16 a[90:91], v206 offset:3584\n"
      "ds_read_b64_tr_b16 a[92:93], v207 offset:3072\n"
      "v_mfma_f32_32x32x16_bf16 v[96:111], a[152:155], a[216:219], v[96:111]\n"
      "ds_read_b64_tr_b16 a[94:95], v207 offset:3584\n"
      "ds_read_b64_tr_b16 a[96:97], v206 offset:8192\n"
      "ds_read_b64_tr_b16 a[98:99], v206 offset:8704\n"
      "v_mfma_f32_32x32x16_bf16 v[112:127], a[156:159], a[216:219], v[112:127]\n"
      "ds_read_b64_tr_b16 a[100:101], v207 offset:8192\n"
      "ds_read_b64_tr_b16 a[102:103], v207 offset:8704\n"
      "v_mfma_f32_32x32x16_bf16 v[0:15], a[160:163], a[220:223], v[0:15]\n"
      "s_add_u32 m0, s41, 50944\n"
      "ds_read_b128 a[200:203], v210 offset:0\n"
      "buffer_load_dwordx4 v217, %2, s44 offen offset:256 lds\n"
      "ds_read_b64_tr_b16 a[104:105], v206 offset:9216\n"
      "ds_read_b64_tr_b16 a[106:107], v206 offset:9728\n"
      "ds_read_b64_tr_b16 a[108:109], v207 offset:9216\n"
      "v_mfma_f32_32x32x16_bf16 v[16:31], a[164:167], a[220:223], v[16:31]\n"
      "ds_read_b64_tr_b16 a[110:111], v207 offset:9728\n"
      "ds_read_b64_tr_b16 a[112:113], v206 offset:10240\n"
      "ds_read_b64_tr_b16 a[114:115], v206 offset:10752\n"
      "v_mfma_f32_32x32x16_bf16 v[32:47], a[168:171], a[220:223], v[32:47]\n"
      "ds_read_b64_tr_b16 a[116:117], v207 offset:10240\n"
      "ds_read_b64_tr_b16 a[118:119], v207 offset:10752\n"
      "v_mfma_f32_32x32x16_bf16 v[48:63], a[172:175], a[220:223], v[48:63]\n"
      "ds_read_b64_tr_b16 a[120:121], v206 offset:11264\n"
      "ds_read_b64_tr_b16 a[122:123], v206 offset:11776\n"
      "ds_read_b64_tr_b16 a[124:125], v207 offset:11264\n"
      "v_mfma_f32_32x32x16_bf16 v[64:79], a[176:179], a[220:223], v[64:79]\n"
      "s_add_u32 m0, s41, 51840\n"
      "ds_read_b128 a[204:207], v211 offset:0\n"
      "buffer_load_dwordx4 v217, %2, s44 offen offset:384 lds\n"
      "s_add_u32 s44, s44, 0x4000\n"
      "ds_read_b64_tr_b16 a[126:127], v207 offset:11776\n"
      "v_mfma_f32_32x32x16_bf16 v[80:95], a[180:183], a[220:223], v[80:95]\n"
      "v_mfma_f32_32x32x16_bf16 v[96:111], a[184:187], a[220:223], v[96:111]\n"
      "v_mfma_f32_32x32x16_bf16 v[112:127], a[188:191], a[220:223], v[112:127]\n"
      "s_waitcnt vmcnt(8) lgkmcnt(0)\n"
      "s_barrier\n"
      "v_mfma_f32_32x32x16_bf16 v[0:15], a[64:67], a[192:195], v[0:15]\n"
      "s_add_u32 m0, s41, 0\n"
      "ds_read_b64_tr_b16 a[128:129], v206 offset:16384\n"
      "buffer_load_dwordx4 v217, %2, s44 offen offset:0 lds\n"
      "ds_read_b64_tr_b16 a[136:137], v206 offset:17408\n"
      "ds_read_b64_tr_b16 a[138:139], v206 offset:17920\n"
      "ds_read_b64_tr_b16 a[140:141], v207 offset:17408\n"
      "v_mfma_f32_32x32x16_bf16 v[16:31], a[68:71], a[192:195], v[16:31]\n"
      "ds_read_b64_tr_b16 a[142:143], v207 offset:17920\n"
      "ds_read_b64_tr_b16 a[144:145], v206 offset:18432\n"
      "v_mfma_f32_32x32x16_bf16 v[32:47], a[72:75], a[192:195], v[32:47]\n"
      "ds_read_b64_tr_b16 a[146:147], v206 offset:18944\n"
      "ds_read_b64_tr_b16 a[148:149], v207 offset:18432\n"
      "v_mfma_f32_32x32x16_bf16 v[48:63], a[76:79], a[192:195], v[48:63]\n"
      "ds_read_b64_tr_b16 a[150:151], v207 offset:18944\n"
      "ds_read_b64_tr_b16 a[152:153], v206 offset:19456\n"
      "v_mfma_f32_32x32x16_bf16 v[64:79], a[80:83], a[192:195], v[64:79]\n"
      "s_add_u32 m0, s41, 896\n"
      "ds_read_b64_tr_b16 a[130:131], v206 offset:16896\n"
      "buffer_load_dwordx4 v217, %2, s44 offen offset:128 lds\n"
      "ds_read_b64_tr_b16 a[154:155], v206 offset:19968\n"
      "ds_read_b64_tr_b16 a[156:157], v207 offset:19456\n"
      "ds_read_b64_tr_b16 a[158:159], v207 offset:19968\n"
      "v_mfma_f32_32x32x16_bf16 v[80:95], a[84:87], a[192:195], v[80:95]\n"
      "ds_read_b64_tr_b16 a[160:161], v206 offset:24576\n"
      "ds_read_b64_tr_b16 a[162:163], v206 offset:25088\n"
      "v_mfma_f32_32x32x16_bf16 v[96:111], a[88:91], a[192:195], v[96:111]\n"
      "ds_read_b64_tr_b16 a[164:165], v207 offset:24576\n"
      "ds_read_b64_tr_b16 a[166:167], v207 offset:25088\n"
      "v_mfma_f32_32x32x16_bf16 v[112:127], a[92:95], a[192:195], v[112:127]\n"
      "ds_read_b64_tr_b16 a[168:169], v206 offset:25600\n"
      "ds_read_b64_tr_b16 a[170:171], v206 offset:26112\n"
      "v_mfma_f32_32x32x16_bf16 v[0:15], a[96:99], a[196:199], v[0:15]\n"
      "s_add_u32 m0, s41, 1792\n"
      "ds_read_b64_tr_b16 a[132:133], v207 offset:16384\n"
      "buffer_load_dwordx4 v217, %2, s44 offen offset:256 lds\n"
      "ds_read_b64_tr_b16 a[172:173], v207 offset:25600\n"
      "ds_read_b64_tr_b16 a[174:175], v207 offset:26112\n"
      "ds_read_b64_tr_b16 a[176:177], v206 offset:26624\n"
      "v_mfma_f32_32x32x16_bf16 v[16:31], a[100:103], a[196:199], v[16:31]\n"
      "ds_read_b64_tr_b16 a[178:179], v206 offset:27136\n"
      "ds_read_b64_tr_b16 a[180:181], v207 offset:26624\n"
      "v_mfma_f32_32x32x16_bf16 v[32:47], a[104:107], a[196:199], v[32:47]\n"
      "ds_read_b64_tr_b16 a[182:183], v207 offset:27136\n"
      "ds_read_b64_tr_b16 a[184:185], v206 offset:27648\n"
      "v_mfma_f32_32x32x16_bf16 v[48:63], a[108:111], a[196:199], v[48:63]\n"
      "ds_read_b64_tr_b16 a[186:187], v206 offset:28160\n"
      "ds_read_b64_tr_b16 a[188:189], v207 offset:27648\n"
      "v_mfma_f32_32x32x16_bf16 v[64:79], a[112:115], a[196:199], v[64:79]\n"
      "s_add_u32 m0, s41, 2688\n"
      "ds_read_b64_tr_b16 a[134:135], v207 offset:16896\n"
      "buffer_load_dwordx4 v217, %2, s44 offen offset:384 lds\n"
      "s_add_u32 s44, s44, 0x4000\n"
      "ds_read_b64_tr_b16 a[190:191], v207 offset:28160\n"
      "v_mfma_f32_32x32x16_bf16 v[80:95], a[116:119], a[196:199], v[80:95]\n"
      "v_mfma_f32_32x32x16_bf16 v[96:111], a[120:123], a[196:199], v[96:111]\n"
      "v_mfma_f32_32x32x16_bf16 v[112:127], a[124:127], a[196:199], v[112:127]\n"
      "s_waitcnt vmcnt(12) lgkmcnt(0)\n"
      "s_barrier\n"
      "v_mfma_f32_32x32x16_bf16 v[0:15], a[128:131], a[200:203], v[0:15]\n"
      "ds_read_b128 a[64:67], v200 offset:0\n"
      "ds_read_b128 a[68:71], v201 offset:0\n"
      "v_mfma_f32_32x32x16_bf16 v[0:15], a[160:163], a[204:207], v[0:15]\n"
      "ds_read_b128 a[72:75], v202 offset:0\n"
      "v_mfma_f32_32x32x16_bf16 v[16:31], a[132:135], a[200:203], v[16:31]\n"
      "ds_read_b128 a[76:79], v203 offset:0\n"
      "s_cmp_lg_u32 %14, 0\n"
      "s_cbranch_scc1 LcA_cf_%=\n"
      "v_mul_lo_u32 v230, %0, s72\n"
      "v_add_u32 v230, 0x9e3779b9, v230\n"
      "v_not_b32 v230, v230\n"
      "global_store_dword v233, v230, %15 sc1\n"
      "global_store_dword v233, v230, %15 offset:-256 sc1\n"
      "LcA_cf_%=:\n"
      "v_mfma_f32_32x32x16_bf16 v[16:31], a[164:167], a[204:207], v[16:31]\n"
      "ds_read_b128 a[80:83], v200 offset:1024\n"
      "ds_read_b128 a[84:87], v201 offset:1024\n"
      "v_mfma_f32_32x32x16_bf16 v[32:47], a[136:139], a[200:203], v[32:47]\n"
      "ds_read_b128 a[88:91], v202 offset:1024\n"
      "v_mfma_f32_32x32x16_bf16 v[32:47], a[168:171], a[204:207], v[32:47]\n"
      "ds_read_b128 a[92:95], v203 offset:1024\n"
      "s_nop 1\n"
      "v_pk_mul_f32 v[0:1], v[0:1], s[52:53]\n"
      "v_pk_mul_f32 v[2:3], v[2:3], s[52:53]\n"
      "v_pk_mul_f32 v[4:5], v[4:5], s[52:53]\n"
      "v_pk_mul_f32 v[6:7], v[6:7], s[52:53]\n"
      "v_cvt_pk_bf16_f32 v184, v0, v1\n"
      "v_cvt_pk_bf16_f32 v185, v2, v3\n"
      "v_cvt_pk_bf16_f32 v186, v4, v5\n"
      "v_cvt_pk_bf16_f32 v187, v6, v7\n"
      "v_mfma_f32_32x32x16_bf16 v[48:63], a[140:143], a[200:203], v[48:63]\n"
      "ds_read_b128 a[96:99], v200 offset:2048\n"
      "ds_read_b128 a[100:103], v201 offset:2048\n"
      "v_accvgpr_write_b32 a0, v184\n"
      "v_accvgpr_write_b32 a1, v185\n"
      "v_accvgpr_write_b32 a2, v186\n"
      "v_accvgpr_write_b32 a3, v187\n"
      "v_pk_mul_f32 v[8:9], v[8:9], s[52:53]\n"
      "v_pk_mul_f32 v[10:11], v[10:11], s[52:53]\n"
      "v_pk_mul_f32 v[12:13], v[12:13], s[52:53]\n"
      "v_pk_mul_f32 v[14:15], v[14:15], s[52:53]\n"
      "v_mfma_f32_32x32x16_bf16 v[48:63], a[172:175], a[204:207], v[48:63]\n"
      "ds_read_b128 a[104:107], v202 offset:2048\n"
      "v_cvt_pk_bf16_f32 v188, v8, v9\n"
      "v_cvt_pk_bf16_f32 v189, v10, v11\n"
      "v_cvt_pk_bf16_f32 v190, v12, v13\n"
      "v_cvt_pk_bf16_f32 v191, v14, v15\n"
      "v_accvgpr_write_b32 a4, v188\n"
      "v_accvgpr_write_b32 a5, v189\n"
      "v_accvgpr_write_b32 a6, v190\n"
      "v_accvgpr_write_b32 a7, v191\n"
      "v_mfma_f32_32x32x16_bf16 v[64:79], a[144:147], a[200:203], v[64:79]\n"
      "ds_read_b128 a[108:111], v203 offset:2048\n"
      "v_pk_mul_f32 v[16:17], v[16:17], s[52:53]\n"
      "v_pk_mul_f32 v[18:19], v[18:19], s[52:53]\n"
      "v_pk_mul_f32 v[20:21], v[20:21], s[52:53]\n"
      "v_pk_mul_f32 v[22:23], v[22:23], s[52:53]\n"
      "v_cvt_pk_bf16_f32 v184, v16, v17\n"
      "v_cvt_pk_bf16_f32 v185, v18, v19\n"
      "v_cvt_pk_bf16_f32 v186, v20, v21\n"
      "v_cvt_pk_bf16_f32 v187, v22, v23\n"
      "v_mfma_f32_32x32x16_bf16 v[64:79], a[176:179], a[204:207], v[64:79]\n"
      "ds_read_b128 a[112:115], v200 offset:3072\n"
      "ds_read_b128 a[116:119], v201 offset:3072\n"
      "v_accvgpr_write_b32 a8, v184\n"
      "v_accvgpr_write_b32 a9, v185\n"
      "v_accvgpr_write_b32 a10, v186\n"
      "v_accvgpr_write_b32 a11, v187\n"
      "v_pk_mul_f32 v[24:25], v[24:25], s[52:53]\n"
      "v_pk_mul_f32 v[26:27], v[26:27], s[52:53]\n"
      "v_pk_mul_f32 v[28:29], v[28:29], s[52:53]\n"
      "v_pk_mul_f32 v[30:31], v[30:31], s[52:53]\n"
      "v_mfma_f32_32x32x16_bf16 v[80:95], a[148:151], a[200:203], v[80:95]\n"
      "ds_read_b128 a[120:123], v202 offset:3072\n"
      "v_cvt_pk_bf16_f32 v188, v24, v25\n"
      "v_cvt_pk_bf16_f32 v189, v26, v27\n"
      "v_cvt_pk_bf16_f32 v190, v28, v29\n"
      "v_cvt_pk_bf16_f32 v191, v30, v31\n"
      "v_accvgpr_write_b32 a12, v188\n"
      "v_accvgpr_write_b32 a13, v189\n"
      "v_accvgpr_write_b32 a14, v190\n"
      "v_accvgpr_write_b32 a15, v191\n"
      "v_mfma_f32_32x32x16_bf16 v[80:95], a[180:183], a[204:207], v[80:95]\n"
      "ds_read_b128 a[124:127], v203 offset:3072\n"
      "v_pk_mul_f32 v[32:33], v[32:33], s[52:53]\n"
      "v_pk_mul_f32 v[34:35], v[34:35], s[52:53]\n"
      "v_pk_mul_f32 v[36:37], v[36:37], s[52:53]\n"
      "v_pk_mul_f32 v[38:39], v[38:39], s[52:53]\n"
      "v_cvt_pk_bf16_f32 v184, v32, v33\n"
      "v_cvt_pk_bf16_f32 v185, v34, v35\n"
      "v_cvt_pk_bf16_f32 v186, v36, v37\n"
      "v_cvt_pk_bf16_f32 v187, v38, v39\n"
      "v_mfma_f32_32x32x16_bf16 v[96:111], a[152:155], a[200:203], v[96:111]\n"
      "v_accvgpr_write_b32 a16, v184\n"
      "v_accvgpr_write_b32 a17, v185\n"
      "v_accvgpr_write_b32 a18, v186\n"
      "v_accvgpr_write_b32 a19, v187\n"
      "v_pk_mul_f32 v[40:41], v[40:41], s[52:53]\n"
      "v_pk_mul_f32 v[42:43], v[42:43], s[52:53]\n"
      "v_pk_mul_f32 v[44:45], v[44:45], s[52:53]\n"
      "v_pk_mul_f32 v[46:47], v[46:47], s[52:53]\n"
      "v_mfma_f32_32x32x16_bf16 v[96:111], a[184:187], a[204:207], v[96:111]\n"
      "v_cvt_pk_bf16_f32 v188, v40, v41\n"
      "v_cvt_pk_bf16_f32 v189, v42, v43\n"
      "v_cvt_pk_bf16_f32 v190, v44, v45\n"
      "v_cvt_pk_bf16_f32 v191, v46, v47\n"
      "v_accvgpr_write_b32 a20, v188\n"
      "v_accvgpr_write_b32 a21, v189\n"
      "v_accvgpr_write_b32 a22, v190\n"
      "v_accvgpr_write_b32 a23, v191\n"
      "v_mfma_f32_32x32x16_bf16 v[112:127], a[156:159], a[200:203], v[112:127]\n"
      "v_pk_mul_f32 v[48:49], v[48:49], s[52:53]\n"
      "v_pk_mul_f32 v[50:51], v[50:51], s[52:53]\n"
      "v_pk_mul_f32 v[52:53], v[52:53], s[52:53]\n"
      "v_pk_mul_f32 v[54:55], v[54:55], s[52:53]\n"
      "v_cvt_pk_bf16_f32 v184, v48, v49\n"
      "v_cvt_pk_bf16_f32 v185, v50, v51\n"
      "v_cvt_pk_bf16_f32 v186, v52, v53\n"
      "v_cvt_pk_bf16_f32 v187, v54, v55\n"
      "v_accvgpr_write_b32 a24, v184\n"
      "v_accvgpr_write_b32 a25, v185\n"
      "v_accvgpr_write_b32 a26, v186\n"
      "v_accvgpr_write_b32 a27, v187\n"
      "v_mfma_f32_32x32x16_bf16 v[112:127], a[188:191], a[204:207], v[112:127]\n"
      "v_pk_mul_f32 v[56:57], v[56:57], s[52:53]\n"
      "v_pk_mul_f32 v[58:59], v[58:59], s[52:53]\n"
      "v_pk_mul_f32 v[60:61], v[60:61], s[52:53]\n"
      "v_pk_mul_f32 v[62:63], v[62:63], s[52:53]\n"
      "v_cvt_pk_bf16_f32 v188, v56, v57\n"
      "v_cvt_pk_bf16_f32 v189, v58, v59\n"
      "v_cvt_pk_bf16_f32 v190, v60, v61\n"
      "v_cvt_pk_bf16_f32 v191, v62, v63\n"
      "v_accvgpr_write_b32 a28, v188\n"
      "v_accvgpr_write_b32 a29, v189\n"
      "v_accvgpr_write_b32 a30, v190\n"
      "v_accvgpr_write_b32 a31, v191\n"
      "s_waitcnt lgkmcnt(15)\n"
      "v_mfma_f32_32x32x16_bf16 v[128:143], a[64:67], a[0:3], 0\n"
      "v_pk_mul_f32 v[64:65], v[64:65], s[52:53]\n"
      "v_pk_mul_f32 v[66:67], v[66:67], s[52:53]\n"
      "v_pk_mul_f32 v[68:69], v[68:69], s[52:53]\n"
      "v_pk_mul_f32 v[70:71], v[70:71], s[52:53]\n"
      "v_cvt_pk_bf16_f32 v184, v64, v65\n"
      "v_cvt_pk_bf16_f32 v185, v66, v67\n"
      "v_cvt_pk_bf16_f32 v186, v68, v69\n"
      "v_cvt_pk_bf16_f32 v187, v70, v71\n"
      "s_waitcnt lgkmcnt(14)\n"
      "v_mfma_f32_32x32x16_bf16 v[128:143], a[68:71], a[4:7], v[128:143]\n"
      "v_accvgpr_write_b32 a32, v184\n"
      "v_accvgpr_write_b32 a33, v185\n"
      "v_accvgpr_write_b32 a34, v186\n"
      "v_accvgpr_write_b32 a35, v187\n"
      "v_pk_mul_f32 v[72:73], v[72:73], s[52:53]\n"
      "v_pk_mul_f32 v[74:75], v[74:75], s[52:53]\n"
      "v_pk_mul_f32 v[76:77], v[76:77], s[52:53]\n"
      "v_pk_mul_f32 v[78:79], v[78:79], s[52:53]\n"
      "s_waitcnt lgkmcnt(13)\n"
      "v_mfma_f32_32x32x16_bf16 v[128:143], a[72:75], a[8:11], v[128:143]\n"
      "v_cvt_pk_bf16_f32 v188, v72, v73\n"
      "v_cvt_pk_bf16_f32 v189, v74, v75\n"
      "v_cvt_pk_bf16_f32 v190, v76, v77\n"
      "v_cvt_pk_bf16_f32 v191, v78, v79\n"
      "v_accvgpr_write_b32 a36, v188\n"
      "v_accvgpr_write_b32 a37, v189\n"
      "v_accvgpr_write_b32 a38, v190\n"
      "v_accvgpr_write_b32 a39, v191\n"
      "s_waitcnt lgkmcnt(12)\n"
      "v_mfma_f32_32x32x16_bf16 v[128:143], a[76:79], a[12:15], v[128:143]\n"
      "v_pk_mul_f32 v[80:81], v[80:81], s[52:53]\n"
      "v_pk_mul_f32 v[82:83], v[82:83], s[52:53]\n"
      "v_pk_mul_f32 v[84:85], v[84:85], s[52:53]\n"
      "v_pk_mul_f32 v[86:87], v[86:87], s[52:53]\n"
      "v_cvt_pk_bf16_f32 v184, v80, v81\n"
      "v_cvt_pk_bf16_f32 v185, v82, v83\n"
      "v_cvt_pk_bf16_f32 v186, v84, v85\n"
      "v_cvt_pk_bf16_f32 v187, v86, v87\n"
      "s_waitcnt lgkmcnt(11)\n"
      "v_mfma_f32_32x32x16_bf16 v[128:143], a[80:83], a[16:19], v[128:143]\n"
      "v_accvgpr_write_b32 a40, v184\n"
      "v_accvgpr_write_b32 a41, v185\n"
      "v_accvgpr_write_b32 a42, v186\n"
      "v_accvgpr_write_b32 a43, v187\n"
      "v_pk_mul_f32 v[88:89], v[88:89], s[52:53]\n"
      "v_pk_mul_f32 v[90:91], v[90:91], s[52:53]\n"
      "v_pk_mul_f32 v[92:93], v[92:93], s[52:53]\n"
      "v_pk_mul_f32 v[94:95], v[94:95], s[52:53]\n"
      "s_waitcnt lgkmcnt(10)\n"
      "v_mfma_f32_32x32x16_bf16 v[128:143], a[84:87], a[20:23], v[128:143]\n"
      "v_cvt_pk_bf16_f32 v188, v88, v89\n"
      "v_cvt_pk_bf16_f32 v189, v90, v91\n"
      "v_cvt_pk_bf16_f32 v190, v92, v93\n"
      "v_cvt_pk_bf16_f32 v191, v94, v95\n"
      "v_accvgpr_write_b32 a44, v188\n"
      "v_accvgpr_write_b32 a45, v189\n"
      "v_accvgpr_write_b32 a46, v190\n"
      "v_accvgpr_write_b32 a47, v191\n"
      "s_waitcnt lgkmcnt(9)\n"
      "v_mfma_f32_32x32x16_bf16 v[128:143], a[88:91], a[24:27], v[128:143]\n"
      "v_pk_mul_f32 v[96:97], v[96:97], s[52:53]\n"
      "v_pk_mul_f32 v[98:99], v[98:99], s[52:53]\n"
      "v_pk_mul_f32 v[100:101], v[100:101], s[52:53]\n"
      "v_pk_mul_f32 v[102:103], v[102:103], s[52:53]\n"
      "v_cvt_pk_bf16_f32 v184, v96, v97\n"
      "v_cvt_pk_bf16_f32 v185, v98, v99\n"
      "v_cvt_pk_bf16_f32 v186, v100, v101\n"
      "v_cvt_pk_bf16_f32 v187, v102, v103\n"
      "s_waitcnt lgkmcnt(8)\n"
      "v_mfma_f32_32x32x16_bf16 v[128:143], a[92:95], a[28:31], v[128:143]\n"
      "v_accvgpr_write_b32 a48, v184\n"
      "v_accvgpr_write_b32 a49, v185\n"
      "v_accvgpr_write_b32 a50, v186\n"
      "v_accvgpr_write_b32 a51, v187\n"
      "v_pk_mul_f32 v[104:105], v[104:105], s[52:53]\n"
      "v_pk_mul_f32 v[106:107], v[106:107], s[52:53]\n"
      "v_pk_mul_f32 v[108:109], v[108:109], s[52:53]\n"
      "v_pk_mul_f32 v[110:111], v[110:111], s[52:53]\n"
      "s_waitcnt lgkmcnt(7)\n"
      "v_mfma_f32_32x32x16_bf16 v[128:143], a[96:99], a[32:35], v[128:143]\n"
      "v_cvt_pk_bf16_f32 v188, v104, v105\n"
      "v_cvt_pk_bf16_f32 v189, v106, v107\n"
      "v_cvt_pk_bf16_f32 v190, v108, v109\n"
      "v_cvt_pk_bf16_f32 v191, v110, v111\n"
      "v_accvgpr_write_b32 a52, v188\n"
      "v_accvgpr_write_b32 a53, v189\n"
      "v_accvgpr_write_b32 a54, v190\n"
      "v_accvgpr_write_b32 a55, v191\n"
      "s_waitcnt lgkmcnt(6)\n"
      "v_mfma_f32_32x32x16_bf16 v[128:143], a[100:103], a[36:39], v[128:143]\n"
      "v_pk_mul_f32 v[112:113], v[112:113], s[52:53]\n"
      "v_pk_mul_f32 v[114:115], v[114:115], s[52:53]\n"
      "v_pk_mul_f32 v[116:117], v[116:117], s[52:53]\n"
      "v_pk_mul_f32 v[118:119], v[118:119], s[52:53]\n"
      "v_cvt_pk_bf16_f32 v184, v112, v113\n"
      "v_cvt_pk_bf16_f32 v185, v114, v115\n"
      "v_cvt_pk_bf16_f32 v186, v116, v117\n"
      "v_cvt_pk_bf16_f32 v187, v118, v119\n"
      "s_waitcnt lgkmcnt(5)\n"
      "v_mfma_f32_32x32x16_bf16 v[128:143], a[104:107], a[40:43], v[128:143]\n"
      "v_accvgpr_write_b32 a56, v184\n"
      "v_accvgpr_write_b32 a57, v185\n"
      "v_accvgpr_write_b32 a58, v186\n"
      "v_accvgpr_write_b32 a59, v187\n"
      "v_pk_mul_f32 v[120:121], v[120:121], s[52:53]\n"
      "v_pk_mul_f32 v[122:123], v[122:123], s[52:53]\n"
      "v_pk_mul_f32 v[124:125], v[124:125], s[52:53]\n"
      "v_pk_mul_f32 v[126:127], v[126:127], s[52:53]\n"
      "s_waitcnt lgkmcnt(4)\n"
      "v_mfma_f32_32x32x16_bf16 v[128:143], a[108:111], a[44:47], v[128:143]\n"
      "v_cvt_pk_bf16_f32 v188, v120, v121\n"
      "v_cvt_pk_bf16_f32 v189, v122, v123\n"
      "v_cvt_pk_bf16_f32 v190, v124, v125\n"
      "v_cvt_pk_bf16_f32 v191, v126, v127\n"
      "v_accvgpr_write_b32 a60, v188\n"
      "v_accvgpr_write_b32 a61, v189\n"
      "v_accvgpr_write_b32 a62, v190\n"
      "v_accvgpr_write_b32 a63, v191\n"
      "s_waitcnt lgkmcnt(3)\n"
      "v_mfma_f32_32x32x16_bf16 v[128:143], a[112:115], a[48:51], v[128:143]\n"
      "s_waitcnt lgkmcnt(2)\n"
      "v_mfma_f32_32x32x16_bf16 v[128:143], a[116:119], a[52:55], v[128:143]\n"
      "s_waitcnt lgkmcnt(1)\n"
      "v_mfma_f32_32x32x16_bf16 v[128:143], a[120:123], a[56:59], v[128:143]\n"
      "s_waitcnt lgkmcnt(0)\n"
      "v_mfma_f32_32x32x16_bf16 v[128:143], a[124:127], a[60:63], v[128:143]\n"
      "s_waitcnt vmcnt(8) lgkmcnt(0)\n"
      "s_barrier\n"
      "s_add_u32 m0, s40, 0\n"
      "ds_read_b128 a[64:67], v200 offset:16384\n"
      "buffer_load_dwordx4 v216, %1, s43 offen offset:0 lds\n"
      "ds_read_b128 a[68:71], v201 offset:16384\n"
      "ds_read_b64_tr_b16 a[128:129], v204 offset:32768\n"
      "ds_read_b128 a[72:75], v202 offset:16384\n"
      "ds_read_b64_tr_b16 a[130:131], v204 offset:36864\n"
      "ds_read_b128 a[76:79], v203 offset:16384\n"
      "ds_read_b64_tr_b16 a[132:133], v205 offset:32768\n"
      "s_nop 0\n"
      "v_max3_f32 v194, v128, v129, v130\n"
      "v_max3_f32 v195, v131, v132, v133\n"
      "s_add_u32 m0, s40, 896\n"
      "ds_read_b128 a[80:83], v200 offset:17408\n"
      "v_max3_f32 v194, v194, v134, v135\n"
      "v_max3_f32 v195, v195, v136, v137\n"
      "buffer_load_dwordx4 v216, %1, s43 offen offset:128 lds\n"
      "ds_read_b128 a[84:87], v201 offset:17408\n"
      "ds_read_b64_tr_b16 a[134:135], v205 offset:36864\n"
      "v_max3_f32 v194, v194, v138, v139\n"
      "v_max3_f32 v195, v195, v140, v141\n"
      "ds_read_b128 a[88:91], v202 offset:17408\n"
      "ds_read_b64_tr_b16 a[136:137], v204 offset:33792\n"
      "v_max3_f32 v194, v194, v142, v143\n"
      "v_max_f32 v194, v194, v195\n"
      "ds_read_b128 a[92:95], v203 offset:17408\n"
      "ds_read_b64_tr_b16 a[138:139], v204 offset:37888\n"
      "ds_read_b64_tr_b16 a[140:141], v205 offset:33792\n"
      "s_add_u32 m0, s40, 1792\n"
      "ds_read_b128 a[96:99], v200 offset:18432\n"
      "v_mov_b32 v196, v194\n"
      "s_nop 1\n"
      "v_permlane32_swap_b32 v196, v194\n"
      "v_max_f32 v194, v194, v196\n"
      "v_sub_f32 v160, 0, v194\n"
      "v_sub_f32 v161, 0, v194\n"
      "v_sub_f32 v162, 0, v194\n"
      "v_sub_f32 v163, 0, v194\n"
      "v_sub_f32 v164, 0, v194\n"
      "v_sub_f32 v165, 0, v194\n"
      "v_sub_f32 v166, 0, v194\n"
      "v_sub_f32 v167, 0, v194\n"
      "v_sub_f32 v168, 0, v194\n"
      "v_sub_f32 v169, 0, v194\n"
      "v_sub_f32 v170, 0, v194\n"
      "v_sub_f32 v171, 0, v194\n"
      "v_sub_f32 v172, 0, v194\n"
      "v_sub_f32 v173, 0, v194\n"
      "v_sub_f32 v174, 0, v194\n"
      "v_sub_f32 v175, 0, v194\n"
      "v_sub_f32 v128, v128, v194\n"
      "v_sub_f32 v129, v129, v194\n"
      "v_sub_f32 v130, v130, v194\n"
      "v_sub_f32 v131, v131, v194\n"
      "v_sub_f32 v132, v132, v194\n"
      "v_sub_f32 v133, v133, v194\n"
      "v_sub_f32 v134, v134, v194\n"
      "v_sub_f32 v135, v135, v194\n"
      "v_sub_f32 v136, v136, v194\n"
      "v_sub_f32 v137, v137, v194\n"
      "v_sub_f32 v138, v138, v194\n"
      "v_sub_f32 v139, v139, v194\n"
      "v_sub_f32 v140, v140, v194\n"
      "v_sub_f32 v141, v141, v194\n"
      "v_sub_f32 v142, v142, v194\n"
      "v_sub_f32 v143, v143, v194\n"
      "v_mov_b32 v192, 0\n"
      "v_mov_b32 v193, 0\n"
      "buffer_load_dwordx4 v216, %1, s43 offen offset:256 lds\n"
      "ds_read_b128 a[100:103], v201 offset:18432\n"
      "ds_read_b64_tr_b16 a[142:143], v205 offset:37888\n"
      "ds_read_b64_tr_b16 a[144:145], v204 offset:34816\n"
      "v_exp_f32 v128, v128\n"
      "v_exp_f32 v129, v129\n"
      "v_exp_f32 v130, v130\n"
      "v_exp_f32 v131, v131\n"
      "v_exp_f32 v132, v132\n"
      "v_exp_f32 v133, v133\n"
      "v_exp_f32 v134, v134\n"
      "v_exp_f32 v135, v135\n"
      "v_exp_f32 v136, v136\n"
      "v_exp_f32 v137, v137\n"
      "v_exp_f32 v138, v138\n"
      "v_exp_f32 v139, v139\n"
      "v_exp_f32 v140, v140\n"
      "v_exp_f32 v141, v141\n"
      "v_exp_f32 v142, v142\n"
      "v_exp_f32 v143, v143\n"
      "s_waitcnt lgkmcnt(2)\n"
      "v_mfma_f32_32x32x16_bf16 v[144:159], a[64:67], a[0:3], v[160:175]\n"
      "s_add_u32 m0, s40, 2688\n"
      "ds_read_b128 a[104:107], v202 offset:18432\n"
      "v_add_f32 v192, v192, v128\n"
      "v_add_f32 v193, v193, v129\n"
      "buffer_load_dwordx4 v216, %1, s43 offen offset:384 lds\n"
      "s_add_u32 s43, s43, 0x4000\n"
      "v_mfma_f32_32x32x16_bf16 v[144:159], a[68:71], a[4:7], v[144:159]\n"
      "ds_read_b128 a[108:111], v203 offset:18432\n"
      "ds_read_b64_tr_b16 a[146:147], v204 offset:38912\n"
      "v_add_f32 v192, v192, v130\n"
      "v_cvt_pk_bf16_f32 v176, v128, v129\n"
      "v_mfma_f32_32x32x16_bf16 v[144:159], a[72:75], a[8:11], v[144:159]\n"
      "ds_read_b128 a[112:115], v200 offset:19456\n"
      "ds_read_b64_tr_b16 a[148:149], v205 offset:34816\n"
      "v_add_f32 v193, v193, v131\n"
      "v_cvt_pk_bf16_f32 v177, v130, v131\n"
      "v_mfma_f32_32x32x16_bf16 v[144:159], a[76:79], a[12:15], v[144:159]\n"
      "ds_read_b128 a[116:119], v201 offset:19456\n"
      "ds_read_b64_tr_b16 a[150:151], v205 offset:38912\n"
      "v_add_f32 v192, v192, v132\n"
      "v_cvt_pk_bf16_f32 v178, v132, v133\n"
      "v_mfma_f32_32x32x16_bf16 v[144:159], a[80:83], a[16:19], v[144:159]\n"
      "s_add_u32 m0, s41, 16384\n"
      "ds_read_b128 a[120:123], v202 offset:19456\n"
      "v_add_f32 v193, v193, v133\n"
      "v_cvt_pk_bf16_f32 v179, v134, v135\n"
      "buffer_load_dwordx4 v217, %2, s44 offen offset:0 lds\n"
      "v_mfma_f32_32x32x16_bf16 v[144:159], a[84:87], a[20:23], v[144:159]\n"
      "ds_read_b128 a[124:127], v203 offset:19456\n"
      "ds_read_b64_tr_b16 a[152:153], v204 offset:35840\n"
      "v_add_f32 v192, v192, v134\n"
      "v_add_f32 v193, v193, v135\n"
      "v_mfma_f32_32x32x16_bf16 v[144:159], a[88:91], a[24:27], v[144:159]\n"
      "ds_read_b64_tr_b16 a[154:155], v204 offset:39936\n"
      "ds_read_b64_tr_b16 a[156:157], v205 offset:35840\n"
      "ds_read_b64_tr_b16 a[158:159], v205 offset:39936\n"
      "v_add_f32 v192, v192, v136\n"
      "v_mfma_f32_32x32x16_bf16 v[144:159], a[92:95], a[28:31], v[144:159]\n"
      "ds_read_b64_tr_b16 a[160:161], v204 offset:40960\n"
      "ds_read_b64_tr_b16 a[162:163], v204 offset:45056\n"
      "ds_read_b64_tr_b16 a[164:165], v205 offset:40960\n"
      "v_add_f32 v193, v193, v137\n"
      "v_mfma_f32_32x32x16_bf16 v[144:159], a[96:99], a[32:35], v[144:159]\n"
      "s_add_u32 m0, s41, 17280\n"
      "ds_read_b64_tr_b16 a[166:167], v205 offset:45056\n"
      "ds_read_b64_tr_b16 a[168:169], v204 offset:41984\n"
      "v_add_f32 v192, v192, v138\n"
      "buffer_load_dwordx4 v217, %2, s44 offen offset:128 lds\n"
      "v_mfma_f32_32x32x16_bf16 v[144:159], a[100:103], a[36:39], v[144:159]\n"
      "ds_read_b64_tr_b16 a[170:171], v204 offset:46080\n"
      "ds_read_b64_tr_b16 a[172:173], v205 offset:41984\n"
      "ds_read_b64_tr_b16 a[174:175], v205 offset:46080\n"
      "v_add_f32 v193, v193, v139\n"
      "s_waitcnt lgkmcnt(12)\n"
      "v_mfma_f32_32x32x16_bf16 v[144:159], a[104:107], a[40:43], v[144:159]\n"
      "ds_read_b64_tr_b16 a[176:177], v204 offset:43008\n"
      "ds_read_b64_tr_b16 a[178:179], v204 offset:47104\n"
      "ds_read_b64_tr_b16 a[180:181], v205 offset:43008\n"
      "v_add_f32 v192, v192, v140\n"
      "v_mfma_f32_32x32x16_bf16 v[144:159], a[108:111], a[44:47], v[144:159]\n"
      "ds_read_b64_tr_b16 a[182:183], v205 offset:47104\n"
      "ds_read_b64_tr_b16 a[184:185], v204 offset:44032\n"
      "ds_read_b64_tr_b16 a[186:187], v204 offset:48128\n"
      "v_add_f32 v193, v193, v141\n"
      "v_mfma_f32_32x32x16_bf16 v[144:159], a[112:115], a[48:51], v[144:159]\n"
      "s_add_u32 m0, s41, 18176\n"
      "ds_read_b64_tr_b16 a[188:189], v205 offset:44032\n"
      "ds_read_b64_tr_b16 a[190:191], v205 offset:48128\n"
      "buffer_load_dwordx4 v217, %2, s44 offen offset:256 lds\n"
      "v_mfma_f32_32x32x16_bf16 v[144:159], a[116:119], a[52:55], v[144:159]\n"
      "v_add_f32 v192, v192, v142\n"
      "v_add_f32 v193, v193, v143\n"
      "v_cvt_pk_bf16_f32 v180, v136, v137\n"
      "v_mfma_f32_32x32x16_bf16 v[144:159], a[120:123], a[56:59], v[144:159]\n"
      "s_add_u32 m0, s41, 19072\n"
      "v_cvt_pk_bf16_f32 v181, v138, v139\n"
      "buffer_load_dwordx4 v217, %2, s44 offen offset:384 lds\n"
      "s_add_u32 s44, s44, 0x4000\n"
      "s_and_b32 s44, s44, 0x7ffff\n"
      "v_mfma_f32_32x32x16_bf16 v[144:159], a[124:127], a[60:63], v[144:159]\n"
      "s_waitcnt vmcnt(12) lgkmcnt(0)\n"
      "s_barrier\n"
      "v_mfma_f32_32x32x16_bf16 v[0:15], a[128:131], v[176:179], 0\n"
      "s_add_u32 m0, s40, 16384\n"
      "ds_read_b128 a[64:67], v200 offset:32768\n"
      "buffer_load_dwordx4 v216, %1, s43 offen offset:0 lds\n"
      "v_mfma_f32_32x32x16_bf16 v[16:31], a[132:135], v[176:179], 0\n"
      "ds_read_b128 a[68:71], v201 offset:32768\n"
      "ds_read_b64_tr_b16 a[128:129], v204 offset:49152\n"
      "v_cvt_pk_bf16_f32 v182, v140, v141\n"
      "v_mfma_f32_32x32x16_bf16 v[32:47], a[136:139], v[176:179], 0\n"
      "ds_read_b128 a[72:75], v202 offset:32768\n"
      "ds_read_b64_tr_b16 a[130:131], v204 offset:53248\n"
      "v_cvt_pk_bf16_f32 v183, v142, v143\n"
      "global_load_dword v235, v231, s[70:71]\n"
      "v_mfma_f32_32x32x16_bf16 v[48:63], a[140:143], v[176:179], 0\n"
      "ds_read_b128 a[76:79], v203 offset:32768\n"
      "ds_read_b64_tr_b16 a[132:133], v205 offset:49152\n"
      "v_max3_f32 v194, v144, v145, v146\n"
      "v_max3_f32 v195, v147, v148, v149\n"
      "v_mfma_f32_32x32x16_bf16 v[64:79], a[144:147], v[176:179], 0\n"
      "s_add_u32 m0, s40, 17280\n"
      "ds_read_b128 a[80:83], v200 offset:33792\n"
      "v_max3_f32 v194, v194, v150, v151\n"
      "v_max3_f32 v195, v195, v152, v153\n"
      "buffer_load_dwordx4 v216, %1, s43 offen offset:128 lds\n"
      "v_mfma_f32_32x32x16_bf16 v[80:95], a[148:151], v[176:179], 0\n"
      "ds_read_b128 a[84:87], v201 offset:33792\n"
      "ds_read_b64_tr_b16 a[134:135], v205 offset:53248\n"
      "v_max3_f32 v194, v194, v154, v155\n"
      "v_max3_f32 v195, v195, v156, v157\n"
      "v_mfma_f32_32x32x16_bf16 v[96:111], a[152:155], v[176:179], 0\n"
      "ds_read_b128 a[88:91], v202 offset:33792\n"
      "ds_read_b64_tr_b16 a[136:137], v204 offset:50176\n"
      "v_max3_f32 v194, v194, v158, v159\n"
      "v_max_f32 v194, v194, v195\n"
      "v_mfma_f32_32x32x16_bf16 v[112:127], a[156:159], v[176:179], 0\n"
      "ds_read_b128 a[92:95], v203 offset:33792\n"
      "ds_read_b64_tr_b16 a[138:139], v204 offset:54272\n"
      "ds_read_b64_tr_b16 a[140:141], v205 offset:50176\n"
      "v_cmp_lt_f32 vcc, 0x41000000, v194\n"
      "v_mfma_f32_32x32x16_bf16 v[0:15], a[160:163], v[180:183], v[0:15]\n"
      "s_add_u32 m0, s40, 18176\n"
      "ds_read_b128 a[96:99], v200 offset:34816\n"
      "s_cbranch_vccnz LcA_RA1_%=\n"
      "LcA_RRA1_%=:\n"
      "buffer_load_dwordx4 v216, %1, s43 offen offset:256 lds\n"
      "v_mfma_f32_32x32x16_bf16 v[16:31], a[164:167], v[180:183], v[16:31]\n"
      "ds_read_b128 a[100:103], v201 offset:34816\n"
      "ds_read_b64_tr_b16 a[142:143], v205 offset:54272\n"
      "ds_read_b64_tr_b16 a[144:145], v204 offset:51200\n"
      "v_mfma_f32_32x32x16_bf16 v[32:47], a[168:171], v[180:183], v[32:47]\n"
      "v_exp_f32 v144, v144\n"
      "v_exp_f32 v145, v145\n"
      "v_exp_f32 v146, v146\n"
      "v_mfma_f32_32x32x16_bf16 v[48:63], a[172:175], v[180:183], v[48:63]\n"
      "v_exp_f32 v147, v147\n"
      "v_exp_f32 v148, v148\n"
      "v_exp_f32 v149, v149\n"
      "v_mfma_f32_32x32x16_bf16 v[64:79], a[176:179], v[180:183], v[64:79]\n"
      "v_exp_f32 v150, v150\n"
      "v_exp_f32 v151, v151\n"
      "v_exp_f32 v152, v152\n"
      "v_mfma_f32_32x32x16_bf16 v[80:95], a[180:183], v[180:183], v[80:95]\n"
      "v_exp_f32 v153, v153\n"
      "v_exp_f32 v154, v154\n"
      "v_exp_f32 v155, v155\n"
      "v_mfma_f32_32x32x16_bf16 v[96:111], a[184:187], v[180:183], v[96:111]\n"
      "v_exp_f32 v156, v156\n"
      "v_exp_f32 v157, v157\n"
      "v_exp_f32 v158, v158\n"
      "v_mfma_f32_32x32x16_bf16 v[112:127], a[188:191], v[180:183], v[112:127]\n"
      "v_exp_f32 v159, v159\n"
      "s_waitcnt lgkmcnt(2)\n"
      "s_cmp_lg_u32 s50, 0\n"
      "s_cbranch_scc1 LcA_RB1_%=\n"
      "LcA_RRB1_%=:\n"
      "v_mfma_f32_32x32x16_bf16 v[128:143], a[64:67], a[0:3], v[160:175]\n"
      "s_add_u32 m0, s40, 19072\n"
      "ds_read_b128 a[104:107], v202 offset:34816\n"
      "v_add_f32 v192, v192, v144\n"
      "v_add_f32 v193, v193, v145\n"
      "buffer_load_dwordx4 v216, %1, s43 offen offset:384 lds\n"
      "s_add_u32 s43, s43, 0x4000\n"
      "v_mfma_f32_32x32x16_bf16 v[128:143], a[68:71], a[4:7], v[128:143]\n"
      "ds_read_b128 a[108:111], v203 offset:34816\n"
      "ds_read_b64_tr_b16 a[146:147], v204 offset:55296\n"
      "v_add_f32 v192, v192, v146\n"
      "v_cvt_pk_bf16_f32 v176, v144, v145\n"
      "v_mfma_f32_32x32x16_bf16 v[128:143], a[72:75], a[8:11], v[128:143]\n"
      "ds_read_b128 a[112:115], v200 offset:35840\n"
      "ds_read_b64_tr_b16 a[148:149], v205 offset:51200\n"
      "v_add_f32 v193, v193, v147\n"
      "v_cvt_pk_bf16_f32 v177, v146, v147\n"
      "v_mfma_f32_32x32x16_bf16 v[128:143], a[76:79], a[12:15], v[128:143]\n"
      "ds_read_b128 a[116:119], v201 offset:35840\n"
      "ds_read_b64_tr_b16 a[150:151], v205 offset:55296\n"
      "v_add_f32 v192, v192, v148\n"
      "v_cvt_pk_bf16_f32 v178, v148, v149\n"
      "v_mfma_f32_32x32x16_bf16 v[128:143], a[80:83], a[16:19], v[128:143]\n"
      "s_add_u32 m0, s41, 32768\n"
      "ds_read_b128 a[120:123], v202 offset:35840\n"
      "v_add_f32 v193, v193, v149\n"
      "v_cvt_pk_bf16_f32 v179, v150, v151\n"
      "buffer_load_dwordx4 v217, %2, s44 offen offset:0 lds\n"
      "v_mfma_f32_32x32x16_bf16 v[128:143], a[84:87], a[20:23], v[128:143]\n"
      "ds_read_b128 a[124:127], v203 offset:35840\n"
      "ds_read_b64_tr_b16 a[152:153], v204 offset:52224\n"
      "v_add_f32 v192, v192, v150\n"
      "v_add_f32 v193, v193, v151\n"
      "v_mfma_f32_32x32x16_bf16 v[128:143], a[88:91], a[24:27], v[128:143]\n"
      "ds_read_b64_tr_b16 a[154:155], v204 offset:56320\n"
      "ds_read_b64_tr_b16 a[156:157], v205 offset:52224\n"
      "ds_read_b64_tr_b16 a[158:159], v205 offset:56320\n"
      "v_add_f32 v192, v192, v152\n"
      "v_mfma_f32_32x32x16_bf16 v[128:143], a[92:95], a[28:31], v[128:143]\n"
      "ds_read_b64_tr_b16 a[160:161], v204 offset:57344\n"
      "ds_read_b64_tr_b16 a[162:163], v204 offset:61440\n"
      "ds_read_b64_tr_b16 a[164:165], v205 offset:57344\n"
      "v_add_f32 v193, v193, v153\n"
      "v_mfma_f32_32x32x16_bf16 v[128:143], a[96:99], a[32:35], v[128:143]\n"
      "s_add_u32 m0, s41, 33664\n"
      "ds_read_b64_tr_b16 a[166:167], v205 offset:61440\n"
      "ds_read_b64_tr_b16 a[168:169], v204 offset:58368\n"
      "v_add_f32 v192, v192, v154\n"
      "buffer_load_dwordx4 v217, %2, s44 offen offset:128 lds\n"
      "v_mfma_f32_32x32x16_bf16 v[128:143], a[100:103], a[36:39], v[128:143]\n"
      "ds_read_b64_tr_b16 a[170:171], v204 offset:62464\n"
      "ds_read_b64_tr_b16 a[172:173], v205 offset:58368\n"
      "ds_read_b64_tr_b16 a[174:175], v205 offset:62464\n"
      "v_add_f32 v193, v193, v155\n"
      "s_waitcnt lgkmcnt(12)\n"
      "v_mfma_f32_32x32x16_bf16 v[128:143], a[104:107], a[40:43], v[128:143]\n"
      "ds_read_b64_tr_b16 a[176:177], v204 offset:59392\n"
      "ds_read_b64_tr_b16 a[178:179], v204 offset:63488\n"
      "ds_read_b64_tr_b16 a[180:181], v205 offset:59392\n"
      "v_add_f32 v192, v192, v156\n"
      "v_mfma_f32_32x32x16_bf16 v[128:143], a[108:111], a[44:47], v[128:143]\n"
      "ds_read_b64_tr_b16 a[182:183], v205 offset:63488\n"
      "ds_read_b64_tr_b16 a[184:185], v204 offset:60416\n"
      "ds_read_b64_tr_b16 a[186:187], v204 offset:64512\n"
      "v_add_f32 v193, v193, v157\n"
      "v_mfma_f32_32x32x16_bf16 v[128:143], a[112:115], a[48:51], v[128:143]\n"
      "s_add_u32 m0, s41, 34560\n"
      "ds_read_b64_tr_b16 a[188:189], v205 offset:60416\n"
      "ds_read_b64_tr_b16 a[190:191], v205 offset:64512\n"
      "buffer_load_dwordx4 v217, %2, s44 offen offset:256 lds\n"
      "v_mfma_f32_32x32x16_bf16 v[128:143], a[116:119], a[52:55], v[128:143]\n"
      "v_add_f32 v192, v192, v158\n"
      "v_add_f32 v193, v193, v159\n"
      "v_cvt_pk_bf16_f32 v180, v152, v153\n"
      "v_mfma_f32_32x32x16_bf16 v[128:143], a[120:123], a[56:59], v[128:143]\n"
      "s_add_u32 m0, s41, 35456\n"
      "v_cvt_pk_bf16_f32 v181, v154, v155\n"
      "buffer_load_dwordx4 v217, %2, s44 offen offset:384 lds\n"
      "s_add_u32 s44, s44, 0x4000\n"
      "v_mfma_f32_32x32x16_bf16 v[128:143], a[124:127], a[60:63], v[128:143]\n"
      "s_waitcnt vmcnt(17) lgkmcnt(0)\n"
      "s_barrier\n"
      "v_mfma_f32_32x32x16_bf16 v[0:15], a[128:131], v[176:179], v[0:15]\n"
      "s_add_u32 m0, s40, 32768\n"
      "ds_read_b128 a[64:67], v200 offset:49152\n"
      "buffer_load_dwordx4 v216, %1, s43 offen offset:0 lds\n"
      "v_mfma_f32_32x32x16_bf16 v[16:31], a[132:135], v[176:179], v[16:31]\n"
      "ds_read_b128 a[68:71], v201 offset:49152\n"
      "ds_read_b64_tr_b16 a[128:129], v204 offset:0\n"
      "v_cvt_pk_bf16_f32 v182, v156, v157\n"
      "v_mfma_f32_32x32x16_bf16 v[32:47], a[136:139], v[176:179], v[32:47]\n"
      "ds_read_b128 a[72:75], v202 offset:49152\n"
      "ds_read_b64_tr_b16 a[130:131], v204 offset:4096\n"
      "v_cvt_pk_bf16_f32 v183, v158, v159\n"
      "v_mfma_f32_32x32x16_bf16 v[48:63], a[140:143], v[176:179], v[48:63]\n"
      "ds_read_b128 a[76:79], v203 offset:49152\n"
      "ds_read_b64_tr_b16 a[132:133], v205 offset:0\n"
      "v_max3_f32 v194, v128, v129, v130\n"
      "v_max3_f32 v195, v131, v132, v133\n"
      "v_mfma_f32_32x32x16_bf16 v[64:79], a[144:147], v[176:179], v[64:79]\n"
      "s_add_u32 m0, s40, 33664\n"
      "ds_read_b128 a[80:83], v200 offset:50176\n"
      "v_max3_f32 v194, v194, v134, v135\n"
      "v_max3_f32 v195, v195, v136, v137\n"
      "buffer_load_dwordx4 v216, %1, s43 offen offset:128 lds\n"
      "v_mfma_f32_32x32x16_bf16 v[80:95], a[148:151], v[176:179], v[80:95]\n"
      "ds_read_b128 a[84:87], v201 offset:50176\n"
      "ds_read_b64_tr_b16 a[134:135], v205 offset:4096\n"
      "v_max3_f32 v194, v194, v138, v139\n"
      "v_max3_f32 v195, v195, v140, v141\n"
      "v_mfma_f32_32x32x16_bf16 v[96:111], a[152:155], v[176:179], v[96:111]\n"
      "ds_read_b128 a[88:91], v202 offset:50176\n"
      "ds_read_b64_tr_b16 a[136:137], v204 offset:1024\n"
      "v_max3_f32 v194, v194, v142, v143\n"
      "v_max_f32 v194, v194, v195\n"
      "v_mfma_f32_32x32x16_bf16 v[112:127], a[156:159], v[176:179], v[112:127]\n"
      "ds_read_b128 a[92:95], v203 offset:50176\n"
      "ds_read_b64_tr_b16 a[138:139], v204 offset:5120\n"
      "ds_read_b64_tr_b16 a[140:141], v205 offset:1024\n"
      "v_cmp_lt_f32 vcc, 0x41000000, v194\n"
      "v_mfma_f32_32x32x16_bf16 v[0:15], a[160:163], v[180:183], v[0:15]\n"
      "s_add_u32 m0, s40, 34560\n"
      "ds_read_b128 a[96:99], v200 offset:51200\n"
      "s_cbranch_vccnz LcA_RA2_%=\n"
      "LcA_RRA2_%=:\n"
      "buffer_load_dwordx4 v216, %1, s43 offen offset:256 lds\n"
      "v_mfma_f32_32x32x16_bf16 v[16:31], a[164:167], v[180:183], v[16:31]\n"
      "ds_read_b128 a[100:103], v201 offset:51200\n"
      "ds_read_b64_tr_b16 a[142:143], v205 offset:5120\n"
      "ds_read_b64_tr_b16 a[144:145], v204 offset:2048\n"
      "v_mfma_f32_32x32x16_bf16 v[32:47], a[168:171], v[180:183], v[32:47]\n"
      "v_exp_f32 v128, v128\n"
      "v_exp_f32 v129, v129\n"
      "v_exp_f32 v130, v130\n"
      "v_mfma_f32_32x32x16_bf16 v[48:63], a[172:175], v[180:183], v[48:63]\n"
      "v_exp_f32 v131, v131\n"
      "v_exp_f32 v132, v132\n"
      "v_exp_f32 v133, v133\n"
      "v_mfma_f32_32x32x16_bf16 v[64:79], a[176:179], v[180:183], v[64:79]\n"
      "v_exp_f32 v134, v134\n"
      "v_exp_f32 v135, v135\n"
      "v_exp_f32 v136, v136\n"
      "v_mfma_f32_32x32x16_bf16 v[80:95], a[180:183], v[180:183], v[80:95]\n"
      "v_exp_f32 v137, v137\n"
      "v_exp_f32 v138, v138\n"
      "v_exp_f32 v139, v139\n"
      "v_mfma_f32_32x32x16_bf16 v[96:111], a[184:187], v[180:183], v[96:111]\n"
      "v_exp_f32 v140, v140\n"
      "v_exp_f32 v141, v141\n"
      "v_exp_f32 v142, v142\n"
      "v_mfma_f32_32x32x16_bf16 v[112:127], a[188:191], v[180:183], v[112:127]\n"
      "v_exp_f32 v143, v143\n"
      "s_waitcnt lgkmcnt(2)\n"
      "s_cmp_lg_u32 s50, 0\n"
      "s_cbranch_scc1 LcA_RB2_%=\n"
      "LcA_RRB2_%=:\n"
      "v_mfma_f32_32x32x16_bf16 v[144:159], a[64:67], a[0:3], v[160:175]\n"
      "s_add_u32 m0, s40, 35456\n"
      "ds_read_b128 a[104:107], v202 offset:51200\n"
      "v_add_f32 v192, v192, v128\n"
      "v_add_f32 v193, v193, v129\n"
      "buffer_load_dwordx4 v216, %1, s43 offen offset:384 lds\n"
      "s_add_u32 s43, s43, 0x4000\n"
      "v_mfma_f32_32x32x16_bf16 v[144:159], a[68:71], a[4:7], v[144:159]\n"
      "ds_read_b128 a[108:111], v203 offset:51200\n"
      "ds_read_b64_tr_b16 a[146:147], v204 offset:6144\n"
      "v_add_f32 v192, v192, v130\n"
      "v_cvt_pk_bf16_f32 v176, v128, v129\n"
      "v_mfma_f32_32x32x16_bf16 v[144:159], a[72:75], a[8:11], v[144:159]\n"
      "ds_read_b128 a[112:115], v200 offset:52224\n"
      "ds_read_b64_tr_b16 a[148:149], v205 offset:2048\n"
      "v_add_f32 v193, v193, v131\n"
      "v_cvt_pk_bf16_f32 v177, v130, v131\n"
      "v_mfma_f32_32x32x16_bf16 v[144:159], a[76:79], a[12:15], v[144:159]\n"
      "ds_read_b128 a[116:119], v201 offset:52224\n"
      "ds_read_b64_tr_b16 a[150:151], v205 offset:6144\n"
      "v_add_f32 v192, v192, v132\n"
      "v_cvt_pk_bf16_f32 v178, v132, v133\n"
      "v_mfma_f32_32x32x16_bf16 v[144:159], a[80:83], a[16:19], v[144:159]\n"
      "s_add_u32 m0, s41, 49152\n"
      "ds_read_b128 a[120:123], v202 offset:52224\n"
      "v_add_f32 v193, v193, v133\n"
      "v_cvt_pk_bf16_f32 v179, v134, v135\n"
      "buffer_load_dwordx4 v217, %2, s44 offen offset:0 lds\n"
      "v_mfma_f32_32x32x16_bf16 v[144:159], a[84:87], a[20:23], v[144:159]\n"
      "ds_read_b128 a[124:127], v203 offset:52224\n"
      "ds_read_b64_tr_b16 a[152:153], v204 offset:3072\n"
      "v_add_f32 v192, v192, v134\n"
      "v_add_f32 v193, v193, v135\n"
      "v_mfma_f32_32x32x16_bf16 v[144:159], a[88:91], a[24:27], v[144:159]\n"
      "ds_read_b64_tr_b16 a[154:155], v204 offset:7168\n"
      "ds_read_b64_tr_b16 a[156:157], v205 offset:3072\n"
      "ds_read_b64_tr_b16 a[158:159], v205 offset:7168\n"
      "v_add_f32 v192, v192, v136\n"
      "v_mfma_f32_32x32x16_bf16 v[144:159], a[92:95], a[28:31], v[144:159]\n"
      "ds_read_b64_tr_b16 a[160:161], v204 offset:8192\n"
      "ds_read_b64_tr_b16 a[162:163], v204 offset:12288\n"
      "ds_read_b64_tr_b16 a[164:165], v205 offset:8192\n"
      "v_add_f32 v193, v193, v137\n"
      "v_mfma_f32_32x32x16_bf16 v[144:159], a[96:99], a[32:35], v[144:159]\n"
      "s_add_u32 m0, s41, 50048\n"
      "ds_read_b64_tr_b16 a[166:167], v205 offset:12288\n"
      "ds_read_b64_tr_b16 a[168:169], v204 offset:9216\n"
      "v_add_f32 v192, v192, v138\n"
      "buffer_load_dwordx4 v217, %2, s44 offen offset:128 lds\n"
      "v_mfma_f32_32x32x16_bf16 v[144:159], a[100:103], a[36:39], v[144:159]\n"
      "ds_read_b64_tr_b16 a[170:171], v204 offset:13312\n"
      "ds_read_b64_tr_b16 a[172:173], v205 offset:9216\n"
      "ds_read_b64_tr_b16 a[174:175], v205 offset:13312\n"
      "v_add_f32 v193, v193, v139\n"
      "s_waitcnt lgkmcnt(12)\n"
      "v_mfma_f32_32x32x16_bf16 v[144:159], a[104:107], a[40:43], v[144:159]\n"
      "ds_read_b64_tr_b16 a[176:177], v204 offset:10240\n"
      "ds_read_b64_tr_b16 a[178:179], v204 offset:14336\n"
      "ds_read_b64_tr_b16 a[180:181], v205 offset:10240\n"
      "v_add_f32 v192, v192, v140\n"
      "v_mfma_f32_32x32x16_bf16 v[144:159], a[108:111], a[44:47], v[144:159]\n"
      "ds_read_b64_tr_b16 a[182:183], v205 offset:14336\n"
      "ds_read_b64_tr_b16 a[184:185], v204 offset:11264\n"
      "ds_read_b64_tr_b16 a[186:187], v204 offset:15360\n"
      "v_add_f32 v193, v193, v141\n"
      "v_mfma_f32_32x32x16_bf16 v[144:159], a[112:115], a[48:51], v[144:159]\n"
      "s_add_u32 m0, s41, 50944\n"
      "ds_read_b64_tr_b16 a[188:189], v205 offset:11264\n"
      "ds_read_b64_tr_b16 a[190:191], v205 offset:15360\n"
      "buffer_load_dwordx4 v217, %2, s44 offen offset:256 lds\n"
      "v_mfma_f32_32x32x16_bf16 v[144:159], a[116:119], a[52:55], v[144:159]\n"
      "v_add_f32 v192, v192, v142\n"
      "v_add_f32 v193, v193, v143\n"
      "v_cvt_pk_bf16_f32 v180, v136, v137\n"
      "v_mfma_f32_32x32x16_bf16 v[144:159], a[120:123], a[56:59], v[144:159]\n"
      "s_add_u32 m0, s41, 51840\n"
      "v_cvt_pk_bf16_f32 v181, v138, v139\n"
      "buffer_load_dwordx4 v217, %2, s44 offen offset:384 lds\n"
      "s_add_u32 s44, s44, 0x4000\n"
      "v_mfma_f32_32x32x16_bf16 v[144:159], a[124:127], a[60:63], v[144:159]\n"
      "s_waitcnt vmcnt(17) lgkmcnt(0)\n"
      "s_barrier\n"
      "v_mfma_f32_32x32x16_bf16 v[0:15], a[128:131], v[176:179], v[0:15]\n"
      "s_add_u32 m0, s40, 49152\n"
      "ds_read_b128 a[64:67], v200 offset:0\n"
      "buffer_load_dwordx4 v216, %1, s43 offen offset:0 lds\n"
      "v_mfma_f32_32x32x16_bf16 v[16:31], a[132:135], v[176:179], v[16:31]\n"
      "ds_read_b128 a[68:71], v201 offset:0\n"
      "ds_read_b64_tr_b16 a[128:129], v204 offset:16384\n"
      "v_cvt_pk_bf16_f32 v182, v140, v141\n"
      "v_mfma_f32_32x32x16_bf16 v[32:47], a[136:139], v[176:179], v[32:47]\n"
      "ds_read_b128 a[72:75], v202 offset:0\n"
      "ds_read_b64_tr_b16 a[130:131], v204 offset:20480\n"
      "v_cvt_pk_bf16_f32 v183, v142, v143\n"
      "v_mfma_f32_32x32x16_bf16 v[48:63], a[140:143], v[176:179], v[48:63]\n"
      "ds_read_b128 a[76:79], v203 offset:0\n"
      "ds_read_b64_tr_b16 a[132:133], v205 offset:16384\n"
      "v_max3_f32 v194, v144, v145, v146\n"
      "v_max3_f32 v195, v147, v148, v149\n"
      "v_mfma_f32_32x32x16_bf16 v[64:79], a[144:147], v[176:179], v[64:79]\n"
      "s_add_u32 m0, s40, 50048\n"
      "ds_read_b128 a[80:83], v200 offset:1024\n"
      "v_max3_f32 v194, v194, v150, v151\n"
      "v_max3_f32 v195, v195, v152, v153\n"
      "buffer_load_dwordx4 v216, %1, s43 offen offset:128 lds\n"
      "v_mfma_f32_32x32x16_bf16 v[80:95], a[148:151], v[176:179], v[80:95]\n"
      "ds_read_b128 a[84:87], v201 offset:1024\n"
      "ds_read_b64_tr_b16 a[134:135], v205 offset:20480\n"
      "v_max3_f32 v194, v194, v154, v155\n"
      "v_max3_f32 v195, v195, v156, v157\n"
      "v_mfma_f32_32x32x16_bf16 v[96:111], a[152:155], v[176:179], v[96:111]\n"
      "ds_read_b128 a[88:91], v202 offset:1024\n"
      "ds_read_b64_tr_b16 a[136:137], v204 offset:17408\n"
      "v_max3_f32 v194, v194, v158, v159\n"
      "v_max_f32 v194, v194, v195\n"
      "v_mfma_f32_32x32x16_bf16 v[112:127], a[156:159], v[176:179], v[112:127]\n"
      "ds_read_b128 a[92:95], v203 offset:1024\n"
      "ds_read_b64_tr_b16 a[138:139], v204 offset:21504\n"
      "ds_read_b64_tr_b16 a[140:141], v205 offset:17408\n"
      "v_cmp_lt_f32 vcc, 0x41000000, v194\n"
      "v_mfma_f32_32x32x16_bf16 v[0:15], a[160:163], v[180:183], v[0:15]\n"
      "s_add_u32 m0, s40, 50944\n"
      "ds_read_b128 a[96:99], v200 offset:2048\n"
      "s_cbranch_vccnz LcA_RA3_%=\n"
      "LcA_RRA3_%=:\n"
      "buffer_load_dwordx4 v216, %1, s43 offen offset:256 lds\n"
      "v_mfma_f32_32x32x16_bf16 v[16:31], a[164:167], v[180:183], v[16:31]\n"
      "ds_read_b128 a[100:103], v201 offset:2048\n"
      "ds_read_b64_tr_b16 a[142:143], v205 offset:21504\n"
      "ds_read_b64_tr_b16 a[144:145], v204 offset:18432\n"
      "v_mfma_f32_32x32x16_bf16 v[32:47], a[168:171], v[180:183], v[32:47]\n"
      "v_exp_f32 v144, v144\n"
      "v_exp_f32 v145, v145\n"
      "v_exp_f32 v146, v146\n"
      "v_mfma_f32_32x32x16_bf16 v[48:63], a[172:175], v[180:183], v[48:63]\n"
      "v_exp_f32 v147, v147\n"
      "v_exp_f32 v148, v148\n"
      "v_exp_f32 v149, v149\n"
      "v_mfma_f32_32x32x16_bf16 v[64:79], a[176:179], v[180:183], v[64:79]\n"
      "v_exp_f32 v150, v150\n"
      "v_exp_f32 v151, v151\n"
      "v_exp_f32 v152, v152\n"
      "v_mfma_f32_32x32x16_bf16 v[80:95], a[180:183], v[180:183], v[80:95]\n"
      "v_exp_f32 v153, v153\n"
      "v_exp_f32 v154, v154\n"
      "v_exp_f32 v155, v155\n"
      "v_mfma_f32_32x32x16_bf16 v[96:111], a[184:187], v[180:183], v[96:111]\n"
      "v_exp_f32 v156, v156\n"
      "v_exp_f32 v157, v157\n"
      "v_exp_f32 v158, v158\n"
      "v_mfma_f32_32x32x16_bf16 v[112:127], a[188:191], v[180:183], v[112:127]\n"
      "v_exp_f32 v159, v159\n"
      "s_waitcnt lgkmcnt(2)\n"
      "s_cmp_lg_u32 s50, 0\n"
      "s_cbranch_scc1 LcA_RB3_%=\n"
      "LcA_RRB3_%=:\n"
      "s_mov_b32 s51, 6\n"
      "LcA_loop_%=:\n"
      "v_mfma_f32_32x32x16_bf16 v[128:143], a[64:67], a[0:3], v[160:175]\n"
      "s_add_u32 m0, s40, 51840\n"
      "ds_read_b128 a[104:107], v202 offset:2048\n"
      "v_add_f32 v192, v192, v144\n"
      "v_add_f32 v193, v193, v145\n"
      "buffer_load_dwordx4 v216, %1, s43 offen offset:384 lds\n"
      "s_add_u32 s43, s43, 0x4000\n"
      "s_and_b32 s43, s43, 0x7ffff\n"
      "v_mfma_f32_32x32x16_bf16 v[128:143], a[68:71], a[4:7], v[128:143]\n"
      "ds_read_b128 a[108:111], v203 offset:2048\n"
      "ds_read_b64_tr_b16 a[146:147], v204 offset:22528\n"
      "v_add_f32 v192, v192, v146\n"
      "v_cvt_pk_bf16_f32 v176, v144, v145\n"
      "v_mfma_f32_32x32x16_bf16 v[128:143], a[72:75], a[8:11], v[128:143]\n"
      "ds_read_b128 a[112:115], v200 offset:3072\n"
      "ds_read_b64_tr_b16 a[148:149], v205 offset:18432\n"
      "v_add_f32 v193, v193, v147\n"
      "v_cvt_pk_bf16_f32 v177, v146, v147\n"
      "v_mfma_f32_32x32x16_bf16 v[128:143], a[76:79], a[12:15], v[128:143]\n"
      "ds_read_b128 a[116:119], v201 offset:3072\n"
      "ds_read_b64_tr_b16 a[150:151], v205 offset:22528\n"
      "v_add_f32 v192, v192, v148\n"
      "v_cvt_pk_bf16_f32 v178, v148, v149\n"
      "v_mfma_f32_32x32x16_bf16 v[128:143], a[80:83], a[16:19], v[128:143]\n"
      "s_add_u32 m0, s41, 0\n"
      "ds_read_b128 a[120:123], v202 offset:3072\n"
      "v_add_f32 v193, v193, v149\n"
      "v_cvt_pk_bf16_f32 v179, v150, v151\n"
      "buffer_load_dwordx4 v217, %2, s44 offen offset:0 lds\n"
      "v_mfma_f32_32x32x16_bf16 v[128:143], a[84:87], a[20:23], v[128:143]\n"
      "ds_read_b128 a[124:127], v203 offset:3072\n"
      "ds_read_b64_tr_b16 a[152:153], v204 offset:19456\n"
      "v_add_f32 v192, v192, v150\n"
      "v_add_f32 v193, v193, v151\n"
      "v_mfma_f32_32x32x16_bf16 v[128:143], a[88:91], a[24:27], v[128:143]\n"
      "ds_read_b64_tr_b16 a[154:155], v204 offset:23552\n"
      "ds_read_b64_tr_b16 a[156:157], v205 offset:19456\n"
      "ds_read_b64_tr_b16 a[158:159], v205 offset:23552\n"
      "v_add_f32 v192, v192, v152\n"
      "v_mfma_f32_32x32x16_bf16 v[128:143], a[92:95], a[28:31], v[128:143]\n"
      "ds_read_b64_tr_b16 a[160:161], v204 offset:24576\n"
      "ds_read_b64_tr_b16 a[162:163], v204 offset:28672\n"
      "ds_read_b64_tr_b16 a[164:165], v205 offset:24576\n"
      "v_add_f32 v193, v193, v153\n"
      "v_mfma_f32_32x32x16_bf16 v[128:143], a[96:99], a[32:35], v[128:143]\n"
      "s_add_u32 m0, s41, 896\n"
      "ds_read_b64_tr_b16 a[166:167], v205 offset:28672\n"
      "ds_read_b64_tr_b16 a[168:169], v204 offset:25600\n"
      "v_add_f32 v192, v192, v154\n"
      "buffer_load_dwordx4 v217, %2, s44 offen offset:128 lds\n"
      "v_mfma_f32_32x32x16_bf16 v[128:143], a[100:103], a[36:39], v[128:143]\n"
      "ds_read_b64_tr_b16 a[170:171], v204 offset:29696\n"
      "ds_read_b64_tr_b16 a[172:173], v205 offset:25600\n"
      "ds_read_b64_tr_b16 a[174:175], v205 offset:29696\n"
      "v_add_f32 v193, v193, v155\n"
      "s_waitcnt lgkmcnt(12)\n"
      "v_mfma_f32_32x32x16_bf16 v[128:143], a[104:107], a[40:43], v[128:143]\n"
      "ds_read_b64_tr_b16 a[176:177], v204 offset:26624\n"
      "ds_read_b64_tr_b16 a[178:179], v204 offset:30720\n"
      "ds_read_b64_tr_b16 a[180:181], v205 offset:26624\n"
      "v_add_f32 v192, v192, v156\n"
      "v_mfma_f32_32x32x16_bf16 v[128:143], a[108:111], a[44:47], v[128:143]\n"
      "ds_read_b64_tr_b16 a[182:183], v205 offset:30720\n"
      "ds_read_b64_tr_b16 a[184:185], v204 offset:27648\n"
      "ds_read_b64_tr_b16 a[186:187], v204 offset:31744\n"
      "v_add_f32 v193, v193, v157\n"
      "v_mfma_f32_32x32x16_bf16 v[128:143], a[112:115], a[48:51], v[128:143]\n"
      "s_add_u32 m0, s41, 1792\n"
      "ds_read_b64_tr_b16 a[188:189], v205 offset:27648\n"
      "ds_read_b64_tr_b16 a[190:191], v205 offset:31744\n"
      "buffer_load_dwordx4 v217, %2, s44 offen offset:256 lds\n"
      "v_mfma_f32_32x32x16_bf16 v[128:143], a[116:119], a[52:55], v[128:143]\n"
      "v_add_f32 v192, v192, v158\n"
      "v_add_f32 v193, v193, v159\n"
      "v_cvt_pk_bf16_f32 v180, v152, v153\n"
      "v_mfma_f32_32x32x16_bf16 v[128:143], a[120:123], a[56:59], v[128:143]\n"
      "s_add_u32 m0, s41, 2688\n"
      "v_cvt_pk_bf16_f32 v181, v154, v155\n"
      "buffer_load_dwordx4 v217, %2, s44 offen offset:384 lds\n"
      "s_add_u32 s44, s44, 0x4000\n"
      "v_mfma_f32_32x32x16_bf16 v[128:143], a[124:127], a[60:63], v[128:143]\n"
      "s_waitcnt vmcnt(16) lgkmcnt(0)\n"
      "s_barrier\n"
      "v_mfma_f32_32x32x16_bf16 v[0:15], a[128:131], v[176:179], v[0:15]\n"
      "s_add_u32 m0, s40, 0\n"
      "ds_read_b128 a[64:67], v200 offset:16384\n"
      "buffer_load_dwordx4 v216, %1, s43 offen offset:0 lds\n"
      "v_mfma_f32_32x32x16_bf16 v[16:31], a[132:135], v[176:179], v[16:31]\n"
      "ds_read_b128 a[68:71], v201 offset:16384\n"
      "ds_read_b64_tr_b16 a[128:129], v204 offset:32768\n"
      "v_cvt_pk_bf16_f32 v182, v156, v157\n"
      "v_mfma_f32_32x32x16_bf16 v[32:47], a[136:139], v[176:179], v[32:47]\n"
      "ds_read_b128 a[72:75], v202 offset:16384\n"
      "ds_read_b64_tr_b16 a[130:131], v204 offset:36864\n"
      "v_cvt_pk_bf16_f32 v183, v158, v159\n"
      "v_mfma_f32_32x32x16_bf16 v[48:63], a[140:143], v[176:179], v[48:63]\n"
      "ds_read_b128 a[76:79], v203 offset:16384\n"
      "ds_read_b64_tr_b16 a[132:133], v205 offset:32768\n"
      "v_max3_f32 v194, v128, v129, v130\n"
      "v_max3_f32 v195, v131, v132, v133\n"
      "v_mfma_f32_32x32x16_bf16 v[64:79], a[144:147], v[176:179], v[64:79]\n"
      "s_add_u32 m0, s40, 896\n"
      "ds_read_b128 a[80:83], v200 offset:17408\n"
      "v_max3_f32 v194, v194, v134, v135\n"
      "v_max3_f32 v195, v195, v136, v137\n"
      "buffer_load_dwordx4 v216, %1, s43 offen offset:128 lds\n"
      "v_mfma_f32_32x32x16_bf16 v[80:95], a[148:151], v[176:179], v[80:95]\n"
      "ds_read_b128 a[84:87], v201 offset:17408\n"
      "ds_read_b64_tr_b16 a[134:135], v205 offset:36864\n"
      "v_max3_f32 v194, v194, v138, v139\n"
      "v_max3_f32 v195, v195, v140, v141\n"
      "v_mfma_f32_32x32x16_bf16 v[96:111], a[152:155], v[176:179], v[96:111]\n"
      "ds_read_b128 a[88:91], v202 offset:17408\n"
      "ds_read_b64_tr_b16 a[136:137], v204 offset:33792\n"
      "v_max3_f32 v194, v194, v142, v143\n"
      "v_max_f32 v194, v194, v195\n"
      "v_mfma_f32_32x32x16_bf16 v[112:127], a[156:159], v[176:179], v[112:127]\n"
      "ds_read_b128 a[92:95], v203 offset:17408\n"
      "ds_read_b64_tr_b16 a[138:139], v204 offset:37888\n"
      "ds_read_b64_tr_b16 a[140:141], v205 offset:33792\n"
      "v_cmp_lt_f32 vcc, 0x41000000, v194\n"
      "v_mfma_f32_32x32x16_bf16 v[0:15], a[160:163], v[180:183], v[0:15]\n"
      "s_add_u32 m0, s40, 1792\n"
      "ds_read_b128 a[96:99], v200 offset:18432\n"
      "s_cbranch_vccnz LcA_RA4_%=\n"
      "LcA_RRA4_%=:\n"
      "buffer_load_dwordx4 v216, %1, s43 offen offset:256 lds\n"
      "v_mfma_f32_32x32x16_bf16 v[16:31], a[164:167], v[180:183], v[16:31]\n"
      "ds_read_b128 a[100:103], v201 offset:18432\n"
      "ds_read_b64_tr_b16 a[142:143], v205 offset:37888\n"
      "ds_read_b64_tr_b16 a[144:145], v204 offset:34816\n"
      "v_mfma_f32_32x32x16_bf16 v[32:47], a[168:171], v[180:183], v[32:47]\n"
      "v_exp_f32 v128, v128\n"
      "v_exp_f32 v129, v129\n"
      "v_exp_f32 v130, v130\n"
      "v_mfma_f32_32x32x16_bf16 v[48:63], a[172:175], v[180:183], v[48:63]\n"
      "v_exp_f32 v131, v131\n"
      "v_exp_f32 v132, v132\n"
      "v_exp_f32 v133, v133\n"
      "v_mfma_f32_32x32x16_bf16 v[64:79], a[176:179], v[180:183], v[64:79]\n"
      "v_exp_f32 v134, v134\n"
      "v_exp_f32 v135, v135\n"
      "v_exp_f32 v136, v136\n"
      "v_mfma_f32_32x32x16_bf16 v[80:95], a[180:183], v[180:183], v[80:95]\n"
      "v_exp_f32 v137, v137\n"
      "v_exp_f32 v138, v138\n"
      "v_exp_f32 v139, v139\n"
      "v_mfma_f32_32x32x16_bf16 v[96:111], a[184:187], v[180:183], v[96:111]\n"
      "v_exp_f32 v140, v140\n"
      "v_exp_f32 v141, v141\n"
      "v_exp_f32 v142, v142\n"
      "v_mfma_f32_32x32x16_bf16 v[112:127], a[188:191], v[180:183], v[112:127]\n"
      "v_exp_f32 v143, v143\n"
      "s_waitcnt lgkmcnt(2)\n"
      "s_cmp_lg_u32 s50, 0\n"
      "s_cbranch_scc1 LcA_RB4_%=\n"
      "LcA_RRB4_%=:\n"
      "v_mfma_f32_32x32x16_bf16 v[144:159], a[64:67], a[0:3], v[160:175]\n"
      "s_add_u32 m0, s40, 2688\n"
      "ds_read_b128 a[104:107], v202 offset:18432\n"
      "v_add_f32 v192, v192, v128\n"
      "v_add_f32 v193, v193, v129\n"
      "buffer_load_dwordx4 v216, %1, s43 offen offset:384 lds\n"
      "s_add_u32 s43, s43, 0x4000\n"
      "v_mfma_f32_32x32x16_bf16 v[144:159], a[68:71], a[4:7], v[144:159]\n"
      "ds_read_b128 a[108:111], v203 offset:18432\n"
      "ds_read_b64_tr_b16 a[146:147], v204 offset:38912\n"
      "v_add_f32 v192, v192, v130\n"
      "v_cvt_pk_bf16_f32 v176, v128, v129\n"
      "v_mfma_f32_32x32x16_bf16 v[144:159], a[72:75], a[8:11], v[144:159]\n"
      "ds_read_b128 a[112:115], v200 offset:19456\n"
      "ds_read_b64_tr_b16 a[148:149], v205 offset:34816\n"
      "v_add_f32 v193, v193, v131\n"
      "v_cvt_pk_bf16_f32 v177, v130, v131\n"
      "v_mfma_f32_32x32x16_bf16 v[144:159], a[76:79], a[12:15], v[144:159]\n"
      "ds_read_b128 a[116:119], v201 offset:19456\n"
      "ds_read_b64_tr_b16 a[150:151], v205 offset:38912\n"
      "v_add_f32 v192, v192, v132\n"
      "v_cvt_pk_bf16_f32 v178, v132, v133\n"
      "v_mfma_f32_32x32x16_bf16 v[144:159], a[80:83], a[16:19], v[144:159]\n"
      "s_add_u32 m0, s41, 16384\n"
      "ds_read_b128 a[120:123], v202 offset:19456\n"
      "v_add_f32 v193, v193, v133\n"
      "v_cvt_pk_bf16_f32 v179, v134, v135\n"
      "buffer_load_dwordx4 v217, %2, s44 offen offset:0 lds\n"
      "v_mfma_f32_32x32x16_bf16 v[144:159], a[84:87], a[20:23], v[144:159]\n"
      "ds_read_b128 a[124:127], v203 offset:19456\n"
      "ds_read_b64_tr_b16 a[152:153], v204 offset:35840\n"
      "v_add_f32 v192, v192, v134\n"
      "v_add_f32 v193, v193, v135\n"
      "v_mfma_f32_32x32x16_bf16 v[144:159], a[88:91], a[24:27], v[144:159]\n"
      "ds_read_b64_tr_b16 a[154:155], v204 offset:39936\n"
      "ds_read_b64_tr_b16 a[156:157], v205 offset:35840\n"
      "ds_read_b64_tr_b16 a[158:159], v205 offset:39936\n"
      "v_add_f32 v192, v192, v136\n"
      "v_mfma_f32_32x32x16_bf16 v[144:159], a[92:95], a[28:31], v[144:159]\n"
      "ds_read_b64_tr_b16 a[160:161], v204 offset:40960\n"
      "ds_read_b64_tr_b16 a[162:163], v204 offset:45056\n"
      "ds_read_b64_tr_b16 a[164:165], v205 offset:40960\n"
      "v_add_f32 v193, v193, v137\n"
      "v_mfma_f32_32x32x16_bf16 v[144:159], a[96:99], a[32:35], v[144:159]\n"
      "s_add_u32 m0, s41, 17280\n"
      "ds_read_b64_tr_b16 a[166:167], v205 offset:45056\n"
      "ds_read_b64_tr_b16 a[168:169], v204 offset:41984\n"
      "v_add_f32 v192, v192, v138\n"
      "buffer_load_dwordx4 v217, %2, s44 offen offset:128 lds\n"
      "v_mfma_f32_32x32x16_bf16 v[144:159], a[100:103], a[36:39], v[144:159]\n"
      "ds_read_b64_tr_b16 a[170:171], v204 offset:46080\n"
      "ds_read_b64_tr_b16 a[172:173], v205 offset:41984\n"
      "ds_read_b64_tr_b16 a[174:175], v205 offset:46080\n"
      "v_add_f32 v193, v193, v139\n"
      "s_waitcnt lgkmcnt(12)\n"
      "v_mfma_f32_32x32x16_bf16 v[144:159], a[104:107], a[40:43], v[144:159]\n"
      "ds_read_b64_tr_b16 a[176:177], v204 offset:43008\n"
      "ds_read_b64_tr_b16 a[178:179], v204 offset:47104\n"
      "ds_read_b64_tr_b16 a[180:181], v205 offset:43008\n"
      "v_add_f32 v192, v192, v140\n"
      "v_mfma_f32_32x32x16_bf16 v[144:159], a[108:111], a[44:47], v[144:159]\n"
      "ds_read_b64_tr_b16 a[182:183], v205 offset:47104\n"
      "ds_read_b64_tr_b16 a[184:185], v204 offset:44032\n"
      "ds_read_b64_tr_b16 a[186:187], v204 offset:48128\n"
      "v_add_f32 v193, v193, v141\n"
      "v_mfma_f32_32x32x16_bf16 v[144:159], a[112:115], a[48:51], v[144:159]\n"
      "s_add_u32 m0, s41, 18176\n"
      "ds_read_b64_tr_b16 a[188:189], v205 offset:44032\n"
      "ds_read_b64_tr_b16 a[190:191], v205 offset:48128\n"
      "buffer_load_dwordx4 v217, %2, s44 offen offset:256 lds\n"
      "v_mfma_f32_32x32x16_bf16 v[144:159], a[116:119], a[52:55], v[144:159]\n"
      "v_add_f32 v192, v192, v142\n"
      "v_add_f32 v193, v193, v143\n"
      "v_cvt_pk_bf16_f32 v180, v136, v137\n"
      "v_mfma_f32_32x32x16_bf16 v[144:159], a[120:123], a[56:59], v[144:159]\n"
      "s_add_u32 m0, s41, 19072\n"
      "v_cvt_pk_bf16_f32 v181, v138, v139\n"
      "buffer_load_dwordx4 v217, %2, s44 offen offset:384 lds\n"
      "s_add_u32 s44, s44, 0x4000\n"
      "s_and_b32 s44, s44, 0x7ffff\n"
      "v_mfma_f32_32x32x16_bf16 v[144:159], a[124:127], a[60:63], v[144:159]\n"
      "s_waitcnt vmcnt(16) lgkmcnt(0)\n"
      "s_barrier\n"
      "v_mfma_f32_32x32x16_bf16 v[0:15], a[128:131], v[176:179], v[0:15]\n"
      "s_add_u32 m0, s40, 16384\n"
      "ds_read_b128 a[64:67], v200 offset:32768\n"
      "buffer_load_dwordx4 v216, %1, s43 offen offset:0 lds\n"
      "v_mfma_f32_32x32x16_bf16 v[16:31], a[132:135], v[176:179], v[16:31]\n"
      "ds_read_b128 a[68:71], v201 offset:32768\n"
      "ds_read_b64_tr_b16 a[128:129], v204 offset:49152\n"
      "v_cvt_pk_bf16_f32 v182, v140, v141\n"
      "v_mfma_f32_32x32x16_bf16 v[32:47], a[136:139], v[176:179], v[32:47]\n"
      "ds_read_b128 a[72:75], v202 offset:32768\n"
      "ds_read_b64_tr_b16 a[130:131], v204 offset:53248\n"
      "v_cvt_pk_bf16_f32 v183, v142, v143\n"
      "global_load_dword v235, v231, s[70:71]\n"
      "v_mfma_f32_32x32x16_bf16 v[48:63], a[140:143], v[176:179], v[48:63]\n"
      "ds_read_b128 a[76:79], v203 offset:32768\n"
      "ds_read_b64_tr_b16 a[132:133], v205 offset:49152\n"
      "v_max3_f32 v194, v144, v145, v146\n"
      "v_max3_f32 v195, v147, v148, v149\n"
      "v_mfma_f32_32x32x16_bf16 v[64:79], a[144:147], v[176:179], v[64:79]\n"
      "s_add_u32 m0, s40, 17280\n"
      "ds_read_b128 a[80:83], v200 offset:33792\n"
      "v_max3_f32 v194, v194, v150, v151\n"
      "v_max3_f32 v195, v195, v152, v153\n"
      "buffer_load_dwordx4 v216, %1, s43 offen offset:128 lds\n"
      "v_mfma_f32_32x32x16_bf16 v[80:95], a[148:151], v[176:179], v[80:95]\n"
      "ds_read_b128 a[84:87], v201 offset:33792\n"
      "ds_read_b64_tr_b16 a[134:135], v205 offset:53248\n"
      "v_max3_f32 v194, v194, v154, v155\n"
      "v_max3_f32 v195, v195, v156, v157\n"
      "v_mfma_f32_32x32x16_bf16 v[96:111], a[152:155], v[176:179], v[96:111]\n"
      "ds_read_b128 a[88:91], v202 offset:33792\n"
      "ds_read_b64_tr_b16 a[136:137], v204 offset:50176\n"
      "v_max3_f32 v194, v194, v158, v159\n"
      "v_max_f32 v194, v194, v195\n"
      "v_mfma_f32_32x32x16_bf16 v[112:127], a[156:159], v[176:179], v[112:127]\n"
      "ds_read_b128 a[92:95], v203 offset:33792\n"
      "ds_read_b64_tr_b16 a[138:139], v204 offset:54272\n"
      "ds_read_b64_tr_b16 a[140:141], v205 offset:50176\n"
      "v_cmp_lt_f32 vcc, 0x41000000, v194\n"
      "v_mfma_f32_32x32x16_bf16 v[0:15], a[160:163], v[180:183], v[0:15]\n"
      "s_add_u32 m0, s40, 18176\n"
      "ds_read_b128 a[96:99], v200 offset:34816\n"
      "s_cbranch_vccnz LcA_RA5_%=\n"
      "LcA_RRA5_%=:\n"
      "buffer_load_dwordx4 v216, %1, s43 offen offset:256 lds\n"
      "v_mfma_f32_32x32x16_bf16 v[16:31], a[164:167], v[180:183], v[16:31]\n"
      "ds_read_b128 a[100:103], v201 offset:34816\n"
      "ds_read_b64_tr_b16 a[142:143], v205 offset:54272\n"
      "ds_read_b64_tr_b16 a[144:145], v204 offset:51200\n"
      "v_mfma_f32_32x32x16_bf16 v[32:47], a[168:171], v[180:183], v[32:47]\n"
      "v_exp_f32 v144, v144\n"
      "v_exp_f32 v145, v145\n"
      "v_exp_f32 v146, v146\n"
      "v_mfma_f32_32x32x16_bf16 v[48:63], a[172:175], v[180:183], v[48:63]\n"
      "v_exp_f32 v147, v147\n"
      "v_exp_f32 v148, v148\n"
      "v_exp_f32 v149, v149\n"
      "v_mfma_f32_32x32x16_bf16 v[64:79], a[176:179], v[180:183], v[64:79]\n"
      "v_exp_f32 v150, v150\n"
      "v_exp_f32 v151, v151\n"
      "v_exp_f32 v152, v152\n"
      "v_mfma_f32_32x32x16_bf16 v[80:95], a[180:183], v[180:183], v[80:95]\n"
      "v_exp_f32 v153, v153\n"
      "v_exp_f32 v154, v154\n"
      "v_exp_f32 v155, v155\n"
      "v_mfma_f32_32x32x16_bf16 v[96:111], a[184:187], v[180:183], v[96:111]\n"
      "v_exp_f32 v156, v156\n"
      "v_exp_f32 v157, v157\n"
      "v_exp_f32 v158, v158\n"
      "v_mfma_f32_32x32x16_bf16 v[112:127], a[188:191], v[180:183], v[112:127]\n"
      "v_exp_f32 v159, v159\n"
      "s_waitcnt lgkmcnt(2)\n"
      "s_cmp_lg_u32 s50, 0\n"
      "s_cbranch_scc1 LcA_RB5_%=\n"
      "LcA_RRB5_%=:\n"
      "v_mfma_f32_32x32x16_bf16 v[128:143], a[64:67], a[0:3], v[160:175]\n"
      "s_add_u32 m0, s40, 19072\n"
      "ds_read_b128 a[104:107], v202 offset:34816\n"
      "v_add_f32 v192, v192, v144\n"
      "v_add_f32 v193, v193, v145\n"
      "buffer_load_dwordx4 v216, %1, s43 offen offset:384 lds\n"
      "s_add_u32 s43, s43, 0x4000\n"
      "v_mfma_f32_32x32x16_bf16 v[128:143], a[68:71], a[4:7], v[128:143]\n"
      "ds_read_b128 a[108:111], v203 offset:34816\n"
      "ds_read_b64_tr_b16 a[146:147], v204 offset:55296\n"
      "v_add_f32 v192, v192, v146\n"
      "v_cvt_pk_bf16_f32 v176, v144, v145\n"
      "v_mfma_f32_32x32x16_bf16 v[128:143], a[72:75], a[8:11], v[128:143]\n"
      "ds_read_b128 a[112:115], v200 offset:35840\n"
      "ds_read_b64_tr_b16 a[148:149], v205 offset:51200\n"
      "v_add_f32 v193, v193, v147\n"
      "v_cvt_pk_bf16_f32 v177, v146, v147\n"
      "v_mfma_f32_32x32x16_bf16 v[128:143], a[76:79], a[12:15], v[128:143]\n"
      "ds_read_b128 a[116:119], v201 offset:35840\n"
      "ds_read_b64_tr_b16 a[150:151], v205 offset:55296\n"
      "v_add_f32 v192, v192, v148\n"
      "v_cvt_pk_bf16_f32 v178, v148, v149\n"
      "v_mfma_f32_32x32x16_bf16 v[128:143], a[80:83], a[16:19], v[128:143]\n"
      "s_add_u32 m0, s41, 32768\n"
      "ds_read_b128 a[120:123], v202 offset:35840\n"
      "v_add_f32 v193, v193, v149\n"
      "v_cvt_pk_bf16_f32 v179, v150, v151\n"
      "buffer_load_dwordx4 v217, %2, s44 offen offset:0 lds\n"
      "v_mfma_f32_32x32x16_bf16 v[128:143], a[84:87], a[20:23], v[128:143]\n"
      "ds_read_b128 a[124:127], v203 offset:35840\n"
      "ds_read_b64_tr_b16 a[152:153], v204 offset:52224\n"
      "v_add_f32 v192, v192, v150\n"
      "v_add_f32 v193, v193, v151\n"
      "v_mfma_f32_32x32x16_bf16 v[128:143], a[88:91], a[24:27], v[128:143]\n"
      "ds_read_b64_tr_b16 a[154:155], v204 offset:56320\n"
      "ds_read_b64_tr_b16 a[156:157], v205 offset:52224\n"
      "ds_read_b64_tr_b16 a[158:159], v205 offset:56320\n"
      "v_add_f32 v192, v192, v152\n"
      "v_mfma_f32_32x32x16_bf16 v[128:143], a[92:95], a[28:31], v[128:143]\n"
      "ds_read_b64_tr_b16 a[160:161], v204 offset:57344\n"
      "ds_read_b64_tr_b16 a[162:163], v204 offset:61440\n"
      "ds_read_b64_tr_b16 a[164:165], v205 offset:57344\n"
      "v_add_f32 v193, v193, v153\n"
      "v_mfma_f32_32x32x16_bf16 v[128:143], a[96:99], a[32:35], v[128:143]\n"
      "s_add_u32 m0, s41, 33664\n"
      "ds_read_b64_tr_b16 a[166:167], v205 offset:61440\n"
      "ds_read_b64_tr_b16 a[168:169], v204 offset:58368\n"
      "v_add_f32 v192, v192, v154\n"
      "buffer_load_dwordx4 v217, %2, s44 offen offset:128 lds\n"
      "v_mfma_f32_32x32x16_bf16 v[128:143], a[100:103], a[36:39], v[128:143]\n"
      "ds_read_b64_tr_b16 a[170:171], v204 offset:62464\n"
      "ds_read_b64_tr_b16 a[172:173], v205 offset:58368\n"
      "ds_read_b64_tr_b16 a[174:175], v205 offset:62464\n"
      "v_add_f32 v193, v193, v155\n"
      "s_waitcnt lgkmcnt(12)\n"
      "v_mfma_f32_32x32x16_bf16 v[128:143], a[104:107], a[40:43], v[128:143]\n"
      "ds_read_b64_tr_b16 a[176:177], v204 offset:59392\n"
      "ds_read_b64_tr_b16 a[178:179], v204 offset:63488\n"
      "ds_read_b64_tr_b16 a[180:181], v205 offset:59392\n"
      "v_add_f32 v192, v192, v156\n"
      "v_mfma_f32_32x32x16_bf16 v[128:143], a[108:111], a[44:47], v[128:143]\n"
      "ds_read_b64_tr_b16 a[182:183], v205 offset:63488\n"
      "ds_read_b64_tr_b16 a[184:185], v204 offset:60416\n"
      "ds_read_b64_tr_b16 a[186:187], v204 offset:64512\n"
      "v_add_f32 v193, v193, v157\n"
      "v_mfma_f32_32x32x16_bf16 v[128:143], a[112:115], a[48:51], v[128:143]\n"
      "s_add_u32 m0, s41, 34560\n"
      "ds_read_b64_tr_b16 a[188:189], v205 offset:60416\n"
      "ds_read_b64_tr_b16 a[190:191], v205 offset:64512\n"
      "buffer_load_dwordx4 v217, %2, s44 offen offset:256 lds\n"
      "v_mfma_f32_32x32x16_bf16 v[128:143], a[116:119], a[52:55], v[128:143]\n"
      "v_add_f32 v192, v192, v158\n"
      "v_add_f32 v193, v193, v159\n"
      "v_cvt_pk_bf16_f32 v180, v152, v153\n"
      "v_mfma_f32_32x32x16_bf16 v[128:143], a[120:123], a[56:59], v[128:143]\n"
      "s_add_u32 m0, s41, 35456\n"
      "v_cvt_pk_bf16_f32 v181, v154, v155\n"
      "buffer_load_dwordx4 v217, %2, s44 offen offset:384 lds\n"
      "s_add_u32 s44, s44, 0x4000\n"
      "v_mfma_f32_32x32x16_bf16 v[128:143], a[124:127], a[60:63], v[128:143]\n"
      "s_waitcnt vmcnt(17) lgkmcnt(0)\n"
      "s_barrier\n"
      "v_mfma_f32_32x32x16_bf16 v[0:15], a[128:131], v[176:179], v[0:15]\n"
      "s_add_u32 m0, s40, 32768\n"
      "ds_read_b128 a[64:67], v200 offset:49152\n"
      "buffer_load_dwordx4 v216, %1, s43 offen offset:0 lds\n"
      "v_mfma_f32_32x32x16_bf16 v[16:31], a[132:135], v[176:179], v[16:31]\n"
      "ds_read_b128 a[68:71], v201 offset:49152\n"
      "ds_read_b64_tr_b16 a[128:129], v204 offset:0\n"
      "v_cvt_pk_bf16_f32 v182, v156, v157\n"
      "v_mfma_f32_32x32x16_bf16 v[32:47], a[136:139], v[176:179], v[32:47]\n"
      "ds_read_b128 a[72:75], v202 offset:49152\n"
      "ds_read_b64_tr_b16 a[130:131], v204 offset:4096\n"
      "v_cvt_pk_bf16_f32 v183, v158, v159\n"
      "v_mfma_f32_32x32x16_bf16 v[48:63], a[140:143], v[176:179], v[48:63]\n"
      "ds_read_b128 a[76:79], v203 offset:49152\n"
      "ds_read_b64_tr_b16 a[132:133], v205 offset:0\n"
      "v_max3_f32 v194, v128, v129, v130\n"
      "v_max3_f32 v195, v131, v132, v133\n"
      "v_mfma_f32_32x32x16_bf16 v[64:79], a[144:147], v[176:179], v[64:79]\n"
      "s_add_u32 m0, s40, 33664\n"
      "ds_read_b128 a[80:83], v200 offset:50176\n"
      "v_max3_f32 v194, v194, v134, v135\n"
      "v_max3_f32 v195, v195, v136, v137\n"
      "buffer_load_dwordx4 v216, %1, s43 offen offset:128 lds\n"
      "v_mfma_f32_32x32x16_bf16 v[80:95], a[148:151], v[176:179], v[80:95]\n"
      "ds_read_b128 a[84:87], v201 offset:50176\n"
      "ds_read_b64_tr_b16 a[134:135], v205 offset:4096\n"
      "v_max3_f32 v194, v194, v138, v139\n"
      "v_max3_f32 v195, v195, v140, v141\n"
      "v_mfma_f32_32x32x16_bf16 v[96:111], a[152:155], v[176:179], v[96:111]\n"
      "ds_read_b128 a[88:91], v202 offset:50176\n"
      "ds_read_b64_tr_b16 a[136:137], v204 offset:1024\n"
      "v_max3_f32 v194, v194, v142, v143\n"
      "v_max_f32 v194, v194, v195\n"
      "v_mfma_f32_32x32x16_bf16 v[112:127], a[156:159], v[176:179], v[112:127]\n"
      "ds_read_b128 a[92:95], v203 offset:50176\n"
      "ds_read_b64_tr_b16 a[138:139], v204 offset:5120\n"
      "ds_read_b64_tr_b16 a[140:141], v205 offset:1024\n"
      "v_cmp_lt_f32 vcc, 0x41000000, v194\n"
      "v_mfma_f32_32x32x16_bf16 v[0:15], a[160:163], v[180:183], v[0:15]\n"
      "s_add_u32 m0, s40, 34560\n"
      "ds_read_b128 a[96:99], v200 offset:51200\n"
      "s_cbranch_vccnz LcA_RA6_%=\n"
      "LcA_RRA6_%=:\n"
      "buffer_load_dwordx4 v216, %1, s43 offen offset:256 lds\n"
      "v_mfma_f32_32x32x16_bf16 v[16:31], a[164:167], v[180:183], v[16:31]\n"
      "ds_read_b128 a[100:103], v201 offset:51200\n"
      "ds_read_b64_tr_b16 a[142:143], v205 offset:5120\n"
      "ds_read_b64_tr_b16 a[144:145], v204 offset:2048\n"
      "v_mfma_f32_32x32x16_bf16 v[32:47], a[168:171], v[180:183], v[32:47]\n"
      "v_exp_f32 v128, v128\n"
      "v_exp_f32 v129, v129\n"
      "v_exp_f32 v130, v130\n"
      "v_mfma_f32_32x32x16_bf16 v[48:63], a[172:175], v[180:183], v[48:63]\n"
      "v_exp_f32 v131, v131\n"
      "v_exp_f32 v132, v132\n"
      "v_exp_f32 v133, v133\n"
      "v_mfma_f32_32x32x16_bf16 v[64:79], a[176:179], v[180:183], v[64:79]\n"
      "v_exp_f32 v134, v134\n"
      "v_exp_f32 v135, v135\n"
      "v_exp_f32 v136, v136\n"
      "v_mfma_f32_32x32x16_bf16 v[80:95], a[180:183], v[180:183], v[80:95]\n"
      "v_exp_f32 v137, v137\n"
      "v_exp_f32 v138, v138\n"
      "v_exp_f32 v139, v139\n"
      "v_mfma_f32_32x32x16_bf16 v[96:111], a[184:187], v[180:183], v[96:111]\n"
      "v_exp_f32 v140, v140\n"
      "v_exp_f32 v141, v141\n"
      "v_exp_f32 v142, v142\n"
      "v_mfma_f32_32x32x16_bf16 v[112:127], a[188:191], v[180:183], v[112:127]\n"
      "v_exp_f32 v143, v143\n"
      "s_waitcnt lgkmcnt(2)\n"
      "s_cmp_lg_u32 s50, 0\n"
      "s_cbranch_scc1 LcA_RB6_%=\n"
      "LcA_RRB6_%=:\n"
      "v_mfma_f32_32x32x16_bf16 v[144:159], a[64:67], a[0:3], v[160:175]\n"
      "s_add_u32 m0, s40, 35456\n"
      "ds_read_b128 a[104:107], v202 offset:51200\n"
      "v_add_f32 v192, v192, v128\n"
      "v_add_f32 v193, v193, v129\n"
      "buffer_load_dwordx4 v216, %1, s43 offen offset:384 lds\n"
      "s_add_u32 s43, s43, 0x4000\n"
      "v_mfma_f32_32x32x16_bf16 v[144:159], a[68:71], a[4:7], v[144:159]\n"
      "ds_read_b128 a[108:111], v203 offset:51200\n"
      "ds_read_b64_tr_b16 a[146:147], v204 offset:6144\n"
      "v_add_f32 v192, v192, v130\n"
      "v_cvt_pk_bf16_f32 v176, v128, v129\n"
      "v_mfma_f32_32x32x16_bf16 v[144:159], a[72:75], a[8:11], v[144:159]\n"
      "ds_read_b128 a[112:115], v200 offset:52224\n"
      "ds_read_b64_tr_b16 a[148:149], v205 offset:2048\n"
      "v_add_f32 v193, v193, v131\n"
      "v_cvt_pk_bf16_f32 v177, v130, v131\n"
      "v_mfma_f32_32x32x16_bf16 v[144:159], a[76:79], a[12:15], v[144:159]\n"
      "ds_read_b128 a[116:119], v201 offset:52224\n"
      "ds_read_b64_tr_b16 a[150:151], v205 offset:6144\n"
      "v_add_f32 v192, v192, v132\n"
      "v_cvt_pk_bf16_f32 v178, v132, v133\n"
      "v_mfma_f32_32x32x16_bf16 v[144:159], a[80:83], a[16:19], v[144:159]\n"
      "s_add_u32 m0, s41, 49152\n"
      "ds_read_b128 a[120:123], v202 offset:52224\n"
      "v_add_f32 v193, v193, v133\n"
      "v_cvt_pk_bf16_f32 v179, v134, v135\n"
      "buffer_load_dwordx4 v217, %2, s44 offen offset:0 lds\n"
      "v_mfma_f32_32x32x16_bf16 v[144:159], a[84:87], a[20:23], v[144:159]\n"
      "ds_read_b128 a[124:127], v203 offset:52224\n"
      "ds_read_b64_tr_b16 a[152:153], v204 offset:3072\n"
      "v_add_f32 v192, v192, v134\n"
      "v_add_f32 v193, v193, v135\n"
      "v_mfma_f32_32x32x16_bf16 v[144:159], a[88:91], a[24:27], v[144:159]\n"
      "ds_read_b64_tr_b16 a[154:155], v204 offset:7168\n"
      "ds_read_b64_tr_b16 a[156:157], v205 offset:3072\n"
      "ds_read_b64_tr_b16 a[158:159], v205 offset:7168\n"
      "v_add_f32 v192, v192, v136\n"
      "v_mfma_f32_32x32x16_bf16 v[144:159], a[92:95], a[28:31], v[144:159]\n"
      "ds_read_b64_tr_b16 a[160:161], v204 offset:8192\n"
      "ds_read_b64_tr_b16 a[162:163], v204 offset:12288\n"
      "ds_read_b64_tr_b16 a[164:165], v205 offset:8192\n"
      "v_add_f32 v193, v193, v137\n"
      "v_mfma_f32_32x32x16_bf16 v[144:159], a[96:99], a[32:35], v[144:159]\n"
      "s_add_u32 m0, s41, 50048\n"
      "ds_read_b64_tr_b16 a[166:167], v205 offset:12288\n"
      "ds_read_b64_tr_b16 a[168:169], v204 offset:9216\n"
      "v_add_f32 v192, v192, v138\n"
      "buffer_load_dwordx4 v217, %2, s44 offen offset:128 lds\n"
      "v_mfma_f32_32x32x16_bf16 v[144:159], a[100:103], a[36:39], v[144:159]\n"
      "ds_read_b64_tr_b16 a[170:171], v204 offset:13312\n"
      "ds_read_b64_tr_b16 a[172:173], v205 offset:9216\n"
      "ds_read_b64_tr_b16 a[174:175], v205 offset:13312\n"
      "v_add_f32 v193, v193, v139\n"
      "s_waitcnt lgkmcnt(12)\n"
      "v_mfma_f32_32x32x16_bf16 v[144:159], a[104:107], a[40:43], v[144:159]\n"
      "ds_read_b64_tr_b16 a[176:177], v204 offset:10240\n"
      "ds_read_b64_tr_b16 a[178:179], v204 offset:14336\n"
      "ds_read_b64_tr_b16 a[180:181], v205 offset:10240\n"
      "v_add_f32 v192, v192, v140\n"
      "v_mfma_f32_32x32x16_bf16 v[144:159], a[108:111], a[44:47], v[144:159]\n"
      "ds_read_b64_tr_b16 a[182:183], v205 offset:14336\n"
      "ds_read_b64_tr_b16 a[184:185], v204 offset:11264\n"
      "ds_read_b64_tr_b16 a[186:187], v204 offset:15360\n"
      "v_add_f32 v193, v193, v141\n"
      "v_mfma_f32_32x32x16_bf16 v[144:159], a[112:115], a[48:51], v[144:159]\n"
      "s_add_u32 m0, s41, 50944\n"
      "ds_read_b64_tr_b16 a[188:189], v205 offset:11264\n"
      "ds_read_b64_tr_b16 a[190:191], v205 offset:15360\n"
      "buffer_load_dwordx4 v217, %2, s44 offen offset:256 lds\n"
      "v_mfma_f32_32x32x16_bf16 v[144:159], a[116:119], a[52:55], v[144:159]\n"
      "v_add_f32 v192, v192, v142\n"
      "v_add_f32 v193, v193, v143\n"
      "v_cvt_pk_bf16_f32 v180, v136, v137\n"
      "v_mfma_f32_32x32x16_bf16 v[144:159], a[120:123], a[56:59], v[144:159]\n"
      "s_add_u32 m0, s41, 51840\n"
      "v_cvt_pk_bf16_f32 v181, v138, v139\n"
      "buffer_load_dwordx4 v217, %2, s44 offen offset:384 lds\n"
      "s_add_u32 s44, s44, 0x4000\n"
      "v_mfma_f32_32x32x16_bf16 v[144:159], a[124:127], a[60:63], v[144:159]\n"
      "s_waitcnt vmcnt(17) lgkmcnt(0)\n"
      "s_barrier\n"
      "v_mfma_f32_32x32x16_bf16 v[0:15], a[128:131], v[176:179], v[0:15]\n"
      "s_add_u32 m0, s40, 49152\n"
      "ds_read_b128 a[64:67], v200 offset:0\n"
      "buffer_load_dwordx4 v216, %1, s43 offen offset:0 lds\n"
      "v_mfma_f32_32x32x16_bf16 v[16:31], a[132:135], v[176:179], v[16:31]\n"
      "ds_read_b128 a[68:71], v201 offset:0\n"
      "ds_read_b64_tr_b16 a[128:129], v204 offset:16384\n"
      "v_cvt_pk_bf16_f32 v182, v140, v141\n"
      "v_mfma_f32_32x32x16_bf16 v[32:47], a[136:139], v[176:179], v[32:47]\n"
      "ds_read_b128 a[72:75], v202 offset:0\n"
      "ds_read_b64_tr_b16 a[130:131], v204 offset:20480\n"
      "v_cvt_pk_bf16_f32 v183, v142, v143\n"
      "v_mfma_f32_32x32x16_bf16 v[48:63], a[140:143], v[176:179], v[48:63]\n"
      "ds_read_b128 a[76:79], v203 offset:0\n"
      "ds_read_b64_tr_b16 a[132:133], v205 offset:16384\n"
      "v_max3_f32 v194, v144, v145, v146\n"
      "v_max3_f32 v195, v147, v148, v149\n"
      "v_mfma_f32_32x32x16_bf16 v[64:79], a[144:147], v[176:179], v[64:79]\n"
      "s_add_u32 m0, s40, 50048\n"
      "ds_read_b128 a[80:83], v200 offset:1024\n"
      "v_max3_f32 v194, v194, v150, v151\n"
      "v_max3_f32 v195, v195, v152, v153\n"
      "buffer_load_dwordx4 v216, %1, s43 offen offset:128 lds\n"
      "v_mfma_f32_32x32x16_bf16 v[80:95], a[148:151], v[176:179], v[80:95]\n"
      "ds_read_b128 a[84:87], v201 offset:1024\n"
      "ds_read_b64_tr_b16 a[134:135], v205 offset:20480\n"
      "v_max3_f32 v194, v194, v154, v155\n"
      "v_max3_f32 v195, v195, v156, v157\n"
      "v_mfma_f32_32x32x16_bf16 v[96:111], a[152:155], v[176:179], v[96:111]\n"
      "ds_read_b128 a[88:91], v202 offset:1024\n"
      "ds_read_b64_tr_b16 a[136:137], v204 offset:17408\n"
      "v_max3_f32 v194, v194, v158, v159\n"
      "v_max_f32 v194, v194, v195\n"
      "v_mfma_f32_32x32x16_bf16 v[112:127], a[156:159], v[176:179], v[112:127]\n"
      "ds_read_b128 a[92:95], v203 offset:1024\n"
      "ds_read_b64_tr_b16 a[138:139], v204 offset:21504\n"
      "ds_read_b64_tr_b16 a[140:141], v205 offset:17408\n"
      "v_cmp_lt_f32 vcc, 0x41000000, v194\n"
      "v_mfma_f32_32x32x16_bf16 v[0:15], a[160:163], v[180:183], v[0:15]\n"
      "s_add_u32 m0, s40, 50944\n"
      "ds_read_b128 a[96:99], v200 offset:2048\n"
      "s_cbranch_vccnz LcA_RA7_%=\n"
      "LcA_RRA7_%=:\n"
      "buffer_load_dwordx4 v216, %1, s43 offen offset:256 lds\n"
      "v_mfma_f32_32x32x16_bf16 v[16:31], a[164:167], v[180:183], v[16:31]\n"
      "ds_read_b128 a[100:103], v201 offset:2048\n"
      "ds_read_b64_tr_b16 a[142:143], v205 offset:21504\n"
      "ds_read_b64_tr_b16 a[144:145], v204 offset:18432\n"
      "v_mfma_f32_32x32x16_bf16 v[32:47], a[168:171], v[180:183], v[32:47]\n"
      "v_exp_f32 v144, v144\n"
      "v_exp_f32 v145, v145\n"
      "v_exp_f32 v146, v146\n"
      "v_mfma_f32_32x32x16_bf16 v[48:63], a[172:175], v[180:183], v[48:63]\n"
      "v_exp_f32 v147, v147\n"
      "v_exp_f32 v148, v148\n"
      "v_exp_f32 v149, v149\n"
      "v_mfma_f32_32x32x16_bf16 v[64:79], a[176:179], v[180:183], v[64:79]\n"
      "v_exp_f32 v150, v150\n"
      "v_exp_f32 v151, v151\n"
      "v_exp_f32 v152, v152\n"
      "v_mfma_f32_32x32x16_bf16 v[80:95], a[180:183], v[180:183], v[80:95]\n"
      "v_exp_f32 v153, v153\n"
      "v_exp_f32 v154, v154\n"
      "v_exp_f32 v155, v155\n"
      "v_mfma_f32_32x32x16_bf16 v[96:111], a[184:187], v[180:183], v[96:111]\n"
      "v_exp_f32 v156, v156\n"
      "v_exp_f32 v157, v157\n"
      "v_exp_f32 v158, v158\n"
      "v_mfma_f32_32x32x16_bf16 v[112:127], a[188:191], v[180:183], v[112:127]\n"
      "v_exp_f32 v159, v159\n"
      "s_waitcnt lgkmcnt(2)\n"
      "s_cmp_lg_u32 s50, 0\n"
      "s_cbranch_scc1 LcA_RB7_%=\n"
      "LcA_RRB7_%=:\n"
      "s_sub_u32 s51, s51, 1\n"
      "s_cmp_lg_u32 s51, 0\n"
      "s_cbranch_scc1 LcA_loop_%=\n"
      ".Lmha_late:\n"
      "v_mfma_f32_32x32x16_bf16 v[128:143], a[64:67], a[0:3], v[160:175]\n"
      "s_add_u32 m0, s40, 51840\n"
      "ds_read_b128 a[104:107], v202 offset:2048\n"
      "v_add_f32 v192, v192, v144\n"
      "v_add_f32 v193, v193, v145\n"
      "buffer_load_dwordx4 v216, %1, s43 offen offset:384 lds\n"
      "s_add_u32 s43, s43, 0x4000\n"
      "s_and_b32 s43, s43, 0x7ffff\n"
      "v_mfma_f32_32x32x16_bf16 v[128:143], a[68:71], a[4:7], v[128:143]\n"
      "ds_read_b128 a[108:111], v203 offset:2048\n"
      "ds_read_b64_tr_b16 a[146:147], v204 offset:22528\n"
      "v_add_f32 v192, v192, v146\n"
      "v_cvt_pk_bf16_f32 v176, v144, v145\n"
      "v_mfma_f32_32x32x16_bf16 v[128:143], a[72:75], a[8:11], v[128:143]\n"
      "ds_read_b128 a[112:115], v200 offset:3072\n"
      "ds_read_b64_tr_b16 a[148:149], v205 offset:18432\n"
      "v_add_f32 v193, v193, v147\n"
      "v_cvt_pk_bf16_f32 v177, v146, v147\n"
      "v_mfma_f32_32x32x16_bf16 v[128:143], a[76:79], a[12:15], v[128:143]\n"
      "ds_read_b128 a[116:119], v201 offset:3072\n"
      "ds_read_b64_tr_b16 a[150:151], v205 offset:22528\n"
      "v_add_f32 v192, v192, v148\n"
      "v_cvt_pk_bf16_f32 v178, v148, v149\n"
      "v_mfma_f32_32x32x16_bf16 v[128:143], a[80:83], a[16:19], v[128:143]\n"
      "s_add_u32 m0, s41, 0\n"
      "ds_read_b128 a[120:123], v202 offset:3072\n"
      "v_add_f32 v193, v193, v149\n"
      "v_cvt_pk_bf16_f32 v179, v150, v151\n"
      "buffer_load_dwordx4 v217, %2, s44 offen offset:0 lds\n"
      "v_mfma_f32_32x32x16_bf16 v[128:143], a[84:87], a[20:23], v[128:143]\n"
      "ds_read_b128 a[124:127], v203 offset:3072\n"
      "ds_read_b64_tr_b16 a[152:153], v204 offset:19456\n"
      "v_add_f32 v192, v192, v150\n"
      "v_add_f32 v193, v193, v151\n"
      "v_mfma_f32_32x32x16_bf16 v[128:143], a[88:91], a[24:27], v[128:143]\n"
      "ds_read_b64_tr_b16 a[154:155], v204 offset:23552\n"
      "ds_read_b64_tr_b16 a[156:157], v205 offset:19456\n"
      "ds_read_b64_tr_b16 a[158:159], v205 offset:23552\n"
      "v_add_f32 v192, v192, v152\n"
      "v_mfma_f32_32x32x16_bf16 v[128:143], a[92:95], a[28:31], v[128:143]\n"
      "ds_read_b64_tr_b16 a[160:161], v204 offset:24576\n"
      "ds_read_b64_tr_b16 a[162:163], v204 offset:28672\n"
      "ds_read_b64_tr_b16 a[164:165], v205 offset:24576\n"
      "v_add_f32 v193, v193, v153\n"
      "v_mfma_f32_32x32x16_bf16 v[128:143], a[96:99], a[32:35], v[128:143]\n"
      "s_add_u32 m0, s41, 896\n"
      "ds_read_b64_tr_b16 a[166:167], v205 offset:28672\n"
      "ds_read_b64_tr_b16 a[168:169], v204 offset:25600\n"
      "v_add_f32 v192, v192, v154\n"
      "buffer_load_dwordx4 v217, %2, s44 offen offset:128 lds\n"
      "v_mfma_f32_32x32x16_bf16 v[128:143], a[100:103], a[36:39], v[128:143]\n"
      "ds_read_b64_tr_b16 a[170:171], v204 offset:29696\n"
      "ds_read_b64_tr_b16 a[172:173], v205 offset:25600\n"
      "ds_read_b64_tr_b16 a[174:175], v205 offset:29696\n"
      "v_add_f32 v193, v193, v155\n"
      "s_waitcnt lgkmcnt(12)\n"
      "v_mfma_f32_32x32x16_bf16 v[128:143], a[104:107], a[40:43], v[128:143]\n"
      "ds_read_b64_tr_b16 a[176:177], v204 offset:26624\n"
      "ds_read_b64_tr_b16 a[178:179], v204 offset:30720\n"
      "ds_read_b64_tr_b16 a[180:181], v205 offset:26624\n"
      "v_add_f32 v192, v192, v156\n"
      "v_mfma_f32_32x32x16_bf16 v[128:143], a[108:111], a[44:47], v[128:143]\n"
      "ds_read_b64_tr_b16 a[182:183], v205 offset:30720\n"
      "ds_read_b64_tr_b16 a[184:185], v204 offset:27648\n"
      "ds_read_b64_tr_b16 a[186:187], v204 offset:31744\n"
      "v_add_f32 v193, v193, v157\n"
      "v_mfma_f32_32x32x16_bf16 v[128:143], a[112:115], a[48:51], v[128:143]\n"
      "s_add_u32 m0, s41, 1792\n"
      "ds_read_b64_tr_b16 a[188:189], v205 offset:27648\n"
      "ds_read_b64_tr_b16 a[190:191], v205 offset:31744\n"
      "buffer_load_dwordx4 v217, %2, s44 offen offset:256 lds\n"
      "v_mfma_f32_32x32x16_bf16 v[128:143], a[116:119], a[52:55], v[128:143]\n"
      "v_add_f32 v192, v192, v158\n"
      "v_add_f32 v193, v193, v159\n"
      "v_cvt_pk_bf16_f32 v180, v152, v153\n"
      "v_mfma_f32_32x32x16_bf16 v[128:143], a[120:123], a[56:59], v[128:143]\n"
      "s_add_u32 m0, s41, 2688\n"
      "v_cvt_pk_bf16_f32 v181, v154, v155\n"
      "buffer_load_dwordx4 v217, %2, s44 offen offset:384 lds\n"
      "s_add_u32 s44, s44, 0x4000\n"
      "v_mfma_f32_32x32x16_bf16 v[128:143], a[124:127], a[60:63], v[128:143]\n"
      "s_waitcnt vmcnt(16) lgkmcnt(0)\n"
      "s_barrier\n"
      "v_mfma_f32_32x32x16_bf16 v[0:15], a[128:131], v[176:179], v[0:15]\n"
      "s_add_u32 m0, s40, 0\n"
      "ds_read_b128 a[64:67], v200 offset:16384\n"
      "buffer_load_dwordx4 v223, %5, s47 offen lds\n"
      "v_mfma_f32_32x32x16_bf16 v[16:31], a[132:135], v[176:179], v[16:31]\n"
      "ds_read_b128 a[68:71], v201 offset:16384\n"
      "ds_read_b64_tr_b16 a[128:129], v204 offset:32768\n"
      "v_cvt_pk_bf16_f32 v182, v156, v157\n"
      "v_mfma_f32_32x32x16_bf16 v[32:47], a[136:139], v[176:179], v[32:47]\n"
      "ds_read_b128 a[72:75], v202 offset:16384\n"
      "ds_read_b64_tr_b16 a[130:131], v204 offset:36864\n"
      "v_cvt_pk_bf16_f32 v183, v158, v159\n"
      "v_mfma_f32_32x32x16_bf16 v[48:63], a[140:143], v[176:179], v[48:63]\n"
      "ds_read_b128 a[76:79], v203 offset:16384\n"
      "ds_read_b64_tr_b16 a[132:133], v205 offset:32768\n"
      "v_max3_f32 v194, v128, v129, v130\n"
      "v_max3_f32 v195, v131, v132, v133\n"
      "v_mfma_f32_32x32x16_bf16 v[64:79], a[144:147], v[176:179], v[64:79]\n"
      "s_add_u32 m0, s40, 1024\n"
      "ds_read_b128 a[80:83], v200 offset:17408\n"
      "v_max3_f32 v194, v194, v134, v135\n"
      "v_max3_f32 v195, v195, v136, v137\n"
      "buffer_load_dwordx4 v224, %5, s47 offen lds\n"
      "v_mfma_f32_32x32x16_bf16 v[80:95], a[148:151], v[176:179], v[80:95]\n"
      "ds_read_b128 a[84:87], v201 offset:17408\n"
      "ds_read_b64_tr_b16 a[134:135], v205 offset:36864\n"
      "v_max3_f32 v194, v194, v138, v139\n"
      "v_max3_f32 v195, v195, v140, v141\n"
      "v_mfma_f32_32x32x16_bf16 v[96:111], a[152:155], v[176:179], v[96:111]\n"
      "ds_read_b128 a[88:91], v202 offset:17408\n"
      "ds_read_b64_tr_b16 a[136:137], v204 offset:33792\n"
      "v_max3_f32 v194, v194, v142, v143\n"
      "v_max_f32 v194, v194, v195\n"
      "v_mfma_f32_32x32x16_bf16 v[112:127], a[156:159], v[176:179], v[112:127]\n"
      "ds_read_b128 a[92:95], v203 offset:17408\n"
      "ds_read_b64_tr_b16 a[138:139], v204 offset:37888\n"
      "ds_read_b64_tr_b16 a[140:141], v205 offset:33792\n"
      "v_cmp_lt_f32 vcc, 0x41000000, v194\n"
      "v_mfma_f32_32x32x16_bf16 v[0:15], a[160:163], v[180:183], v[0:15]\n"
      "s_add_u32 m0, s40, 2048\n"
      "ds_read_b128 a[96:99], v200 offset:18432\n"
      "s_cbranch_vccnz LcA_RA28_%=\n"
      "LcA_RRA28_%=:\n"
      "buffer_load_dwordx4 v225, %5, s47 offen lds\n"
      "v_mfma_f32_32x32x16_bf16 v[16:31], a[164:167], v[180:183], v[16:31]\n"
      "ds_read_b128 a[100:103], v201 offset:18432\n"
      "ds_read_b64_tr_b16 a[142:143], v205 offset:37888\n"
      "ds_read_b64_tr_b16 a[144:145], v204 offset:34816\n"
      "v_mfma_f32_32x32x16_bf16 v[32:47], a[168:171], v[180:183], v[32:47]\n"
      "v_exp_f32 v128, v128\n"
      "v_exp_f32 v129, v129\n"
      "v_exp_f32 v130, v130\n"
      "v_mfma_f32_32x32x16_bf16 v[48:63], a[172:175], v[180:183], v[48:63]\n"
      "v_exp_f32 v131, v131\n"
      "v_exp_f32 v132, v132\n"
      "v_exp_f32 v133, v133\n"
      "v_mfma_f32_32x32x16_bf16 v[64:79], a[176:179], v[180:183], v[64:79]\n"
      "v_exp_f32 v134, v134\n"
      "v_exp_f32 v135, v135\n"
      "v_exp_f32 v136, v136\n"
      "v_mfma_f32_32x32x16_bf16 v[80:95], a[180:183], v[180:183], v[80:95]\n"
      "v_exp_f32 v137, v137\n"
      "v_exp_f32 v138, v138\n"
      "v_exp_f32 v139, v139\n"
      "v_mfma_f32_32x32x16_bf16 v[96:111], a[184:187], v[180:183], v[96:111]\n"
      "v_exp_f32 v140, v140\n"
      "v_exp_f32 v141, v141\n"
      "v_exp_f32 v142, v142\n"
      "v_mfma_f32_32x32x16_bf16 v[112:127], a[188:191], v[180:183], v[112:127]\n"
      "v_exp_f32 v143, v143\n"
      "s_waitcnt lgkmcnt(2)\n"
      "s_cmp_lg_u32 s50, 0\n"
      "s_cbranch_scc1 LcA_RB28_%=\n"
      "LcA_RRB28_%=:\n"
      "v_mfma_f32_32x32x16_bf16 v[144:159], a[64:67], a[0:3], v[160:175]\n"
      "s_add_u32 m0, s40, 3072\n"
      "ds_read_b128 a[104:107], v202 offset:18432\n"
      "v_add_f32 v192, v192, v128\n"
      "v_add_f32 v193, v193, v129\n"
      "buffer_load_dwordx4 v226, %5, s47 offen lds\n"
      "s_add_u32 s47, s47, 128\n"
      "s_cmp_eq_u32 s47, 896\n"
      "s_cselect_b32 s47, 0, s47\n"
      "v_mfma_f32_32x32x16_bf16 v[144:159], a[68:71], a[4:7], v[144:159]\n"
      "ds_read_b128 a[108:111], v203 offset:18432\n"
      "ds_read_b64_tr_b16 a[146:147], v204 offset:38912\n"
      "v_add_f32 v192, v192, v130\n"
      "v_cvt_pk_bf16_f32 v176, v128, v129\n"
      "v_mfma_f32_32x32x16_bf16 v[144:159], a[72:75], a[8:11], v[144:159]\n"
      "ds_read_b128 a[112:115], v200 offset:19456\n"
      "ds_read_b64_tr_b16 a[148:149], v205 offset:34816\n"
      "v_add_f32 v193, v193, v131\n"
      "v_cvt_pk_bf16_f32 v177, v130, v131\n"
      "v_mfma_f32_32x32x16_bf16 v[144:159], a[76:79], a[12:15], v[144:159]\n"
      "ds_read_b128 a[116:119], v201 offset:19456\n"
      "ds_read_b64_tr_b16 a[150:151], v205 offset:38912\n"
      "v_add_f32 v192, v192, v132\n"
      "v_cvt_pk_bf16_f32 v178, v132, v133\n"
      "v_mfma_f32_32x32x16_bf16 v[144:159], a[80:83], a[16:19], v[144:159]\n"
      "s_add_u32 m0, s41, 16384\n"
      "ds_read_b128 a[120:123], v202 offset:19456\n"
      "v_add_f32 v193, v193, v133\n"
      "v_cvt_pk_bf16_f32 v179, v134, v135\n"
      "buffer_load_dwordx4 v217, %2, s44 offen offset:0 lds\n"
      "v_mfma_f32_32x32x16_bf16 v[144:159], a[84:87], a[20:23], v[144:159]\n"
      "ds_read_b128 a[124:127], v203 offset:19456\n"
      "ds_read_b64_tr_b16 a[152:153], v204 offset:35840\n"
      "v_add_f32 v192, v192, v134\n"
      "v_add_f32 v193, v193, v135\n"
      "v_mfma_f32_32x32x16_bf16 v[144:159], a[88:91], a[24:27], v[144:159]\n"
      "ds_read_b64_tr_b16 a[154:155], v204 offset:39936\n"
      "ds_read_b64_tr_b16 a[156:157], v205 offset:35840\n"
      "ds_read_b64_tr_b16 a[158:159], v205 offset:39936\n"
      "v_add_f32 v192, v192, v136\n"
      "v_mfma_f32_32x32x16_bf16 v[144:159], a[92:95], a[28:31], v[144:159]\n"
      "ds_read_b64_tr_b16 a[160:161], v204 offset:40960\n"
      "ds_read_b64_tr_b16 a[162:163], v204 offset:45056\n"
      "ds_read_b64_tr_b16 a[164:165], v205 offset:40960\n"
      "v_add_f32 v193, v193, v137\n"
      "v_mfma_f32_32x32x16_bf16 v[144:159], a[96:99], a[32:35], v[144:159]\n"
      "s_add_u32 m0, s41, 17280\n"
      "ds_read_b64_tr_b16 a[166:167], v205 offset:45056\n"
      "ds_read_b64_tr_b16 a[168:169], v204 offset:41984\n"
      "v_add_f32 v192, v192, v138\n"
      "buffer_load_dwordx4 v217, %2, s44 offen offset:128 lds\n"
      "v_mfma_f32_32x32x16_bf16 v[144:159], a[100:103], a[36:39], v[144:159]\n"
      "ds_read_b64_tr_b16 a[170:171], v204 offset:46080\n"
      "ds_read_b64_tr_b16 a[172:173], v205 offset:41984\n"
      "ds_read_b64_tr_b16 a[174:175], v205 offset:46080\n"
      "v_add_f32 v193, v193, v139\n"
      "s_waitcnt lgkmcnt(12)\n"
      "v_mfma_f32_32x32x16_bf16 v[144:159], a[104:107], a[40:43], v[144:159]\n"
      "ds_read_b64_tr_b16 a[176:177], v204 offset:43008\n"
      "ds_read_b64_tr_b16 a[178:179], v204 offset:47104\n"
      "ds_read_b64_tr_b16 a[180:181], v205 offset:43008\n"
      "v_add_f32 v192, v192, v140\n"
      "v_mfma_f32_32x32x16_bf16 v[144:159], a[108:111], a[44:47], v[144:159]\n"
      "ds_read_b64_tr_b16 a[182:183], v205 offset:47104\n"
      "ds_read_b64_tr_b16 a[184:185], v204 offset:44032\n"
      "ds_read_b64_tr_b16 a[186:187], v204 offset:48128\n"
      "v_add_f32 v193, v193, v141\n"
      "v_mfma_f32_32x32x16_bf16 v[144:159], a[112:115], a[48:51], v[144:159]\n"
      "s_add_u32 m0, s41, 18176\n"
      "ds_read_b64_tr_b16 a[188:189], v205 offset:44032\n"
      "ds_read_b64_tr_b16 a[190:191], v205 offset:48128\n"
      "buffer_load_dwordx4 v217, %2, s44 offen offset:256 lds\n"
      "v_mfma_f32_32x32x16_bf16 v[144:159], a[116:119], a[52:55], v[144:159]\n"
      "v_add_f32 v192, v192, v142\n"
      "v_add_f32 v193, v193, v143\n"
      "v_cvt_pk_bf16_f32 v180, v136, v137\n"
      "v_mfma_f32_32x32x16_bf16 v[144:159], a[120:123], a[56:59], v[144:159]\n"
      "s_add_u32 m0, s41, 19072\n"
      "v_cvt_pk_bf16_f32 v181, v138, v139\n"
      "buffer_load_dwordx4 v217, %2, s44 offen offset:384 lds\n"
      "s_add_u32 s44, s44, 0x4000\n"
      "s_and_b32 s44, s44, 0x7ffff\n"
      "v_mfma_f32_32x32x16_bf16 v[144:159], a[124:127], a[60:63], v[144:159]\n"
      "s_waitcnt vmcnt(16) lgkmcnt(0)\n"
      "s_barrier\n"
      "v_mfma_f32_32x32x16_bf16 v[0:15], a[128:131], v[176:179], v[0:15]\n"
      "s_add_u32 m0, s40, 16384\n"
      "ds_read_b128 a[64:67], v200 offset:32768\n"
      "buffer_load_dwordx4 v223, %5, s47 offen lds\n"
      "v_mfma_f32_32x32x16_bf16 v[16:31], a[132:135], v[176:179], v[16:31]\n"
      "ds_read_b128 a[68:71], v201 offset:32768\n"
      "ds_read_b64_tr_b16 a[128:129], v204 offset:49152\n"
      "v_cvt_pk_bf16_f32 v182, v140, v141\n"
      "v_mfma_f32_32x32x16_bf16 v[32:47], a[136:139], v[176:179], v[32:47]\n"
      "ds_read_b128 a[72:75], v202 offset:32768\n"
      "ds_read_b64_tr_b16 a[130:131], v204 offset:53248\n"
      "v_cvt_pk_bf16_f32 v183, v142, v143\n"
      "v_mfma_f32_32x32x16_bf16 v[48:63], a[140:143], v[176:179], v[48:63]\n"
      "ds_read_b128 a[76:79], v203 offset:32768\n"
      "ds_read_b64_tr_b16 a[132:133], v205 offset:49152\n"
      "v_max3_f32 v194, v144, v145, v146\n"
      "v_max3_f32 v195, v147, v148, v149\n"
      "v_mfma_f32_32x32x16_bf16 v[64:79], a[144:147], v[176:179], v[64:79]\n"
      "s_add_u32 m0, s40, 17408\n"
      "ds_read_b128 a[80:83], v200 offset:33792\n"
      "v_max3_f32 v194, v194, v150, v151\n"
      "v_max3_f32 v195, v195, v152, v153\n"
      "buffer_load_dwordx4 v224, %5, s47 offen lds\n"
      "v_mfma_f32_32x32x16_bf16 v[80:95], a[148:151], v[176:179], v[80:95]\n"
      "ds_read_b128 a[84:87], v201 offset:33792\n"
      "ds_read_b64_tr_b16 a[134:135], v205 offset:53248\n"
      "v_max3_f32 v194, v194, v154, v155\n"
      "v_max3_f32 v195, v195, v156, v157\n"
      "v_mfma_f32_32x32x16_bf16 v[96:111], a[152:155], v[176:179], v[96:111]\n"
      "ds_read_b128 a[88:91], v202 offset:33792\n"
      "ds_read_b64_tr_b16 a[136:137], v204 offset:50176\n"
      "v_max3_f32 v194, v194, v158, v159\n"
      "v_max_f32 v194, v194, v195\n"
      "v_mfma_f32_32x32x16_bf16 v[112:127], a[156:159], v[176:179], v[112:127]\n"
      "ds_read_b128 a[92:95], v203 offset:33792\n"
      "ds_read_b64_tr_b16 a[138:139], v204 offset:54272\n"
      "ds_read_b64_tr_b16 a[140:141], v205 offset:50176\n"
      "v_cmp_lt_f32 vcc, 0x41000000, v194\n"
      "v_mfma_f32_32x32x16_bf16 v[0:15], a[160:163], v[180:183], v[0:15]\n"
      "s_add_u32 m0, s40, 18432\n"
      "ds_read_b128 a[96:99], v200 offset:34816\n"
      "s_cbranch_vccnz LcA_RA29_%=\n"
      "LcA_RRA29_%=:\n"
      "buffer_load_dwordx4 v225, %5, s47 offen lds\n"
      "v_mfma_f32_32x32x16_bf16 v[16:31], a[164:167], v[180:183], v[16:31]\n"
      "ds_read_b128 a[100:103], v201 offset:34816\n"
      "ds_read_b64_tr_b16 a[142:143], v205 offset:54272\n"
      "ds_read_b64_tr_b16 a[144:145], v204 offset:51200\n"
      "v_mfma_f32_32x32x16_bf16 v[32:47], a[168:171], v[180:183], v[32:47]\n"
      "v_exp_f32 v144, v144\n"
      "v_exp_f32 v145, v145\n"
      "v_exp_f32 v146, v146\n"
      "v_mfma_f32_32x32x16_bf16 v[48:63], a[172:175], v[180:183], v[48:63]\n"
      "v_exp_f32 v147, v147\n"
      "v_exp_f32 v148, v148\n"
      "v_exp_f32 v149, v149\n"
      "v_mfma_f32_32x32x16_bf16 v[64:79], a[176:179], v[180:183], v[64:79]\n"
      "v_exp_f32 v150, v150\n"
      "v_exp_f32 v151, v151\n"
      "v_exp_f32 v152, v152\n"
      "v_mfma_f32_32x32x16_bf16 v[80:95], a[180:183], v[180:183], v[80:95]\n"
      "v_exp_f32 v153, v153\n"
      "v_exp_f32 v154, v154\n"
      "v_exp_f32 v155, v155\n"
      "v_mfma_f32_32x32x16_bf16 v[96:111], a[184:187], v[180:183], v[96:111]\n"
      "v_exp_f32 v156, v156\n"
      "v_exp_f32 v157, v157\n"
      "v_exp_f32 v158, v158\n"
      "v_mfma_f32_32x32x16_bf16 v[112:127], a[188:191], v[180:183], v[112:127]\n"
      "v_exp_f32 v159, v159\n"
      "s_waitcnt lgkmcnt(2)\n"
      "s_cmp_lg_u32 s50, 0\n"
      "s_cbranch_scc1 LcA_RB29_%=\n"
      "LcA_RRB29_%=:\n"
      "v_mfma_f32_32x32x16_bf16 v[128:143], a[64:67], a[0:3], v[160:175]\n"
      "s_add_u32 m0, s40, 19456\n"
      "ds_read_b128 a[104:107], v202 offset:34816\n"
      "v_add_f32 v192, v192, v144\n"
      "v_add_f32 v193, v193, v145\n"
      "buffer_load_dwordx4 v226, %5, s47 offen lds\n"
      "s_add_u32 s47, s47, 128\n"
      "s_cmp_eq_u32 s47, 896\n"
      "s_cselect_b32 s47, 0, s47\n"
      "v_mfma_f32_32x32x16_bf16 v[128:143], a[68:71], a[4:7], v[128:143]\n"
      "ds_read_b128 a[108:111], v203 offset:34816\n"
      "ds_read_b64_tr_b16 a[146:147], v204 offset:55296\n"
      "v_add_f32 v192, v192, v146\n"
      "v_cvt_pk_bf16_f32 v176, v144, v145\n"
      "v_mfma_f32_32x32x16_bf16 v[128:143], a[72:75], a[8:11], v[128:143]\n"
      "ds_read_b128 a[112:115], v200 offset:35840\n"
      "ds_read_b64_tr_b16 a[148:149], v205 offset:51200\n"
      "v_add_f32 v193, v193, v147\n"
      "v_cvt_pk_bf16_f32 v177, v146, v147\n"
      "v_mfma_f32_32x32x16_bf16 v[128:143], a[76:79], a[12:15], v[128:143]\n"
      "ds_read_b128 a[116:119], v201 offset:35840\n"
      "ds_read_b64_tr_b16 a[150:151], v205 offset:55296\n"
      "v_add_f32 v192, v192, v148\n"
      "v_cvt_pk_bf16_f32 v178, v148, v149\n"
      "v_mfma_f32_32x32x16_bf16 v[128:143], a[80:83], a[16:19], v[128:143]\n"
      "s_add_u32 m0, s41, 32768\n"
      "ds_read_b128 a[120:123], v202 offset:35840\n"
      "v_add_f32 v193, v193, v149\n"
      "v_cvt_pk_bf16_f32 v179, v150, v151\n"
      "buffer_load_dwordx4 v223, %5, s48 offen lds\n"
      "v_mfma_f32_32x32x16_bf16 v[128:143], a[84:87], a[20:23], v[128:143]\n"
      "ds_read_b128 a[124:127], v203 offset:35840\n"
      "ds_read_b64_tr_b16 a[152:153], v204 offset:52224\n"
      "v_add_f32 v192, v192, v150\n"
      "v_add_f32 v193, v193, v151\n"
      "v_mfma_f32_32x32x16_bf16 v[128:143], a[88:91], a[24:27], v[128:143]\n"
      "ds_read_b64_tr_b16 a[154:155], v204 offset:56320\n"
      "ds_read_b64_tr_b16 a[156:157], v205 offset:52224\n"
      "ds_read_b64_tr_b16 a[158:159], v205 offset:56320\n"
      "v_add_f32 v192, v192, v152\n"
      "v_mfma_f32_32x32x16_bf16 v[128:143], a[92:95], a[28:31], v[128:143]\n"
      "ds_read_b64_tr_b16 a[160:161], v204 offset:57344\n"
      "ds_read_b64_tr_b16 a[162:163], v204 offset:61440\n"
      "ds_read_b64_tr_b16 a[164:165], v205 offset:57344\n"
      "v_add_f32 v193, v193, v153\n"
      "v_mfma_f32_32x32x16_bf16 v[128:143], a[96:99], a[32:35], v[128:143]\n"
      "s_add_u32 m0, s41, 33792\n"
      "ds_read_b64_tr_b16 a[166:167], v205 offset:61440\n"
      "ds_read_b64_tr_b16 a[168:169], v204 offset:58368\n"
      "v_add_f32 v192, v192, v154\n"
      "buffer_load_dwordx4 v224, %5, s48 offen lds\n"
      "v_mfma_f32_32x32x16_bf16 v[128:143], a[100:103], a[36:39], v[128:143]\n"
      "ds_read_b64_tr_b16 a[170:171], v204 offset:62464\n"
      "ds_read_b64_tr_b16 a[172:173], v205 offset:58368\n"
      "ds_read_b64_tr_b16 a[174:175], v205 offset:62464\n"
      "v_add_f32 v193, v193, v155\n"
      "s_waitcnt lgkmcnt(12)\n"
      "v_mfma_f32_32x32x16_bf16 v[128:143], a[104:107], a[40:43], v[128:143]\n"
      "ds_read_b64_tr_b16 a[176:177], v204 offset:59392\n"
      "ds_read_b64_tr_b16 a[178:179], v204 offset:63488\n"
      "ds_read_b64_tr_b16 a[180:181], v205 offset:59392\n"
      "v_add_f32 v192, v192, v156\n"
      "v_mfma_f32_32x32x16_bf16 v[128:143], a[108:111], a[44:47], v[128:143]\n"
      "ds_read_b64_tr_b16 a[182:183], v205 offset:63488\n"
      "ds_read_b64_tr_b16 a[184:185], v204 offset:60416\n"
      "ds_read_b64_tr_b16 a[186:187], v204 offset:64512\n"
      "v_add_f32 v193, v193, v157\n"
      "v_mfma_f32_32x32x16_bf16 v[128:143], a[112:115], a[48:51], v[128:143]\n"
      "s_add_u32 m0, s41, 34816\n"
      "ds_read_b64_tr_b16 a[188:189], v205 offset:60416\n"
      "ds_read_b64_tr_b16 a[190:191], v205 offset:64512\n"
      "buffer_load_dwordx4 v225, %5, s48 offen lds\n"
      "v_mfma_f32_32x32x16_bf16 v[128:143], a[116:119], a[52:55], v[128:143]\n"
      "v_add_f32 v192, v192, v158\n"
      "v_add_f32 v193, v193, v159\n"
      "v_cvt_pk_bf16_f32 v180, v152, v153\n"
      "v_mfma_f32_32x32x16_bf16 v[128:143], a[120:123], a[56:59], v[128:143]\n"
      "s_add_u32 m0, s41, 35840\n"
      "v_cvt_pk_bf16_f32 v181, v154, v155\n"
      "buffer_load_dwordx4 v226, %5, s48 offen lds\n"
      "s_add_u32 s48, s48, 128\n"
      "s_cmp_eq_u32 s48, 115584\n"
      "s_cselect_b32 s48, 114688, s48\n"
      "v_mfma_f32_32x32x16_bf16 v[128:143], a[124:127], a[60:63], v[128:143]\n"
      "s_waitcnt vmcnt(16) lgkmcnt(0)\n"
      "s_barrier\n"
      "v_mfma_f32_32x32x16_bf16 v[0:15], a[128:131], v[176:179], v[0:15]\n"
      "s_add_u32 m0, s40, 32768\n"
      "ds_read_b128 a[64:67], v200 offset:49152\n"
      "buffer_load_dwordx4 v223, %5, s47 offen lds\n"
      "v_mfma_f32_32x32x16_bf16 v[16:31], a[132:135], v[176:179], v[16:31]\n"
      "ds_read_b128 a[68:71], v201 offset:49152\n"
      "ds_read_b64_tr_b16 a[128:129], v204 offset:0\n"
      "v_cvt_pk_bf16_f32 v182, v156, v157\n"
      "v_mfma_f32_32x32x16_bf16 v[32:47], a[136:139], v[176:179], v[32:47]\n"
      "ds_read_b128 a[72:75], v202 offset:49152\n"
      "ds_read_b64_tr_b16 a[130:131], v204 offset:4096\n"
      "v_cvt_pk_bf16_f32 v183, v158, v159\n"
      "v_mfma_f32_32x32x16_bf16 v[48:63], a[140:143], v[176:179], v[48:63]\n"
      "ds_read_b128 a[76:79], v203 offset:49152\n"
      "ds_read_b64_tr_b16 a[132:133], v205 offset:0\n"
      "v_max3_f32 v194, v128, v129, v130\n"
      "v_max3_f32 v195, v131, v132, v133\n"
      "v_mfma_f32_32x32x16_bf16 v[64:79], a[144:147], v[176:179], v[64:79]\n"
      "s_add_u32 m0, s40, 33792\n"
      "ds_read_b128 a[80:83], v200 offset:50176\n"
      "v_max3_f32 v194, v194, v134, v135\n"
      "v_max3_f32 v195, v195, v136, v137\n"
      "buffer_load_dwordx4 v224, %5, s47 offen lds\n"
      "v_mfma_f32_32x32x16_bf16 v[80:95], a[148:151], v[176:179], v[80:95]\n"
      "ds_read_b128 a[84:87], v201 offset:50176\n"
      "ds_read_b64_tr_b16 a[134:135], v205 offset:4096\n"
      "v_max3_f32 v194, v194, v138, v139\n"
      "v_max3_f32 v195, v195, v140, v141\n"
      "v_mfma_f32_32x32x16_bf16 v[96:111], a[152:155], v[176:179], v[96:111]\n"
      "ds_read_b128 a[88:91], v202 offset:50176\n"
      "ds_read_b64_tr_b16 a[136:137], v204 offset:1024\n"
      "v_max3_f32 v194, v194, v142, v143\n"
      "v_max_f32 v194, v194, v195\n"
      "v_mfma_f32_32x32x16_bf16 v[112:127], a[156:159], v[176:179], v[112:127]\n"
      "ds_read_b128 a[92:95], v203 offset:50176\n"
      "ds_read_b64_tr_b16 a[138:139], v204 offset:5120\n"
      "ds_read_b64_tr_b16 a[140:141], v205 offset:1024\n"
      "v_cmp_lt_f32 vcc, 0x41000000, v194\n"
      "v_mfma_f32_32x32x16_bf16 v[0:15], a[160:163], v[180:183], v[0:15]\n"
      "s_add_u32 m0, s40, 34816\n"
      "ds_read_b128 a[96:99], v200 offset:51200\n"
      "s_cbranch_vccnz LcA_RA30_%=\n"
      "LcA_RRA30_%=:\n"
      "buffer_load_dwordx4 v225, %5, s47 offen lds\n"
      "v_mfma_f32_32x32x16_bf16 v[16:31], a[164:167], v[180:183], v[16:31]\n"
      "ds_read_b128 a[100:103], v201 offset:51200\n"
      "ds_read_b64_tr_b16 a[142:143], v205 offset:5120\n"
      "ds_read_b64_tr_b16 a[144:145], v204 offset:2048\n"
      "v_mfma_f32_32x32x16_bf16 v[32:47], a[168:171], v[180:183], v[32:47]\n"
      "v_exp_f32 v128, v128\n"
      "v_exp_f32 v129, v129\n"
      "v_exp_f32 v130, v130\n"
      "v_mfma_f32_32x32x16_bf16 v[48:63], a[172:175], v[180:183], v[48:63]\n"
      "v_exp_f32 v131, v131\n"
      "v_exp_f32 v132, v132\n"
      "v_exp_f32 v133, v133\n"
      "v_mfma_f32_32x32x16_bf16 v[64:79], a[176:179], v[180:183], v[64:79]\n"
      "v_exp_f32 v134, v134\n"
      "v_exp_f32 v135, v135\n"
      "v_exp_f32 v136, v136\n"
      "v_mfma_f32_32x32x16_bf16 v[80:95], a[180:183], v[180:183], v[80:95]\n"
      "v_exp_f32 v137, v137\n"
      "v_exp_f32 v138, v138\n"
      "v_exp_f32 v139, v139\n"
      "v_mfma_f32_32x32x16_bf16 v[96:111], a[184:187], v[180:183], v[96:111]\n"
      "v_exp_f32 v140, v140\n"
      "v_exp_f32 v141, v141\n"
      "v_exp_f32 v142, v142\n"
      "v_mfma_f32_32x32x16_bf16 v[112:127], a[188:191], v[180:183], v[112:127]\n"
      "v_exp_f32 v143, v143\n"
      "s_waitcnt lgkmcnt(2)\n"
      "s_cmp_lg_u32 s50, 0\n"
      "s_cbranch_scc1 LcA_RB30_%=\n"
      "LcA_RRB30_%=:\n"
      "v_mfma_f32_32x32x16_bf16 v[144:159], a[64:67], a[0:3], v[160:175]\n"
      "s_add_u32 m0, s40, 35840\n"
      "ds_read_b128 a[104:107], v202 offset:51200\n"
      "v_add_f32 v192, v192, v128\n"
      "v_add_f32 v193, v193, v129\n"
      "buffer_load_dwordx4 v226, %5, s47 offen lds\n"
      "s_add_u32 s47, s47, 128\n"
      "s_cmp_eq_u32 s47, 896\n"
      "s_cselect_b32 s47, 0, s47\n"
      "v_mfma_f32_32x32x16_bf16 v[144:159], a[68:71], a[4:7], v[144:159]\n"
      "ds_read_b128 a[108:111], v203 offset:51200\n"
      "ds_read_b64_tr_b16 a[146:147], v204 offset:6144\n"
      "v_add_f32 v192, v192, v130\n"
      "v_cvt_pk_bf16_f32 v176, v128, v129\n"
      "v_mfma_f32_32x32x16_bf16 v[144:159], a[72:75], a[8:11], v[144:159]\n"
      "ds_read_b128 a[112:115], v200 offset:52224\n"
      "ds_read_b64_tr_b16 a[148:149], v205 offset:2048\n"
      "v_add_f32 v193, v193, v131\n"
      "v_cvt_pk_bf16_f32 v177, v130, v131\n"
      "v_mfma_f32_32x32x16_bf16 v[144:159], a[76:79], a[12:15], v[144:159]\n"
      "ds_read_b128 a[116:119], v201 offset:52224\n"
      "ds_read_b64_tr_b16 a[150:151], v205 offset:6144\n"
      "v_add_f32 v192, v192, v132\n"
      "v_cvt_pk_bf16_f32 v178, v132, v133\n"
      "v_mfma_f32_32x32x16_bf16 v[144:159], a[80:83], a[16:19], v[144:159]\n"
      "s_add_u32 m0, s41, 49152\n"
      "ds_read_b128 a[120:123], v202 offset:52224\n"
      "v_add_f32 v193, v193, v133\n"
      "v_cvt_pk_bf16_f32 v179, v134, v135\n"
      "buffer_load_dwordx4 v223, %5, s48 offen lds\n"
      "v_mfma_f32_32x32x16_bf16 v[144:159], a[84:87], a[20:23], v[144:159]\n"
      "ds_read_b128 a[124:127], v203 offset:52224\n"
      "ds_read_b64_tr_b16 a[152:153], v204 offset:3072\n"
      "v_add_f32 v192, v192, v134\n"
      "v_add_f32 v193, v193, v135\n"
      "v_mfma_f32_32x32x16_bf16 v[144:159], a[88:91], a[24:27], v[144:159]\n"
      "ds_read_b64_tr_b16 a[154:155], v204 offset:7168\n"
      "ds_read_b64_tr_b16 a[156:157], v205 offset:3072\n"
      "ds_read_b64_tr_b16 a[158:159], v205 offset:7168\n"
      "v_add_f32 v192, v192, v136\n"
      "v_mfma_f32_32x32x16_bf16 v[144:159], a[92:95], a[28:31], v[144:159]\n"
      "ds_read_b64_tr_b16 a[160:161], v204 offset:8192\n"
      "ds_read_b64_tr_b16 a[162:163], v204 offset:12288\n"
      "ds_read_b64_tr_b16 a[164:165], v205 offset:8192\n"
      "v_add_f32 v193, v193, v137\n"
      "v_mfma_f32_32x32x16_bf16 v[144:159], a[96:99], a[32:35], v[144:159]\n"
      "s_add_u32 m0, s41, 50176\n"
      "ds_read_b64_tr_b16 a[166:167], v205 offset:12288\n"
      "ds_read_b64_tr_b16 a[168:169], v204 offset:9216\n"
      "v_add_f32 v192, v192, v138\n"
      "buffer_load_dwordx4 v224, %5, s48 offen lds\n"
      "v_mfma_f32_32x32x16_bf16 v[144:159], a[100:103], a[36:39], v[144:159]\n"
      "ds_read_b64_tr_b16 a[170:171], v204 offset:13312\n"
      "ds_read_b64_tr_b16 a[172:173], v205 offset:9216\n"
      "ds_read_b64_tr_b16 a[174:175], v205 offset:13312\n"
      "v_add_f32 v193, v193, v139\n"
      "s_waitcnt lgkmcnt(12)\n"
      "v_mfma_f32_32x32x16_bf16 v[144:159], a[104:107], a[40:43], v[144:159]\n"
      "ds_read_b64_tr_b16 a[176:177], v204 offset:10240\n"
      "ds_read_b64_tr_b16 a[178:179], v204 offset:14336\n"
      "ds_read_b64_tr_b16 a[180:181], v205 offset:10240\n"
      "v_add_f32 v192, v192, v140\n"
      "v_mfma_f32_32x32x16_bf16 v[144:159], a[108:111], a[44:47], v[144:159]\n"
      "ds_read_b64_tr_b16 a[182:183], v205 offset:14336\n"
      "ds_read_b64_tr_b16 a[184:185], v204 offset:11264\n"
      "ds_read_b64_tr_b16 a[186:187], v204 offset:15360\n"
      "v_add_f32 v193, v193, v141\n"
      "v_mfma_f32_32x32x16_bf16 v[144:159], a[112:115], a[48:51], v[144:159]\n"
      "s_add_u32 m0, s41, 51200\n"
      "ds_read_b64_tr_b16 a[188:189], v205 offset:11264\n"
      "ds_read_b64_tr_b16 a[190:191], v205 offset:15360\n"
      "buffer_load_dwordx4 v225, %5, s48 offen lds\n"
      "v_mfma_f32_32x32x16_bf16 v[144:159], a[116:119], a[52:55], v[144:159]\n"
      "v_add_f32 v192, v192, v142\n"
      "v_add_f32 v193, v193, v143\n"
      "v_cvt_pk_bf16_f32 v180, v136, v137\n"
      "v_mfma_f32_32x32x16_bf16 v[144:159], a[120:123], a[56:59], v[144:159]\n"
      "s_add_u32 m0, s41, 52224\n"
      "v_cvt_pk_bf16_f32 v181, v138, v139\n"
      "buffer_load_dwordx4 v226, %5, s48 offen lds\n"
      "s_add_u32 s48, s48, 128\n"
      "s_cmp_eq_u32 s48, 115584\n"
      "s_cselect_b32 s48, 114688, s48\n"
      "v_mfma_f32_32x32x16_bf16 v[144:159], a[124:127], a[60:63], v[144:159]\n"
      "s_waitcnt vmcnt(16) lgkmcnt(0)\n"
      "s_barrier\n"
      "v_mfma_f32_32x32x16_bf16 v[0:15], a[128:131], v[176:179], v[0:15]\n"
      "s_add_u32 m0, s40, 49152\n"
      "v_cvt_pk_bf16_f32 v180, v136, v137\n"
      "v_cvt_pk_bf16_f32 v181, v138, v139\n"
      "buffer_load_dwordx4 v223, %5, s47 offen lds\n"
      "v_mfma_f32_32x32x16_bf16 v[16:31], a[132:135], v[176:179], v[16:31]\n"
      "ds_read_b64_tr_b16 a[128:129], v204 offset:16384\n"
      "ds_read_b64_tr_b16 a[130:131], v204 offset:20480\n"
      "v_cvt_pk_bf16_f32 v182, v140, v141\n"
      "v_cvt_pk_bf16_f32 v183, v142, v143\n"
      "v_mfma_f32_32x32x16_bf16 v[32:47], a[136:139], v[176:179], v[32:47]\n"
      "ds_read_b64_tr_b16 a[132:133], v205 offset:16384\n"
      "ds_read_b64_tr_b16 a[134:135], v205 offset:20480\n"
      "v_mfma_f32_32x32x16_bf16 v[48:63], a[140:143], v[176:179], v[48:63]\n"
      "ds_read_b64_tr_b16 a[136:137], v204 offset:17408\n"
      "ds_read_b64_tr_b16 a[138:139], v204 offset:21504\n"
      "v_max3_f32 v194, v144, v145, v146\n"
      "v_max3_f32 v195, v147, v148, v149\n"
      "v_mfma_f32_32x32x16_bf16 v[64:79], a[144:147], v[176:179], v[64:79]\n"
      "s_add_u32 m0, s40, 50176\n"
      "ds_read_b64_tr_b16 a[140:141], v205 offset:17408\n"
      "ds_read_b64_tr_b16 a[142:143], v205 offset:21504\n"
      "v_max3_f32 v194, v194, v150, v151\n"
      "v_max3_f32 v195, v195, v152, v153\n"
      "buffer_load_dwordx4 v224, %5, s47 offen lds\n"
      "v_mfma_f32_32x32x16_bf16 v[80:95], a[148:151], v[176:179], v[80:95]\n"
      "ds_read_b64_tr_b16 a[144:145], v204 offset:18432\n"
      "ds_read_b64_tr_b16 a[146:147], v204 offset:22528\n"
      "v_max3_f32 v194, v194, v154, v155\n"
      "v_max3_f32 v195, v195, v156, v157\n"
      "v_mfma_f32_32x32x16_bf16 v[96:111], a[152:155], v[176:179], v[96:111]\n"
      "ds_read_b64_tr_b16 a[148:149], v205 offset:18432\n"
      "ds_read_b64_tr_b16 a[150:151], v205 offset:22528\n"
      "v_max3_f32 v194, v194, v158, v159\n"
      "v_max_f32 v194, v194, v195\n"
      "v_mfma_f32_32x32x16_bf16 v[112:127], a[156:159], v[176:179], v[112:127]\n"
      "ds_read_b64_tr_b16 a[152:153], v204 offset:19456\n"
      "ds_read_b64_tr_b16 a[154:155], v204 offset:23552\n"
      "v_cmp_lt_f32 vcc, 0x41000000, v194\n"
      "v_mfma_f32_32x32x16_bf16 v[0:15], a[160:163], v[180:183], v[0:15]\n"
      "s_add_u32 m0, s40, 51200\n"
      "ds_read_b64_tr_b16 a[156:157], v205 offset:19456\n"
      "ds_read_b64_tr_b16 a[158:159], v205 offset:23552\n"
      "s_cbranch_vccnz LcA_RA31_%=\n"
      "LcA_RRA31_%=:\n"
      "buffer_load_dwordx4 v225, %5, s47 offen lds\n"
      "v_mfma_f32_32x32x16_bf16 v[16:31], a[164:167], v[180:183], v[16:31]\n"
      "ds_read_b64_tr_b16 a[160:161], v204 offset:24576\n"
      "ds_read_b64_tr_b16 a[162:163], v204 offset:28672\n"
      "v_mfma_f32_32x32x16_bf16 v[32:47], a[168:171], v[180:183], v[32:47]\n"
      "v_exp_f32 v144, v144\n"
      "v_exp_f32 v145, v145\n"
      "v_exp_f32 v146, v146\n"
      "v_mfma_f32_32x32x16_bf16 v[48:63], a[172:175], v[180:183], v[48:63]\n"
      "v_exp_f32 v147, v147\n"
      "v_exp_f32 v148, v148\n"
      "v_exp_f32 v149, v149\n"
      "v_mfma_f32_32x32x16_bf16 v[64:79], a[176:179], v[180:183], v[64:79]\n"
      "v_exp_f32 v150, v150\n"
      "v_exp_f32 v151, v151\n"
      "v_exp_f32 v152, v152\n"
      "v_mfma_f32_32x32x16_bf16 v[80:95], a[180:183], v[180:183], v[80:95]\n"
      "v_exp_f32 v153, v153\n"
      "v_exp_f32 v154, v154\n"
      "v_exp_f32 v155, v155\n"
      "v_mfma_f32_32x32x16_bf16 v[96:111], a[184:187], v[180:183], v[96:111]\n"
      "v_exp_f32 v156, v156\n"
      "v_exp_f32 v157, v157\n"
      "v_exp_f32 v158, v158\n"
      "v_mfma_f32_32x32x16_bf16 v[112:127], a[188:191], v[180:183], v[112:127]\n"
      "v_exp_f32 v159, v159\n"
      "v_add_f32 v192, v192, v144\n"
      "v_add_f32 v193, v193, v145\n"
      "s_cmp_lg_u32 s50, 0\n"
      "s_cbranch_scc1 LcA_RB31_%=\n"
      "LcA_RRB31_%=:\n"
      "s_add_u32 m0, s40, 52224\n"
      "ds_read_b64_tr_b16 a[164:165], v205 offset:24576\n"
      "ds_read_b64_tr_b16 a[166:167], v205 offset:28672\n"
      "ds_read_b64_tr_b16 a[168:169], v204 offset:25600\n"
      "ds_read_b64_tr_b16 a[170:171], v204 offset:29696\n"
      "ds_read_b64_tr_b16 a[172:173], v205 offset:25600\n"
      "ds_read_b64_tr_b16 a[174:175], v205 offset:29696\n"
      "ds_read_b64_tr_b16 a[176:177], v204 offset:26624\n"
      "ds_read_b64_tr_b16 a[178:179], v204 offset:30720\n"
      "ds_read_b64_tr_b16 a[180:181], v205 offset:26624\n"
      "ds_read_b64_tr_b16 a[182:183], v205 offset:30720\n"
      "ds_read_b64_tr_b16 a[184:185], v204 offset:27648\n"
      "ds_read_b64_tr_b16 a[186:187], v204 offset:31744\n"
      "ds_read_b64_tr_b16 a[188:189], v205 offset:27648\n"
      "ds_read_b64_tr_b16 a[190:191], v205 offset:31744\n"
      "buffer_load_dwordx4 v226, %5, s47 offen lds\n"
      "s_add_u32 s47, s47, 128\n"
      "s_cmp_eq_u32 s47, 896\n"
      "s_cselect_b32 s47, 0, s47\n"
      "v_cvt_pk_bf16_f32 v176, v144, v145\n"
      "v_cvt_pk_bf16_f32 v177, v146, v147\n"
      "v_cvt_pk_bf16_f32 v178, v148, v149\n"
      "v_cvt_pk_bf16_f32 v179, v150, v151\n"
      "v_add_f32 v192, v192, v146\n"
      "v_add_f32 v193, v193, v147\n"
      "v_add_f32 v192, v192, v148\n"
      "v_add_f32 v193, v193, v149\n"
      "v_add_f32 v192, v192, v150\n"
      "v_add_f32 v193, v193, v151\n"
      "v_add_f32 v192, v192, v152\n"
      "v_add_f32 v193, v193, v153\n"
      "v_add_f32 v192, v192, v154\n"
      "v_add_f32 v193, v193, v155\n"
      "v_add_f32 v192, v192, v156\n"
      "v_add_f32 v193, v193, v157\n"
      "v_add_f32 v192, v192, v158\n"
      "v_add_f32 v193, v193, v159\n"
      "s_add_u32 m0, s41, 0\n"
      "s_nop 0\n"
      "buffer_load_dwordx4 v223, %5, s48 offen lds\n"
      "s_add_u32 m0, s41, 1024\n"
      "s_nop 0\n"
      "buffer_load_dwordx4 v224, %5, s48 offen lds\n"
      "s_add_u32 m0, s41, 2048\n"
      "s_nop 0\n"
      "buffer_load_dwordx4 v225, %5, s48 offen lds\n"
      "s_add_u32 m0, s41, 3072\n"
      "s_nop 0\n"
      "buffer_load_dwordx4 v226, %5, s48 offen lds\n"
      "s_add_u32 s48, s48, 128\n"
      "s_cmp_eq_u32 s48, 115584\n"
      "s_cselect_b32 s48, 114688, s48\n"
      "v_cvt_pk_bf16_f32 v180, v152, v153\n"
      "v_cvt_pk_bf16_f32 v181, v154, v155\n"
      "v_cvt_pk_bf16_f32 v182, v156, v157\n"
      "v_cvt_pk_bf16_f32 v183, v158, v159\n"
      "s_waitcnt vmcnt(24) lgkmcnt(0)\n"
      "s_barrier\n"
      "v_mfma_f32_32x32x16_bf16 v[0:15], a[128:131], v[176:179], v[0:15]\n"
      "s_add_u32 m0, s41, 16384\n"
      "ds_read_b64_tr_b16 a[64:65], v212 offset:0\n"
      "buffer_load_dwordx4 v223, %5, s48 offen lds\n"
      "ds_read_b64_tr_b16 a[72:73], v212 offset:2048\n"
      "ds_read_b64_tr_b16 a[74:75], v212 offset:3072\n"
      "ds_read_b64_tr_b16 a[76:77], v213 offset:2048\n"
      "v_mfma_f32_32x32x16_bf16 v[0:15], a[160:163], v[180:183], v[0:15]\n"
      "ds_read_b64_tr_b16 a[78:79], v213 offset:3072\n"
      "ds_read_b64_tr_b16 a[80:81], v212 offset:4096\n"
      "v_add_f32 v192, v192, v193\n"
      "v_mov_b32 v196, v192\n"
      "v_mfma_f32_32x32x16_bf16 v[16:31], a[132:135], v[176:179], v[16:31]\n"
      "s_add_u32 m0, s41, 17408\n"
      "ds_read_b64_tr_b16 a[66:67], v212 offset:1024\n"
      "buffer_load_dwordx4 v224, %5, s48 offen lds\n"
      "ds_read_b64_tr_b16 a[82:83], v212 offset:5120\n"
      "ds_read_b64_tr_b16 a[84:85], v213 offset:4096\n"
      "v_mfma_f32_32x32x16_bf16 v[16:31], a[164:167], v[180:183], v[16:31]\n"
      "ds_read_b64_tr_b16 a[86:87], v213 offset:5120\n"
      "ds_read_b64_tr_b16 a[88:89], v212 offset:6144\n"
      "v_permlane32_swap_b32 v196, v192\n"
      "v_add_f32 v192, v192, v196\n"
      "v_rcp_f32 v198, v192\n"
      "v_mfma_f32_32x32x16_bf16 v[32:47], a[136:139], v[176:179], v[32:47]\n"
      "s_add_u32 m0, s41, 18432\n"
      "ds_read_b64_tr_b16 a[68:69], v213 offset:0\n"
      "buffer_load_dwordx4 v225, %5, s48 offen lds\n"
      "ds_read_b64_tr_b16 a[90:91], v212 offset:7168\n"
      "ds_read_b64_tr_b16 a[92:93], v213 offset:6144\n"
      "ds_read_b64_tr_b16 a[94:95], v213 offset:7168\n"
      "v_mfma_f32_32x32x16_bf16 v[32:47], a[168:171], v[180:183], v[32:47]\n"
      "ds_read_b64_tr_b16 a[96:97], v212 offset:8192\n"
      "ds_read_b64_tr_b16 a[98:99], v212 offset:9216\n"
      "v_pk_mul_f32 v[0:1], v[0:1], v[198:199] op_sel_hi:[1,0]\n"
      "v_pk_mul_f32 v[2:3], v[2:3], v[198:199] op_sel_hi:[1,0]\n"
      "v_pk_mul_f32 v[4:5], v[4:5], v[198:199] op_sel_hi:[1,0]\n"
      "v_pk_mul_f32 v[6:7], v[6:7], v[198:199] op_sel_hi:[1,0]\n"
      "v_cvt_pk_bf16_f32 v128, v0, v1\n"
      "v_cvt_pk_bf16_f32 v129, v2, v3\n"
      "v_mfma_f32_32x32x16_bf16 v[48:63], a[140:143], v[176:179], v[48:63]\n"
      "s_add_u32 m0, s41, 19456\n"
      "ds_read_b64_tr_b16 a[70:71], v213 offset:1024\n"
      "buffer_load_dwordx4 v226, %5, s48 offen lds\n"
      "s_add_u32 s48, s48, 128\n"
      "s_cmp_eq_u32 s48, 115584\n"
      "s_cselect_b32 s48, 114688, s48\n"
      "ds_read_b64_tr_b16 a[100:101], v213 offset:8192\n"
      "ds_read_b64_tr_b16 a[102:103], v213 offset:9216\n"
      "v_cvt_pk_bf16_f32 v130, v4, v5\n"
      "v_cvt_pk_bf16_f32 v131, v6, v7\n"
      "v_pk_mul_f32 v[8:9], v[8:9], v[198:199] op_sel_hi:[1,0]\n"
      "v_pk_mul_f32 v[10:11], v[10:11], v[198:199] op_sel_hi:[1,0]\n"
      "v_pk_mul_f32 v[12:13], v[12:13], v[198:199] op_sel_hi:[1,0]\n"
      "v_mfma_f32_32x32x16_bf16 v[48:63], a[172:175], v[180:183], v[48:63]\n"
      "ds_read_b64_tr_b16 a[104:105], v212 offset:10240\n"
      "ds_read_b64_tr_b16 a[106:107], v212 offset:11264\n"
      "v_pk_mul_f32 v[14:15], v[14:15], v[198:199] op_sel_hi:[1,0]\n"
      "v_cvt_pk_bf16_f32 v132, v8, v9\n"
      "v_cvt_pk_bf16_f32 v133, v10, v11\n"
      "v_cvt_pk_bf16_f32 v134, v12, v13\n"
      "v_cvt_pk_bf16_f32 v135, v14, v15\n"
      "v_mfma_f32_32x32x16_bf16 v[64:79], a[144:147], v[176:179], v[64:79]\n"
      "ds_read_b64_tr_b16 a[108:109], v213 offset:10240\n"
      "ds_read_b64_tr_b16 a[110:111], v213 offset:11264\n"
      "ds_read_b64_tr_b16 a[112:113], v212 offset:12288\n"
      "v_pk_mul_f32 v[16:17], v[16:17], v[198:199] op_sel_hi:[1,0]\n"
      "v_pk_mul_f32 v[18:19], v[18:19], v[198:199] op_sel_hi:[1,0]\n"
      "v_pk_mul_f32 v[20:21], v[20:21], v[198:199] op_sel_hi:[1,0]\n"
      "v_pk_mul_f32 v[22:23], v[22:23], v[198:199] op_sel_hi:[1,0]\n"
      "v_cvt_pk_bf16_f32 v136, v16, v17\n"
      "v_cvt_pk_bf16_f32 v137, v18, v19\n"
      "v_mfma_f32_32x32x16_bf16 v[64:79], a[176:179], v[180:183], v[64:79]\n"
      "ds_read_b64_tr_b16 a[114:115], v212 offset:13312\n"
      "ds_read_b64_tr_b16 a[116:117], v213 offset:12288\n"
      "v_cvt_pk_bf16_f32 v138, v20, v21\n"
      "v_cvt_pk_bf16_f32 v139, v22, v23\n"
      "v_pk_mul_f32 v[24:25], v[24:25], v[198:199] op_sel_hi:[1,0]\n"
      "v_pk_mul_f32 v[26:27], v[26:27], v[198:199] op_sel_hi:[1,0]\n"
      "v_pk_mul_f32 v[28:29], v[28:29], v[198:199] op_sel_hi:[1,0]\n"
      "v_mfma_f32_32x32x16_bf16 v[80:95], a[148:151], v[176:179], v[80:95]\n"
      "ds_read_b64_tr_b16 a[118:119], v213 offset:13312\n"
      "ds_read_b64_tr_b16 a[120:121], v212 offset:14336\n"
      "v_pk_mul_f32 v[30:31], v[30:31], v[198:199] op_sel_hi:[1,0]\n"
      "v_cvt_pk_bf16_f32 v140, v24, v25\n"
      "v_cvt_pk_bf16_f32 v141, v26, v27\n"
      "v_cvt_pk_bf16_f32 v142, v28, v29\n"
      "v_cvt_pk_bf16_f32 v143, v30, v31\n"
      "v_mfma_f32_32x32x16_bf16 v[80:95], a[180:183], v[180:183], v[80:95]\n"
      "ds_read_b64_tr_b16 a[122:123], v212 offset:15360\n"
      "ds_read_b64_tr_b16 a[124:125], v213 offset:14336\n"
      "v_pk_mul_f32 v[32:33], v[32:33], v[198:199] op_sel_hi:[1,0]\n"
      "v_pk_mul_f32 v[34:35], v[34:35], v[198:199] op_sel_hi:[1,0]\n"
      "v_pk_mul_f32 v[36:37], v[36:37], v[198:199] op_sel_hi:[1,0]\n"
      "v_pk_mul_f32 v[38:39], v[38:39], v[198:199] op_sel_hi:[1,0]\n"
      "v_cvt_pk_bf16_f32 v144, v32, v33\n"
      "v_cvt_pk_bf16_f32 v145, v34, v35\n"
      "v_mfma_f32_32x32x16_bf16 v[96:111], a[152:155], v[176:179], v[96:111]\n"
      "ds_read_b64_tr_b16 a[126:127], v213 offset:15360\n"
      "v_cvt_pk_bf16_f32 v146, v36, v37\n"
      "v_cvt_pk_bf16_f32 v147, v38, v39\n"
      "v_pk_mul_f32 v[40:41], v[40:41], v[198:199] op_sel_hi:[1,0]\n"
      "v_pk_mul_f32 v[42:43], v[42:43], v[198:199] op_sel_hi:[1,0]\n"
      "v_pk_mul_f32 v[44:45], v[44:45], v[198:199] op_sel_hi:[1,0]\n"
      "v_mfma_f32_32x32x16_bf16 v[96:111], a[184:187], v[180:183], v[96:111]\n"
      "v_pk_mul_f32 v[46:47], v[46:47], v[198:199] op_sel_hi:[1,0]\n"
      "v_cvt_pk_bf16_f32 v148, v40, v41\n"
      "v_cvt_pk_bf16_f32 v149, v42, v43\n"
      "v_cvt_pk_bf16_f32 v150, v44, v45\n"
      "v_cvt_pk_bf16_f32 v151, v46, v47\n"
      "v_mfma_f32_32x32x16_bf16 v[112:127], a[156:159], v[176:179], v[112:127]\n"
      "v_pk_mul_f32 v[48:49], v[48:49], v[198:199] op_sel_hi:[1,0]\n"
      "v_pk_mul_f32 v[50:51], v[50:51], v[198:199] op_sel_hi:[1,0]\n"
      "v_pk_mul_f32 v[52:53], v[52:53], v[198:199] op_sel_hi:[1,0]\n"
      "v_pk_mul_f32 v[54:55], v[54:55], v[198:199] op_sel_hi:[1,0]\n"
      "v_cvt_pk_bf16_f32 v152, v48, v49\n"
      "v_cvt_pk_bf16_f32 v153, v50, v51\n"
      "v_cvt_pk_bf16_f32 v154, v52, v53\n"
      "v_cvt_pk_bf16_f32 v155, v54, v55\n"
      "v_mfma_f32_32x32x16_bf16 v[112:127], a[188:191], v[180:183], v[112:127]\n"
      "v_pk_mul_f32 v[56:57], v[56:57], v[198:199] op_sel_hi:[1,0]\n"
      "v_pk_mul_f32 v[58:59], v[58:59], v[198:199] op_sel_hi:[1,0]\n"
      "v_pk_mul_f32 v[60:61], v[60:61], v[198:199] op_sel_hi:[1,0]\n"
      "v_pk_mul_f32 v[62:63], v[62:63], v[198:199] op_sel_hi:[1,0]\n"
      "v_cvt_pk_bf16_f32 v156, v56, v57\n"
      "v_cvt_pk_bf16_f32 v157, v58, v59\n"
      "v_cvt_pk_bf16_f32 v158, v60, v61\n"
      "v_cvt_pk_bf16_f32 v159, v62, v63\n"
      "s_waitcnt vmcnt(20) lgkmcnt(0)\n"
      "s_barrier\n"
      "v_mfma_f32_32x32x16_bf16 v[0:15], a[64:67], v[128:131], 0\n"
      "ds_read_b64_tr_b16 a[128:129], v214 offset:32768\n"
      "ds_read_b64_tr_b16 a[130:131], v214 offset:33792\n"
      "ds_read_b64_tr_b16 a[132:133], v215 offset:32768\n"
      "v_pk_mul_f32 v[64:65], v[64:65], v[198:199] op_sel_hi:[1,0]\n"
      "v_pk_mul_f32 v[66:67], v[66:67], v[198:199] op_sel_hi:[1,0]\n"
      "v_pk_mul_f32 v[68:69], v[68:69], v[198:199] op_sel_hi:[1,0]\n"
      "v_pk_mul_f32 v[70:71], v[70:71], v[198:199] op_sel_hi:[1,0]\n"
      "v_mfma_f32_32x32x16_bf16 v[16:31], a[68:71], v[128:131], 0\n"
      "ds_read_b64_tr_b16 a[134:135], v215 offset:33792\n"
      "ds_read_b64_tr_b16 a[136:137], v214 offset:34816\n"
      "ds_read_b64_tr_b16 a[138:139], v214 offset:35840\n"
      "v_cvt_pk_bf16_f32 v160, v64, v65\n"
      "v_cvt_pk_bf16_f32 v161, v66, v67\n"
      "v_cvt_pk_bf16_f32 v162, v68, v69\n"
      "v_cvt_pk_bf16_f32 v163, v70, v71\n"
      "v_mfma_f32_32x32x16_bf16 v[0:15], a[72:75], v[132:135], v[0:15]\n"
      "ds_read_b64_tr_b16 a[140:141], v215 offset:34816\n"
      "ds_read_b64_tr_b16 a[142:143], v215 offset:35840\n"
      "v_pk_mul_f32 v[72:73], v[72:73], v[198:199] op_sel_hi:[1,0]\n"
      "v_pk_mul_f32 v[74:75], v[74:75], v[198:199] op_sel_hi:[1,0]\n"
      "v_pk_mul_f32 v[76:77], v[76:77], v[198:199] op_sel_hi:[1,0]\n"
      "v_pk_mul_f32 v[78:79], v[78:79], v[198:199] op_sel_hi:[1,0]\n"
      "v_mfma_f32_32x32x16_bf16 v[16:31], a[76:79], v[132:135], v[16:31]\n"
      "ds_read_b64_tr_b16 a[144:145], v214 offset:36864\n"
      "ds_read_b64_tr_b16 a[146:147], v214 offset:37888\n"
      "ds_read_b64_tr_b16 a[148:149], v215 offset:36864\n"
      "v_cvt_pk_bf16_f32 v164, v72, v73\n"
      "v_cvt_pk_bf16_f32 v165, v74, v75\n"
      "v_cvt_pk_bf16_f32 v166, v76, v77\n"
      "v_cvt_pk_bf16_f32 v167, v78, v79\n"
      "v_mfma_f32_32x32x16_bf16 v[0:15], a[80:83], v[136:139], v[0:15]\n"
      "ds_read_b64_tr_b16 a[150:151], v215 offset:37888\n"
      "ds_read_b64_tr_b16 a[152:153], v214 offset:38912\n"
      "v_pk_mul_f32 v[80:81], v[80:81], v[198:199] op_sel_hi:[1,0]\n"
      "v_pk_mul_f32 v[82:83], v[82:83], v[198:199] op_sel_hi:[1,0]\n"
      "v_pk_mul_f32 v[84:85], v[84:85], v[198:199] op_sel_hi:[1,0]\n"
      "v_pk_mul_f32 v[86:87], v[86:87], v[198:199] op_sel_hi:[1,0]\n"
      "v_mfma_f32_32x32x16_bf16 v[16:31], a[84:87], v[136:139], v[16:31]\n"
      "ds_read_b64_tr_b16 a[154:155], v214 offset:39936\n"
      "ds_read_b64_tr_b16 a[156:157], v215 offset:38912\n"
      "ds_read_b64_tr_b16 a[158:159], v215 offset:39936\n"
      "v_cvt_pk_bf16_f32 v168, v80, v81\n"
      "v_cvt_pk_bf16_f32 v169, v82, v83\n"
      "v_cvt_pk_bf16_f32 v170, v84, v85\n"
      "v_cvt_pk_bf16_f32 v171, v86, v87\n"
      "v_mfma_f32_32x32x16_bf16 v[0:15], a[88:91], v[140:143], v[0:15]\n"
      "ds_read_b64_tr_b16 a[160:161], v214 offset:40960\n"
      "ds_read_b64_tr_b16 a[162:163], v214 offset:41984\n"
      "v_pk_mul_f32 v[88:89], v[88:89], v[198:199] op_sel_hi:[1,0]\n"
      "v_pk_mul_f32 v[90:91], v[90:91], v[198:199] op_sel_hi:[1,0]\n"
      "v_pk_mul_f32 v[92:93], v[92:93], v[198:199] op_sel_hi:[1,0]\n"
      "v_pk_mul_f32 v[94:95], v[94:95], v[198:199] op_sel_hi:[1,0]\n"
      "v_mfma_f32_32x32x16_bf16 v[16:31], a[92:95], v[140:143], v[16:31]\n"
      "ds_read_b64_tr_b16 a[164:165], v215 offset:40960\n"
      "ds_read_b64_tr_b16 a[166:167], v215 offset:41984\n"
      "ds_read_b64_tr_b16 a[168:169], v214 offset:43008\n"
      "v_cvt_pk_bf16_f32 v172, v88, v89\n"
      "v_cvt_pk_bf16_f32 v173, v90, v91\n"
      "v_cvt_pk_bf16_f32 v174, v92, v93\n"
      "v_cvt_pk_bf16_f32 v175, v94, v95\n"
      "v_mfma_f32_32x32x16_bf16 v[0:15], a[96:99], v[144:147], v[0:15]\n"
      "ds_read_b64_tr_b16 a[170:171], v214 offset:44032\n"
      "ds_read_b64_tr_b16 a[172:173], v215 offset:43008\n"
      "v_pk_mul_f32 v[96:97], v[96:97], v[198:199] op_sel_hi:[1,0]\n"
      "v_pk_mul_f32 v[98:99], v[98:99], v[198:199] op_sel_hi:[1,0]\n"
      "v_pk_mul_f32 v[100:101], v[100:101], v[198:199] op_sel_hi:[1,0]\n"
      "v_pk_mul_f32 v[102:103], v[102:103], v[198:199] op_sel_hi:[1,0]\n"
      "v_mfma_f32_32x32x16_bf16 v[16:31], a[100:103], v[144:147], v[16:31]\n"
      "ds_read_b64_tr_b16 a[174:175], v215 offset:44032\n"
      "ds_read_b64_tr_b16 a[176:177], v214 offset:45056\n"
      "ds_read_b64_tr_b16 a[178:179], v214 offset:46080\n"
      "v_cvt_pk_bf16_f32 v176, v96, v97\n"
      "v_cvt_pk_bf16_f32 v177, v98, v99\n"
      "v_cvt_pk_bf16_f32 v178, v100, v101\n"
      "v_cvt_pk_bf16_f32 v179, v102, v103\n"
      "v_mfma_f32_32x32x16_bf16 v[0:15], a[104:107], v[148:151], v[0:15]\n"
      "ds_read_b64_tr_b16 a[180:181], v215 offset:45056\n"
      "ds_read_b64_tr_b16 a[182:183], v215 offset:46080\n"
      "v_pk_mul_f32 v[104:105], v[104:105], v[198:199] op_sel_hi:[1,0]\n"
      "v_pk_mul_f32 v[106:107], v[106:107], v[198:199] op_sel_hi:[1,0]\n"
      "v_pk_mul_f32 v[108:109], v[108:109], v[198:199] op_sel_hi:[1,0]\n"
      "v_pk_mul_f32 v[110:111], v[110:111], v[198:199] op_sel_hi:[1,0]\n"
      "v_mfma_f32_32x32x16_bf16 v[16:31], a[108:111], v[148:151], v[16:31]\n"
      "ds_read_b64_tr_b16 a[184:185], v214 offset:47104\n"
      "ds_read_b64_tr_b16 a[186:187], v214 offset:48128\n"
      "ds_read_b64_tr_b16 a[188:189], v215 offset:47104\n"
      "v_cvt_pk_bf16_f32 v180, v104, v105\n"
      "v_cvt_pk_bf16_f32 v181, v106, v107\n"
      "v_cvt_pk_bf16_f32 v182, v108, v109\n"
      "v_cvt_pk_bf16_f32 v183, v110, v111\n"
      "v_mfma_f32_32x32x16_bf16 v[0:15], a[112:115], v[152:155], v[0:15]\n"
      "ds_read_b64_tr_b16 a[190:191], v215 offset:48128\n"
      "v_pk_mul_f32 v[112:113], v[112:113], v[198:199] op_sel_hi:[1,0]\n"
      "v_pk_mul_f32 v[114:115], v[114:115], v[198:199] op_sel_hi:[1,0]\n"
      "v_pk_mul_f32 v[116:117], v[116:117], v[198:199] op_sel_hi:[1,0]\n"
      "v_pk_mul_f32 v[118:119], v[118:119], v[198:199] op_sel_hi:[1,0]\n"
      "v_mfma_f32_32x32x16_bf16 v[16:31], a[116:119], v[152:155], v[16:31]\n"
      "v_cvt_pk_bf16_f32 v184, v112, v113\n"
      "v_cvt_pk_bf16_f32 v185, v114, v115\n"
      "v_cvt_pk_bf16_f32 v186, v116, v117\n"
      "v_cvt_pk_bf16_f32 v187, v118, v119\n"
      "v_mfma_f32_32x32x16_bf16 v[0:15], a[120:123], v[156:159], v[0:15]\n"
      "v_pk_mul_f32 v[120:121], v[120:121], v[198:199] op_sel_hi:[1,0]\n"
      "v_pk_mul_f32 v[122:123], v[122:123], v[198:199] op_sel_hi:[1,0]\n"
      "v_pk_mul_f32 v[124:125], v[124:125], v[198:199] op_sel_hi:[1,0]\n"
      "v_pk_mul_f32 v[126:127], v[126:127], v[198:199] op_sel_hi:[1,0]\n"
      "v_mfma_f32_32x32x16_bf16 v[16:31], a[124:127], v[156:159], v[16:31]\n"
      "v_cvt_pk_bf16_f32 v188, v120, v121\n"
      "v_cvt_pk_bf16_f32 v189, v122, v123\n"
      "v_cvt_pk_bf16_f32 v190, v124, v125\n"
      "v_cvt_pk_bf16_f32 v191, v126, v127\n"
      "s_waitcnt lgkmcnt(0)\n"
      "v_mfma_f32_32x32x16_bf16 v[0:15], a[128:131], v[160:163], v[0:15]\n"
      "s_add_u32 m0, s40, 0\n"
      "ds_read_b64_tr_b16 a[64:65], v212 offset:16384\n"
      "buffer_load_dwordx4 v223, %5, s47 offen lds\n"
      "ds_read_b64_tr_b16 a[72:73], v212 offset:18432\n"
      "ds_read_b64_tr_b16 a[74:75], v212 offset:19456\n"
      "ds_read_b64_tr_b16 a[76:77], v213 offset:18432\n"
      "v_mfma_f32_32x32x16_bf16 v[16:31], a[132:135], v[160:163], v[16:31]\n"
      "ds_read_b64_tr_b16 a[78:79], v213 offset:19456\n"
      "ds_read_b64_tr_b16 a[80:81], v212 offset:20480\n"
      "v_mfma_f32_32x32x16_bf16 v[0:15], a[136:139], v[164:167], v[0:15]\n"
      "ds_read_b64_tr_b16 a[82:83], v212 offset:21504\n"
      "ds_read_b64_tr_b16 a[84:85], v213 offset:20480\n"
      "v_mfma_f32_32x32x16_bf16 v[16:31], a[140:143], v[164:167], v[16:31]\n"
      "ds_read_b64_tr_b16 a[86:87], v213 offset:21504\n"
      "ds_read_b64_tr_b16 a[88:89], v212 offset:22528\n"
      "v_mfma_f32_32x32x16_bf16 v[0:15], a[144:147], v[168:171], v[0:15]\n"
      "s_add_u32 m0, s40, 1024\n"
      "ds_read_b64_tr_b16 a[66:67], v212 offset:17408\n"
      "buffer_load_dwordx4 v224, %5, s47 offen lds\n"
      "ds_read_b64_tr_b16 a[90:91], v212 offset:23552\n"
      "ds_read_b64_tr_b16 a[92:93], v213 offset:22528\n"
      "ds_read_b64_tr_b16 a[94:95], v213 offset:23552\n"
      "v_mfma_f32_32x32x16_bf16 v[16:31], a[148:151], v[168:171], v[16:31]\n"
      "ds_read_b64_tr_b16 a[96:97], v212 offset:24576\n"
      "ds_read_b64_tr_b16 a[98:99], v212 offset:25600\n"
      "v_mfma_f32_32x32x16_bf16 v[0:15], a[152:155], v[172:175], v[0:15]\n"
      "ds_read_b64_tr_b16 a[100:101], v213 offset:24576\n"
      "ds_read_b64_tr_b16 a[102:103], v213 offset:25600\n"
      "v_mfma_f32_32x32x16_bf16 v[16:31], a[156:159], v[172:175], v[16:31]\n"
      "ds_read_b64_tr_b16 a[104:105], v212 offset:26624\n"
      "ds_read_b64_tr_b16 a[106:107], v212 offset:27648\n"
      "v_mfma_f32_32x32x16_bf16 v[0:15], a[160:163], v[176:179], v[0:15]\n"
      "s_add_u32 m0, s40, 2048\n"
      "ds_read_b64_tr_b16 a[68:69], v213 offset:16384\n"
      "buffer_load_dwordx4 v225, %5, s47 offen lds\n"
      "ds_read_b64_tr_b16 a[108:109], v213 offset:26624\n"
      "ds_read_b64_tr_b16 a[110:111], v213 offset:27648\n"
      "ds_read_b64_tr_b16 a[112:113], v212 offset:28672\n"
      "v_mfma_f32_32x32x16_bf16 v[16:31], a[164:167], v[176:179], v[16:31]\n"
      "ds_read_b64_tr_b16 a[114:115], v212 offset:29696\n"
      "ds_read_b64_tr_b16 a[116:117], v213 offset:28672\n"
      "v_mfma_f32_32x32x16_bf16 v[0:15], a[168:171], v[180:183], v[0:15]\n"
      "ds_read_b64_tr_b16 a[118:119], v213 offset:29696\n"
      "ds_read_b64_tr_b16 a[120:121], v212 offset:30720\n"
      "v_mfma_f32_32x32x16_bf16 v[16:31], a[172:175], v[180:183], v[16:31]\n"
      "ds_read_b64_tr_b16 a[122:123], v212 offset:31744\n"
      "ds_read_b64_tr_b16 a[124:125], v213 offset:30720\n"
      "v_mfma_f32_32x32x16_bf16 v[0:15], a[176:179], v[184:187], v[0:15]\n"
      "s_add_u32 m0, s40, 3072\n"
      "ds_read_b64_tr_b16 a[70:71], v213 offset:17408\n"
      "buffer_load_dwordx4 v226, %5, s47 offen lds\n"
      "s_add_u32 s47, s47, 128\n"
      "s_cmp_eq_u32 s47, 896\n"
      "s_cselect_b32 s47, 0, s47\n"
      "ds_read_b64_tr_b16 a[126:127], v213 offset:31744\n"
      "v_mfma_f32_32x32x16_bf16 v[16:31], a[180:183], v[184:187], v[16:31]\n"
      "v_mfma_f32_32x32x16_bf16 v[0:15], a[184:187], v[188:191], v[0:15]\n"
      "v_mfma_f32_32x32x16_bf16 v[16:31], a[188:191], v[188:191], v[16:31]\n"
      "s_waitcnt vmcnt(16) lgkmcnt(0)\n"
      "s_barrier\n"
      "v_mfma_f32_32x32x16_bf16 v[32:47], a[64:67], v[128:131], 0\n"
      "ds_read_b64_tr_b16 a[128:129], v214 offset:49152\n"
      "ds_read_b64_tr_b16 a[130:131], v214 offset:50176\n"
      "ds_read_b64_tr_b16 a[132:133], v215 offset:49152\n"
      "v_mfma_f32_32x32x16_bf16 v[48:63], a[68:71], v[128:131], 0\n"
      "ds_read_b64_tr_b16 a[134:135], v215 offset:50176\n"
      "ds_read_b64_tr_b16 a[136:137], v214 offset:51200\n"
      "ds_read_b64_tr_b16 a[138:139], v214 offset:52224\n"
      "v_mfma_f32_32x32x16_bf16 v[32:47], a[72:75], v[132:135], v[32:47]\n"
      "ds_read_b64_tr_b16 a[140:141], v215 offset:51200\n"
      "ds_read_b64_tr_b16 a[142:143], v215 offset:52224\n"
      "v_mfma_f32_32x32x16_bf16 v[48:63], a[76:79], v[132:135], v[48:63]\n"
      "ds_read_b64_tr_b16 a[144:145], v214 offset:53248\n"
      "ds_read_b64_tr_b16 a[146:147], v214 offset:54272\n"
      "ds_read_b64_tr_b16 a[148:149], v215 offset:53248\n"
      "v_mfma_f32_32x32x16_bf16 v[32:47], a[80:83], v[136:139], v[32:47]\n"
      "ds_read_b64_tr_b16 a[150:151], v215 offset:54272\n"
      "ds_read_b64_tr_b16 a[152:153], v214 offset:55296\n"
      "v_cvt_pk_bf16_f32 v192, v0, v1\n"
      "v_cvt_pk_bf16_f32 v193, v2, v3\n"
      "v_mfma_f32_32x32x16_bf16 v[48:63], a[84:87], v[136:139], v[48:63]\n"
      "ds_read_b64_tr_b16 a[154:155], v214 offset:56320\n"
      "ds_read_b64_tr_b16 a[156:157], v215 offset:55296\n"
      "ds_read_b64_tr_b16 a[158:159], v215 offset:56320\n"
      "v_cvt_pk_bf16_f32 v194, v4, v5\n"
      "v_cvt_pk_bf16_f32 v195, v6, v7\n"
      "v_mfma_f32_32x32x16_bf16 v[32:47], a[88:91], v[140:143], v[32:47]\n"
      "ds_read_b64_tr_b16 a[160:161], v214 offset:57344\n"
      "ds_read_b64_tr_b16 a[162:163], v214 offset:58368\n"
      "buffer_store_dwordx4 v[192:195], v227, %6, s49 offen offset:0 sc1\n"
      "v_cvt_pk_bf16_f32 v196, v8, v9\n"
      "v_mfma_f32_32x32x16_bf16 v[48:63], a[92:95], v[140:143], v[48:63]\n"
      "ds_read_b64_tr_b16 a[164:165], v215 offset:57344\n"
      "ds_read_b64_tr_b16 a[166:167], v215 offset:58368\n"
      "ds_read_b64_tr_b16 a[168:169], v214 offset:59392\n"
      "v_cvt_pk_bf16_f32 v197, v10, v11\n"
      "v_cvt_pk_bf16_f32 v198, v12, v13\n"
      "v_mfma_f32_32x32x16_bf16 v[32:47], a[96:99], v[144:147], v[32:47]\n"
      "ds_read_b64_tr_b16 a[170:171], v214 offset:60416\n"
      "ds_read_b64_tr_b16 a[172:173], v215 offset:59392\n"
      "v_cvt_pk_bf16_f32 v199, v14, v15\n"
      "buffer_store_dwordx4 v[196:199], v227, %6, s49 offen offset:1024 sc1\n"
      "v_mfma_f32_32x32x16_bf16 v[48:63], a[100:103], v[144:147], v[48:63]\n"
      "ds_read_b64_tr_b16 a[174:175], v215 offset:60416\n"
      "ds_read_b64_tr_b16 a[176:177], v214 offset:61440\n"
      "ds_read_b64_tr_b16 a[178:179], v214 offset:62464\n"
      "v_cvt_pk_bf16_f32 v200, v16, v17\n"
      "v_cvt_pk_bf16_f32 v201, v18, v19\n"
      "v_mfma_f32_32x32x16_bf16 v[32:47], a[104:107], v[148:151], v[32:47]\n"
      "ds_read_b64_tr_b16 a[180:181], v215 offset:61440\n"
      "ds_read_b64_tr_b16 a[182:183], v215 offset:62464\n"
      "v_cvt_pk_bf16_f32 v202, v20, v21\n"
      "v_cvt_pk_bf16_f32 v203, v22, v23\n"
      "v_mfma_f32_32x32x16_bf16 v[48:63], a[108:111], v[148:151], v[48:63]\n"
      "ds_read_b64_tr_b16 a[184:185], v214 offset:63488\n"
      "ds_read_b64_tr_b16 a[186:187], v214 offset:64512\n"
      "ds_read_b64_tr_b16 a[188:189], v215 offset:63488\n"
      "buffer_store_dwordx4 v[200:203], v227, %6, s49 offen offset:2048 sc1\n"
      "v_cvt_pk_bf16_f32 v204, v24, v25\n"
      "v_mfma_f32_32x32x16_bf16 v[32:47], a[112:115], v[152:155], v[32:47]\n"
      "ds_read_b64_tr_b16 a[190:191], v215 offset:64512\n"
      "v_cvt_pk_bf16_f32 v205, v26, v27\n"
      "v_cvt_pk_bf16_f32 v206, v28, v29\n"
      "v_mfma_f32_32x32x16_bf16 v[48:63], a[116:119], v[152:155], v[48:63]\n"
      "v_cvt_pk_bf16_f32 v207, v30, v31\n"
      "buffer_store_dwordx4 v[204:207], v227, %6, s49 offen offset:3072 sc1\n"
      "v_mfma_f32_32x32x16_bf16 v[32:47], a[120:123], v[156:159], v[32:47]\n"
      "s_add_u32 s49, s49, 4096\n"
      "s_cmp_eq_u32 s49, 28672\n"
      "s_cselect_b32 s49, 0, s49\n"
      "v_mfma_f32_32x32x16_bf16 v[48:63], a[124:127], v[156:159], v[48:63]\n"
      "s_waitcnt lgkmcnt(0)\n"
      "v_mfma_f32_32x32x16_bf16 v[32:47], a[128:131], v[160:163], v[32:47]\n"
      "s_add_u32 m0, s41, 32768\n"
      "ds_read_b64_tr_b16 a[64:65], v212 offset:32768\n"
      "buffer_load_dwordx4 v223, %5, s48 offen lds\n"
      "ds_read_b64_tr_b16 a[80:81], v212 offset:36864\n"
      "ds_read_b64_tr_b16 a[82:83], v212 offset:37888\n"
      "v_mfma_f32_32x32x16_bf16 v[48:63], a[132:135], v[160:163], v[48:63]\n"
      "ds_read_b64_tr_b16 a[84:85], v213 offset:36864\n"
      "ds_read_b64_tr_b16 a[86:87], v213 offset:37888\n"
      "v_mfma_f32_32x32x16_bf16 v[32:47], a[136:139], v[164:167], v[32:47]\n"
      "s_add_u32 m0, s41, 33792\n"
      "ds_read_b64_tr_b16 a[66:67], v212 offset:33792\n"
      "buffer_load_dwordx4 v224, %5, s48 offen lds\n"
      "ds_read_b64_tr_b16 a[88:89], v212 offset:38912\n"
      "ds_read_b64_tr_b16 a[90:91], v212 offset:39936\n"
      "v_mfma_f32_32x32x16_bf16 v[48:63], a[140:143], v[164:167], v[48:63]\n"
      "ds_read_b64_tr_b16 a[92:93], v213 offset:38912\n"
      "ds_read_b64_tr_b16 a[94:95], v213 offset:39936\n"
      "v_mfma_f32_32x32x16_bf16 v[32:47], a[144:147], v[168:171], v[32:47]\n"
      "s_add_u32 m0, s41, 34816\n"
      "ds_read_b64_tr_b16 a[68:69], v213 offset:32768\n"
      "buffer_load_dwordx4 v225, %5, s48 offen lds\n"
      "ds_read_b64_tr_b16 a[96:97], v212 offset:40960\n"
      "ds_read_b64_tr_b16 a[98:99], v212 offset:41984\n"
      "v_mfma_f32_32x32x16_bf16 v[48:63], a[148:151], v[168:171], v[48:63]\n"
      "ds_read_b64_tr_b16 a[100:101], v213 offset:40960\n"
      "ds_read_b64_tr_b16 a[102:103], v213 offset:41984\n"
      "v_mfma_f32_32x32x16_bf16 v[32:47], a[152:155], v[172:175], v[32:47]\n"
      "s_add_u32 m0, s41, 35840\n"
      "ds_read_b64_tr_b16 a[70:71], v213 offset:33792\n"
      "buffer_load_dwordx4 v226, %5, s48 offen lds\n"
      "s_add_u32 s48, s48, 128\n"
      "s_cmp_eq_u32 s48, 115584\n"
      "s_cselect_b32 s48, 114688, s48\n"
      "ds_read_b64_tr_b16 a[104:105], v212 offset:43008\n"
      "ds_read_b64_tr_b16 a[106:107], v212 offset:44032\n"
      "v_mfma_f32_32x32x16_bf16 v[48:63], a[156:159], v[172:175], v[48:63]\n"
      "ds_read_b64_tr_b16 a[108:109], v213 offset:43008\n"
      "ds_read_b64_tr_b16 a[110:111], v213 offset:44032\n"
      "v_mfma_f32_32x32x16_bf16 v[32:47], a[160:163], v[176:179], v[32:47]\n"
      "s_add_u32 m0, s40, 16384\n"
      "ds_read_b64_tr_b16 a[72:73], v212 offset:34816\n"
      "buffer_load_dwordx4 v223, %5, s47 offen lds\n"
      "ds_read_b64_tr_b16 a[112:113], v212 offset:45056\n"
      "ds_read_b64_tr_b16 a[114:115], v212 offset:46080\n"
      "v_mfma_f32_32x32x16_bf16 v[48:63], a[164:167], v[176:179], v[48:63]\n"
      "ds_read_b64_tr_b16 a[116:117], v213 offset:45056\n"
      "ds_read_b64_tr_b16 a[118:119], v213 offset:46080\n"
      "v_mfma_f32_32x32x16_bf16 v[32:47], a[168:171], v[180:183], v[32:47]\n"
      "s_add_u32 m0, s40, 17408\n"
      "ds_read_b64_tr_b16 a[74:75], v212 offset:35840\n"
      "buffer_load_dwordx4 v224, %5, s47 offen lds\n"
      "ds_read_b64_tr_b16 a[120:121], v212 offset:47104\n"
      "ds_read_b64_tr_b16 a[122:123], v212 offset:48128\n"
      "v_mfma_f32_32x32x16_bf16 v[48:63], a[172:175], v[180:183], v[48:63]\n"
      "ds_read_b64_tr_b16 a[124:125], v213 offset:47104\n"
      "ds_read_b64_tr_b16 a[126:127], v213 offset:48128\n"
      "v_mfma_f32_32x32x16_bf16 v[32:47], a[176:179], v[184:187], v[32:47]\n"
      "s_add_u32 m0, s40, 18432\n"
      "ds_read_b64_tr_b16 a[76:77], v213 offset:34816\n"
      "buffer_load_dwordx4 v225, %5, s47 offen lds\n"
      "v_mfma_f32_32x32x16_bf16 v[48:63], a[180:183], v[184:187], v[48:63]\n"
      "v_mfma_f32_32x32x16_bf16 v[32:47], a[184:187], v[188:191], v[32:47]\n"
      "s_add_u32 m0, s40, 19456\n"
      "ds_read_b64_tr_b16 a[78:79], v213 offset:35840\n"
      "buffer_load_dwordx4 v226, %5, s47 offen lds\n"
      "s_add_u32 s47, s47, 128\n"
      "s_cmp_eq_u32 s47, 896\n"
      "s_cselect_b32 s47, 0, s47\n"
      "v_mfma_f32_32x32x16_bf16 v[48:63], a[188:191], v[188:191], v[48:63]\n"
      "s_waitcnt vmcnt(20) lgkmcnt(0)\n"
      "s_barrier\n"
      "v_mfma_f32_32x32x16_bf16 v[0:15], a[64:67], v[128:131], 0\n"
      "ds_read_b64_tr_b16 a[128:129], v214 offset:0\n"
      "ds_read_b64_tr_b16 a[130:131], v214 offset:1024\n"
      "ds_read_b64_tr_b16 a[132:133], v215 offset:0\n"
      "v_mfma_f32_32x32x16_bf16 v[16:31], a[68:71], v[128:131], 0\n"
      "ds_read_b64_tr_b16 a[134:135], v215 offset:1024\n"
      "ds_read_b64_tr_b16 a[136:137], v214 offset:2048\n"
      "ds_read_b64_tr_b16 a[138:139], v214 offset:3072\n"
      "v_mfma_f32_32x32x16_bf16 v[0:15], a[72:75], v[132:135], v[0:15]\n"
      "ds_read_b64_tr_b16 a[140:141], v215 offset:2048\n"
      "ds_read_b64_tr_b16 a[142:143], v215 offset:3072\n"
      "v_mfma_f32_32x32x16_bf16 v[16:31], a[76:79], v[132:135], v[16:31]\n"
      "ds_read_b64_tr_b16 a[144:145], v214 offset:4096\n"
      "ds_read_b64_tr_b16 a[146:147], v214 offset:5120\n"
      "ds_read_b64_tr_b16 a[148:149], v215 offset:4096\n"
      "v_mfma_f32_32x32x16_bf16 v[0:15], a[80:83], v[136:139], v[0:15]\n"
      "ds_read_b64_tr_b16 a[150:151], v215 offset:5120\n"
      "ds_read_b64_tr_b16 a[152:153], v214 offset:6144\n"
      "v_cvt_pk_bf16_f32 v192, v32, v33\n"
      "v_cvt_pk_bf16_f32 v193, v34, v35\n"
      "v_mfma_f32_32x32x16_bf16 v[16:31], a[84:87], v[136:139], v[16:31]\n"
      "ds_read_b64_tr_b16 a[154:155], v214 offset:7168\n"
      "ds_read_b64_tr_b16 a[156:157], v215 offset:6144\n"
      "ds_read_b64_tr_b16 a[158:159], v215 offset:7168\n"
      "v_cvt_pk_bf16_f32 v194, v36, v37\n"
      "v_cvt_pk_bf16_f32 v195, v38, v39\n"
      "v_mfma_f32_32x32x16_bf16 v[0:15], a[88:91], v[140:143], v[0:15]\n"
      "ds_read_b64_tr_b16 a[160:161], v214 offset:8192\n"
      "ds_read_b64_tr_b16 a[162:163], v214 offset:9216\n"
      "buffer_store_dwordx4 v[192:195], v227, %6, s49 offen offset:0 sc1\n"
      "v_cvt_pk_bf16_f32 v196, v40, v41\n"
      "v_mfma_f32_32x32x16_bf16 v[16:31], a[92:95], v[140:143], v[16:31]\n"
      "ds_read_b64_tr_b16 a[164:165], v215 offset:8192\n"
      "ds_read_b64_tr_b16 a[166:167], v215 offset:9216\n"
      "ds_read_b64_tr_b16 a[168:169], v214 offset:10240\n"
      "v_cvt_pk_bf16_f32 v197, v42, v43\n"
      "v_cvt_pk_bf16_f32 v198, v44, v45\n"
      "v_mfma_f32_32x32x16_bf16 v[0:15], a[96:99], v[144:147], v[0:15]\n"
      "ds_read_b64_tr_b16 a[170:171], v214 offset:11264\n"
      "ds_read_b64_tr_b16 a[172:173], v215 offset:10240\n"
      "v_cvt_pk_bf16_f32 v199, v46, v47\n"
      "buffer_store_dwordx4 v[196:199], v227, %6, s49 offen offset:1024 sc1\n"
      "v_mfma_f32_32x32x16_bf16 v[16:31], a[100:103], v[144:147], v[16:31]\n"
      "ds_read_b64_tr_b16 a[174:175], v215 offset:11264\n"
      "ds_read_b64_tr_b16 a[176:177], v214 offset:12288\n"
      "ds_read_b64_tr_b16 a[178:179], v214 offset:13312\n"
      "v_cvt_pk_bf16_f32 v200, v48, v49\n"
      "v_cvt_pk_bf16_f32 v201, v50, v51\n"
      "v_mfma_f32_32x32x16_bf16 v[0:15], a[104:107], v[148:151], v[0:15]\n"
      "ds_read_b64_tr_b16 a[180:181], v215 offset:12288\n"
      "ds_read_b64_tr_b16 a[182:183], v215 offset:13312\n"
      "v_cvt_pk_bf16_f32 v202, v52, v53\n"
      "v_cvt_pk_bf16_f32 v203, v54, v55\n"
      "v_mfma_f32_32x32x16_bf16 v[16:31], a[108:111], v[148:151], v[16:31]\n"
      "ds_read_b64_tr_b16 a[184:185], v214 offset:14336\n"
      "ds_read_b64_tr_b16 a[186:187], v214 offset:15360\n"
      "ds_read_b64_tr_b16 a[188:189], v215 offset:14336\n"
      "buffer_store_dwordx4 v[200:203], v227, %6, s49 offen offset:2048 sc1\n"
      "v_cvt_pk_bf16_f32 v204, v56, v57\n"
      "v_mfma_f32_32x32x16_bf16 v[0:15], a[112:115], v[152:155], v[0:15]\n"
      "ds_read_b64_tr_b16 a[190:191], v215 offset:15360\n"
      "v_cvt_pk_bf16_f32 v205, v58, v59\n"
      "v_cvt_pk_bf16_f32 v206, v60, v61\n"
      "v_mfma_f32_32x32x16_bf16 v[16:31], a[116:119], v[152:155], v[16:31]\n"
      "v_cvt_pk_bf16_f32 v207, v62, v63\n"
      "buffer_store_dwordx4 v[204:207], v227, %6, s49 offen offset:3072 sc1\n"
      "v_mfma_f32_32x32x16_bf16 v[0:15], a[120:123], v[156:159], v[0:15]\n"
      "s_add_u32 s49, s49, 4096\n"
      "s_cmp_eq_u32 s49, 28672\n"
      "s_cselect_b32 s49, 0, s49\n"
      "v_mfma_f32_32x32x16_bf16 v[16:31], a[124:127], v[156:159], v[16:31]\n"
      "s_waitcnt lgkmcnt(0)\n"
      "v_mfma_f32_32x32x16_bf16 v[0:15], a[128:131], v[160:163], v[0:15]\n"
      "s_add_u32 m0, s41, 49152\n"
      "ds_read_b64_tr_b16 a[64:65], v212 offset:49152\n"
      "buffer_load_dwordx4 v223, %5, s48 offen lds\n"
      "ds_read_b64_tr_b16 a[80:81], v212 offset:53248\n"
      "ds_read_b64_tr_b16 a[82:83], v212 offset:54272\n"
      "v_mfma_f32_32x32x16_bf16 v[16:31], a[132:135], v[160:163], v[16:31]\n"
      "ds_read_b64_tr_b16 a[84:85], v213 offset:53248\n"
      "ds_read_b64_tr_b16 a[86:87], v213 offset:54272\n"
      "v_mfma_f32_32x32x16_bf16 v[0:15], a[136:139], v[164:167], v[0:15]\n"
      "s_add_u32 m0, s41, 50176\n"
      "ds_read_b64_tr_b16 a[66:67], v212 offset:50176\n"
      "buffer_load_dwordx4 v224, %5, s48 offen lds\n"
      "ds_read_b64_tr_b16 a[88:89], v212 offset:55296\n"
      "ds_read_b64_tr_b16 a[90:91], v212 offset:56320\n"
      "v_mfma_f32_32x32x16_bf16 v[16:31], a[140:143], v[164:167], v[16:31]\n"
      "ds_read_b64_tr_b16 a[92:93], v213 offset:55296\n"
      "ds_read_b64_tr_b16 a[94:95], v213 offset:56320\n"
      "v_mfma_f32_32x32x16_bf16 v[0:15], a[144:147], v[168:171], v[0:15]\n"
      "s_add_u32 m0, s41, 51200\n"
      "ds_read_b64_tr_b16 a[68:69], v213 offset:49152\n"
      "buffer_load_dwordx4 v225, %5, s48 offen lds\n"
      "ds_read_b64_tr_b16 a[96:97], v212 offset:57344\n"
      "ds_read_b64_tr_b16 a[98:99], v212 offset:58368\n"
      "v_mfma_f32_32x32x16_bf16 v[16:31], a[148:151], v[168:171], v[16:31]\n"
      "ds_read_b64_tr_b16 a[100:101], v213 offset:57344\n"
      "ds_read_b64_tr_b16 a[102:103], v213 offset:58368\n"
      "v_mfma_f32_32x32x16_bf16 v[0:15], a[152:155], v[172:175], v[0:15]\n"
      "s_add_u32 m0, s41, 52224\n"
      "ds_read_b64_tr_b16 a[70:71], v213 offset:50176\n"
      "buffer_load_dwordx4 v226, %5, s48 offen lds\n"
      "s_add_u32 s48, s48, 128\n"
      "s_cmp_eq_u32 s48, 115584\n"
      "s_cselect_b32 s48, 114688, s48\n"
      "ds_read_b64_tr_b16 a[104:105], v212 offset:59392\n"
      "ds_read_b64_tr_b16 a[106:107], v212 offset:60416\n"
      "v_mfma_f32_32x32x16_bf16 v[16:31], a[156:159], v[172:175], v[16:31]\n"
      "ds_read_b64_tr_b16 a[108:109], v213 offset:59392\n"
      "ds_read_b64_tr_b16 a[110:111], v213 offset:60416\n"
      "v_mfma_f32_32x32x16_bf16 v[0:15], a[160:163], v[176:179], v[0:15]\n"
      "s_add_u32 m0, s40, 32768\n"
      "ds_read_b64_tr_b16 a[72:73], v212 offset:51200\n"
      "buffer_load_dwordx4 v223, %5, s47 offen lds\n"
      "ds_read_b64_tr_b16 a[112:113], v212 offset:61440\n"
      "ds_read_b64_tr_b16 a[114:115], v212 offset:62464\n"
      "v_mfma_f32_32x32x16_bf16 v[16:31], a[164:167], v[176:179], v[16:31]\n"
      "ds_read_b64_tr_b16 a[116:117], v213 offset:61440\n"
      "ds_read_b64_tr_b16 a[118:119], v213 offset:62464\n"
      "v_mfma_f32_32x32x16_bf16 v[0:15], a[168:171], v[180:183], v[0:15]\n"
      "s_add_u32 m0, s40, 33792\n"
      "ds_read_b64_tr_b16 a[74:75], v212 offset:52224\n"
      "buffer_load_dwordx4 v224, %5, s47 offen lds\n"
      "ds_read_b64_tr_b16 a[120:121], v212 offset:63488\n"
      "ds_read_b64_tr_b16 a[122:123], v212 offset:64512\n"
      "v_mfma_f32_32x32x16_bf16 v[16:31], a[172:175], v[180:183], v[16:31]\n"
      "ds_read_b64_tr_b16 a[124:125], v213 offset:63488\n"
      "ds_read_b64_tr_b16 a[126:127], v213 offset:64512\n"
      "v_mfma_f32_32x32x16_bf16 v[0:15], a[176:179], v[184:187], v[0:15]\n"
      "s_add_u32 m0, s40, 34816\n"
      "ds_read_b64_tr_b16 a[76:77], v213 offset:51200\n"
      "buffer_load_dwordx4 v225, %5, s47 offen lds\n"
      "v_mfma_f32_32x32x16_bf16 v[16:31], a[180:183], v[184:187], v[16:31]\n"
      "v_mfma_f32_32x32x16_bf16 v[0:15], a[184:187], v[188:191], v[0:15]\n"
      "s_add_u32 m0, s40, 35840\n"
      "ds_read_b64_tr_b16 a[78:79], v213 offset:52224\n"
      "buffer_load_dwordx4 v226, %5, s47 offen lds\n"
      "s_add_u32 s47, s47, 128\n"
      "s_cmp_eq_u32 s47, 896\n"
      "s_cselect_b32 s47, 0, s47\n"
      "v_mfma_f32_32x32x16_bf16 v[16:31], a[188:191], v[188:191], v[16:31]\n"
      "s_waitcnt vmcnt(24) lgkmcnt(0)\n"
      "s_barrier\n"
      "v_mfma_f32_32x32x16_bf16 v[32:47], a[64:67], v[128:131], 0\n"
      "ds_read_b64_tr_b16 a[128:129], v214 offset:16384\n"
      "ds_read_b64_tr_b16 a[130:131], v214 offset:17408\n"
      "ds_read_b64_tr_b16 a[132:133], v215 offset:16384\n"
      "v_mfma_f32_32x32x16_bf16 v[48:63], a[68:71], v[128:131], 0\n"
      "ds_read_b64_tr_b16 a[134:135], v215 offset:17408\n"
      "ds_read_b64_tr_b16 a[136:137], v214 offset:18432\n"
      "ds_read_b64_tr_b16 a[138:139], v214 offset:19456\n"
      "v_mfma_f32_32x32x16_bf16 v[32:47], a[72:75], v[132:135], v[32:47]\n"
      "ds_read_b64_tr_b16 a[140:141], v215 offset:18432\n"
      "ds_read_b64_tr_b16 a[142:143], v215 offset:19456\n"
      "v_mfma_f32_32x32x16_bf16 v[48:63], a[76:79], v[132:135], v[48:63]\n"
      "ds_read_b64_tr_b16 a[144:145], v214 offset:20480\n"
      "ds_read_b64_tr_b16 a[146:147], v214 offset:21504\n"
      "ds_read_b64_tr_b16 a[148:149], v215 offset:20480\n"
      "v_mfma_f32_32x32x16_bf16 v[32:47], a[80:83], v[136:139], v[32:47]\n"
      "ds_read_b64_tr_b16 a[150:151], v215 offset:21504\n"
      "ds_read_b64_tr_b16 a[152:153], v214 offset:22528\n"
      "v_cvt_pk_bf16_f32 v192, v0, v1\n"
      "v_cvt_pk_bf16_f32 v193, v2, v3\n"
      "v_mfma_f32_32x32x16_bf16 v[48:63], a[84:87], v[136:139], v[48:63]\n"
      "ds_read_b64_tr_b16 a[154:155], v214 offset:23552\n"
      "ds_read_b64_tr_b16 a[156:157], v215 offset:22528\n"
      "ds_read_b64_tr_b16 a[158:159], v215 offset:23552\n"
      "v_cvt_pk_bf16_f32 v194, v4, v5\n"
      "v_cvt_pk_bf16_f32 v195, v6, v7\n"
      "v_mfma_f32_32x32x16_bf16 v[32:47], a[88:91], v[140:143], v[32:47]\n"
      "ds_read_b64_tr_b16 a[160:161], v214 offset:24576\n"
      "ds_read_b64_tr_b16 a[162:163], v214 offset:25600\n"
      "buffer_store_dwordx4 v[192:195], v227, %6, s49 offen offset:0 sc1\n"
      "v_cvt_pk_bf16_f32 v196, v8, v9\n"
      "v_mfma_f32_32x32x16_bf16 v[48:63], a[92:95], v[140:143], v[48:63]\n"
      "ds_read_b64_tr_b16 a[164:165], v215 offset:24576\n"
      "ds_read_b64_tr_b16 a[166:167], v215 offset:25600\n"
      "ds_read_b64_tr_b16 a[168:169], v214 offset:26624\n"
      "v_cvt_pk_bf16_f32 v197, v10, v11\n"
      "v_cvt_pk_bf16_f32 v198, v12, v13\n"
      "v_mfma_f32_32x32x16_bf16 v[32:47], a[96:99], v[144:147], v[32:47]\n"
      "ds_read_b64_tr_b16 a[170:171], v214 offset:27648\n"
      "ds_read_b64_tr_b16 a[172:173], v215 offset:26624\n"
      "v_cvt_pk_bf16_f32 v199, v14, v15\n"
      "buffer_store_dwordx4 v[196:199], v227, %6, s49 offen offset:1024 sc1\n"
      "v_mfma_f32_32x32x16_bf16 v[48:63], a[100:103], v[144:147], v[48:63]\n"
      "ds_read_b64_tr_b16 a[174:175], v215 offset:27648\n"
      "ds_read_b64_tr_b16 a[176:177], v214 offset:28672\n"
      "ds_read_b64_tr_b16 a[178:179], v214 offset:29696\n"
      "v_cvt_pk_bf16_f32 v200, v16, v17\n"
      "v_cvt_pk_bf16_f32 v201, v18, v19\n"
      "v_mfma_f32_32x32x16_bf16 v[32:47], a[104:107], v[148:151], v[32:47]\n"
      "ds_read_b64_tr_b16 a[180:181], v215 offset:28672\n"
      "ds_read_b64_tr_b16 a[182:183], v215 offset:29696\n"
      "v_cvt_pk_bf16_f32 v202, v20, v21\n"
      "v_cvt_pk_bf16_f32 v203, v22, v23\n"
      "v_mfma_f32_32x32x16_bf16 v[48:63], a[108:111], v[148:151], v[48:63]\n"
      "ds_read_b64_tr_b16 a[184:185], v214 offset:30720\n"
      "ds_read_b64_tr_b16 a[186:187], v214 offset:31744\n"
      "ds_read_b64_tr_b16 a[188:189], v215 offset:30720\n"
      "buffer_store_dwordx4 v[200:203], v227, %6, s49 offen offset:2048 sc1\n"
      "v_cvt_pk_bf16_f32 v204, v24, v25\n"
      "v_mfma_f32_32x32x16_bf16 v[32:47], a[112:115], v[152:155], v[32:47]\n"
      "ds_read_b64_tr_b16 a[190:191], v215 offset:31744\n"
      "v_cvt_pk_bf16_f32 v205, v26, v27\n"
      "v_cvt_pk_bf16_f32 v206, v28, v29\n"
      "v_mfma_f32_32x32x16_bf16 v[48:63], a[116:119], v[152:155], v[48:63]\n"
      "v_cvt_pk_bf16_f32 v207, v30, v31\n"
      "buffer_store_dwordx4 v[204:207], v227, %6, s49 offen offset:3072 sc1\n"
      "v_mfma_f32_32x32x16_bf16 v[32:47], a[120:123], v[156:159], v[32:47]\n"
      "s_add_u32 s49, s49, 4096\n"
      "s_cmp_eq_u32 s49, 28672\n"
      "s_cselect_b32 s49, 0, s49\n"
      "v_mfma_f32_32x32x16_bf16 v[48:63], a[124:127], v[156:159], v[48:63]\n"
      "s_waitcnt lgkmcnt(0)\n"
      "v_mfma_f32_32x32x16_bf16 v[32:47], a[128:131], v[160:163], v[32:47]\n"
      "s_add_u32 m0, s41, 0\n"
      "ds_read_b64_tr_b16 a[64:65], v212 offset:0\n"
      "buffer_load_dwordx4 v223, %5, s48 offen lds\n"
      "ds_read_b64_tr_b16 a[72:73], v212 offset:2048\n"
      "ds_read_b64_tr_b16 a[74:75], v212 offset:3072\n"
      "ds_read_b64_tr_b16 a[76:77], v213 offset:2048\n"
      "v_mfma_f32_32x32x16_bf16 v[48:63], a[132:135], v[160:163], v[48:63]\n"
      "ds_read_b64_tr_b16 a[78:79], v213 offset:3072\n"
      "ds_read_b64_tr_b16 a[80:81], v212 offset:4096\n"
      "v_mfma_f32_32x32x16_bf16 v[32:47], a[136:139], v[164:167], v[32:47]\n"
      "ds_read_b64_tr_b16 a[82:83], v212 offset:5120\n"
      "ds_read_b64_tr_b16 a[84:85], v213 offset:4096\n"
      "v_mfma_f32_32x32x16_bf16 v[48:63], a[140:143], v[164:167], v[48:63]\n"
      "ds_read_b64_tr_b16 a[86:87], v213 offset:5120\n"
      "ds_read_b64_tr_b16 a[88:89], v212 offset:6144\n"
      "v_mfma_f32_32x32x16_bf16 v[32:47], a[144:147], v[168:171], v[32:47]\n"
      "s_add_u32 m0, s41, 1024\n"
      "ds_read_b64_tr_b16 a[66:67], v212 offset:1024\n"
      "buffer_load_dwordx4 v224, %5, s48 offen lds\n"
      "ds_read_b64_tr_b16 a[90:91], v212 offset:7168\n"
      "ds_read_b64_tr_b16 a[92:93], v213 offset:6144\n"
      "ds_read_b64_tr_b16 a[94:95], v213 offset:7168\n"
      "v_mfma_f32_32x32x16_bf16 v[48:63], a[148:151], v[168:171], v[48:63]\n"
      "ds_read_b64_tr_b16 a[96:97], v212 offset:8192\n"
      "ds_read_b64_tr_b16 a[98:99], v212 offset:9216\n"
      "v_mfma_f32_32x32x16_bf16 v[32:47], a[152:155], v[172:175], v[32:47]\n"
      "ds_read_b64_tr_b16 a[100:101], v213 offset:8192\n"
      "ds_read_b64_tr_b16 a[102:103], v213 offset:9216\n"
      "v_mfma_f32_32x32x16_bf16 v[48:63], a[156:159], v[172:175], v[48:63]\n"
      "ds_read_b64_tr_b16 a[104:105], v212 offset:10240\n"
      "ds_read_b64_tr_b16 a[106:107], v212 offset:11264\n"
      "v_mfma_f32_32x32x16_bf16 v[32:47], a[160:163], v[176:179], v[32:47]\n"
      "s_add_u32 m0, s41, 2048\n"
      "ds_read_b64_tr_b16 a[68:69], v213 offset:0\n"
      "buffer_load_dwordx4 v225, %5, s48 offen lds\n"
      "ds_read_b64_tr_b16 a[108:109], v213 offset:10240\n"
      "ds_read_b64_tr_b16 a[110:111], v213 offset:11264\n"
      "ds_read_b64_tr_b16 a[112:113], v212 offset:12288\n"
      "v_mfma_f32_32x32x16_bf16 v[48:63], a[164:167], v[176:179], v[48:63]\n"
      "ds_read_b64_tr_b16 a[114:115], v212 offset:13312\n"
      "ds_read_b64_tr_b16 a[116:117], v213 offset:12288\n"
      "v_mfma_f32_32x32x16_bf16 v[32:47], a[168:171], v[180:183], v[32:47]\n"
      "ds_read_b64_tr_b16 a[118:119], v213 offset:13312\n"
      "ds_read_b64_tr_b16 a[120:121], v212 offset:14336\n"
      "v_mfma_f32_32x32x16_bf16 v[48:63], a[172:175], v[180:183], v[48:63]\n"
      "ds_read_b64_tr_b16 a[122:123], v212 offset:15360\n"
      "ds_read_b64_tr_b16 a[124:125], v213 offset:14336\n"
      "v_mfma_f32_32x32x16_bf16 v[32:47], a[176:179], v[184:187], v[32:47]\n"
      "s_add_u32 m0, s41, 3072\n"
      "ds_read_b64_tr_b16 a[70:71], v213 offset:1024\n"
      "buffer_load_dwordx4 v226, %5, s48 offen lds\n"
      "s_add_u32 s48, s48, 128\n"
      "s_cmp_eq_u32 s48, 115584\n"
      "s_cselect_b32 s48, 114688, s48\n"
      "ds_read_b64_tr_b16 a[126:127], v213 offset:15360\n"
      "v_mfma_f32_32x32x16_bf16 v[48:63], a[180:183], v[184:187], v[48:63]\n"
      "v_mfma_f32_32x32x16_bf16 v[32:47], a[184:187], v[188:191], v[32:47]\n"
      "v_mfma_f32_32x32x16_bf16 v[48:63], a[188:191], v[188:191], v[48:63]\n"
      "s_waitcnt vmcnt(20) lgkmcnt(0)\n"
      "s_barrier\n"
      "v_mfma_f32_32x32x16_bf16 v[0:15], a[64:67], v[128:131], 0\n"
      "ds_read_b64_tr_b16 a[128:129], v214 offset:32768\n"
      "ds_read_b64_tr_b16 a[130:131], v214 offset:33792\n"
      "ds_read_b64_tr_b16 a[132:133], v215 offset:32768\n"
      "v_mfma_f32_32x32x16_bf16 v[16:31], a[68:71], v[128:131], 0\n"
      "ds_read_b64_tr_b16 a[134:135], v215 offset:33792\n"
      "ds_read_b64_tr_b16 a[136:137], v214 offset:34816\n"
      "ds_read_b64_tr_b16 a[138:139], v214 offset:35840\n"
      "v_mfma_f32_32x32x16_bf16 v[0:15], a[72:75], v[132:135], v[0:15]\n"
      "ds_read_b64_tr_b16 a[140:141], v215 offset:34816\n"
      "ds_read_b64_tr_b16 a[142:143], v215 offset:35840\n"
      "v_mfma_f32_32x32x16_bf16 v[16:31], a[76:79], v[132:135], v[16:31]\n"
      "ds_read_b64_tr_b16 a[144:145], v214 offset:36864\n"
      "ds_read_b64_tr_b16 a[146:147], v214 offset:37888\n"
      "ds_read_b64_tr_b16 a[148:149], v215 offset:36864\n"
      "v_mfma_f32_32x32x16_bf16 v[0:15], a[80:83], v[136:139], v[0:15]\n"
      "ds_read_b64_tr_b16 a[150:151], v215 offset:37888\n"
      "ds_read_b64_tr_b16 a[152:153], v214 offset:38912\n"
      "v_cvt_pk_bf16_f32 v192, v32, v33\n"
      "v_cvt_pk_bf16_f32 v193, v34, v35\n"
      "v_mfma_f32_32x32x16_bf16 v[16:31], a[84:87], v[136:139], v[16:31]\n"
      "ds_read_b64_tr_b16 a[154:155], v214 offset:39936\n"
      "ds_read_b64_tr_b16 a[156:157], v215 offset:38912\n"
      "ds_read_b64_tr_b16 a[158:159], v215 offset:39936\n"
      "v_cvt_pk_bf16_f32 v194, v36, v37\n"
      "v_cvt_pk_bf16_f32 v195, v38, v39\n"
      "v_mfma_f32_32x32x16_bf16 v[0:15], a[88:91], v[140:143], v[0:15]\n"
      "ds_read_b64_tr_b16 a[160:161], v214 offset:40960\n"
      "ds_read_b64_tr_b16 a[162:163], v214 offset:41984\n"
      "buffer_store_dwordx4 v[192:195], v227, %6, s49 offen offset:0 sc1\n"
      "v_cvt_pk_bf16_f32 v196, v40, v41\n"
      "v_mfma_f32_32x32x16_bf16 v[16:31], a[92:95], v[140:143], v[16:31]\n"
      "ds_read_b64_tr_b16 a[164:165], v215 offset:40960\n"
      "ds_read_b64_tr_b16 a[166:167], v215 offset:41984\n"
      "ds_read_b64_tr_b16 a[168:169], v214 offset:43008\n"
      "v_cvt_pk_bf16_f32 v197, v42, v43\n"
      "v_cvt_pk_bf16_f32 v198, v44, v45\n"
      "v_mfma_f32_32x32x16_bf16 v[0:15], a[96:99], v[144:147], v[0:15]\n"
      "ds_read_b64_tr_b16 a[170:171], v214 offset:44032\n"
      "ds_read_b64_tr_b16 a[172:173], v215 offset:43008\n"
      "v_cvt_pk_bf16_f32 v199, v46, v47\n"
      "buffer_store_dwordx4 v[196:199], v227, %6, s49 offen offset:1024 sc1\n"
      "v_mfma_f32_32x32x16_bf16 v[16:31], a[100:103], v[144:147], v[16:31]\n"
      "ds_read_b64_tr_b16 a[174:175], v215 offset:44032\n"
      "ds_read_b64_tr_b16 a[176:177], v214 offset:45056\n"
      "ds_read_b64_tr_b16 a[178:179], v214 offset:46080\n"
      "v_cvt_pk_bf16_f32 v200, v48, v49\n"
      "v_cvt_pk_bf16_f32 v201, v50, v51\n"
      "v_mfma_f32_32x32x16_bf16 v[0:15], a[104:107], v[148:151], v[0:15]\n"
      "ds_read_b64_tr_b16 a[180:181], v215 offset:45056\n"
      "ds_read_b64_tr_b16 a[182:183], v215 offset:46080\n"
      "v_cvt_pk_bf16_f32 v202, v52, v53\n"
      "v_cvt_pk_bf16_f32 v203, v54, v55\n"
      "v_mfma_f32_32x32x16_bf16 v[16:31], a[108:111], v[148:151], v[16:31]\n"
      "ds_read_b64_tr_b16 a[184:185], v214 offset:47104\n"
      "ds_read_b64_tr_b16 a[186:187], v214 offset:48128\n"
      "ds_read_b64_tr_b16 a[188:189], v215 offset:47104\n"
      "buffer_store_dwordx4 v[200:203], v227, %6, s49 offen offset:2048 sc1\n"
      "v_cvt_pk_bf16_f32 v204, v56, v57\n"
      "v_mfma_f32_32x32x16_bf16 v[0:15], a[112:115], v[152:155], v[0:15]\n"
      "ds_read_b64_tr_b16 a[190:191], v215 offset:48128\n"
      "v_cvt_pk_bf16_f32 v205, v58, v59\n"
      "v_cvt_pk_bf16_f32 v206, v60, v61\n"
      "v_mfma_f32_32x32x16_bf16 v[16:31], a[116:119], v[152:155], v[16:31]\n"
      "v_cvt_pk_bf16_f32 v207, v62, v63\n"
      "buffer_store_dwordx4 v[204:207], v227, %6, s49 offen offset:3072 sc1\n"
      "v_mfma_f32_32x32x16_bf16 v[0:15], a[120:123], v[156:159], v[0:15]\n"
      "s_add_u32 s49, s49, 4096\n"
      "s_cmp_eq_u32 s49, 28672\n"
      "s_cselect_b32 s49, 0, s49\n"
      "v_mfma_f32_32x32x16_bf16 v[16:31], a[124:127], v[156:159], v[16:31]\n"
      "s_waitcnt lgkmcnt(0)\n"
      "v_mfma_f32_32x32x16_bf16 v[0:15], a[128:131], v[160:163], v[0:15]\n"
      "ds_read_b64_tr_b16 a[64:65], v212 offset:16384\n"
      "ds_read_b64_tr_b16 a[66:67], v212 offset:17408\n"
      "ds_read_b64_tr_b16 a[68:69], v213 offset:16384\n"
      "v_mfma_f32_32x32x16_bf16 v[16:31], a[132:135], v[160:163], v[16:31]\n"
      "ds_read_b64_tr_b16 a[70:71], v213 offset:17408\n"
      "ds_read_b64_tr_b16 a[72:73], v212 offset:18432\n"
      "ds_read_b64_tr_b16 a[74:75], v212 offset:19456\n"
      "v_mfma_f32_32x32x16_bf16 v[0:15], a[136:139], v[164:167], v[0:15]\n"
      "ds_read_b64_tr_b16 a[76:77], v213 offset:18432\n"
      "ds_read_b64_tr_b16 a[78:79], v213 offset:19456\n"
      "v_mfma_f32_32x32x16_bf16 v[16:31], a[140:143], v[164:167], v[16:31]\n"
      "ds_read_b64_tr_b16 a[80:81], v212 offset:20480\n"
      "ds_read_b64_tr_b16 a[82:83], v212 offset:21504\n"
      "ds_read_b64_tr_b16 a[84:85], v213 offset:20480\n"
      "v_mfma_f32_32x32x16_bf16 v[0:15], a[144:147], v[168:171], v[0:15]\n"
      "ds_read_b64_tr_b16 a[86:87], v213 offset:21504\n"
      "ds_read_b64_tr_b16 a[88:89], v212 offset:22528\n"
      "v_mfma_f32_32x32x16_bf16 v[16:31], a[148:151], v[168:171], v[16:31]\n"
      "ds_read_b64_tr_b16 a[90:91], v212 offset:23552\n"
      "ds_read_b64_tr_b16 a[92:93], v213 offset:22528\n"
      "ds_read_b64_tr_b16 a[94:95], v213 offset:23552\n"
      "v_mfma_f32_32x32x16_bf16 v[0:15], a[152:155], v[172:175], v[0:15]\n"
      "ds_read_b64_tr_b16 a[96:97], v212 offset:24576\n"
      "ds_read_b64_tr_b16 a[98:99], v212 offset:25600\n"
      "v_mfma_f32_32x32x16_bf16 v[16:31], a[156:159], v[172:175], v[16:31]\n"
      "ds_read_b64_tr_b16 a[100:101], v213 offset:24576\n"
      "ds_read_b64_tr_b16 a[102:103], v213 offset:25600\n"
      "ds_read_b64_tr_b16 a[104:105], v212 offset:26624\n"
      "v_mfma_f32_32x32x16_bf16 v[0:15], a[160:163], v[176:179], v[0:15]\n"
      "ds_read_b64_tr_b16 a[106:107], v212 offset:27648\n"
      "ds_read_b64_tr_b16 a[108:109], v213 offset:26624\n"
      "v_mfma_f32_32x32x16_bf16 v[16:31], a[164:167], v[176:179], v[16:31]\n"
      "ds_read_b64_tr_b16 a[110:111], v213 offset:27648\n"
      "ds_read_b64_tr_b16 a[112:113], v212 offset:28672\n"
      "ds_read_b64_tr_b16 a[114:115], v212 offset:29696\n"
      "v_mfma_f32_32x32x16_bf16 v[0:15], a[168:171], v[180:183], v[0:15]\n"
      "ds_read_b64_tr_b16 a[116:117], v213 offset:28672\n"
      "ds_read_b64_tr_b16 a[118:119], v213 offset:29696\n"
      "v_mfma_f32_32x32x16_bf16 v[16:31], a[172:175], v[180:183], v[16:31]\n"
      "ds_read_b64_tr_b16 a[120:121], v212 offset:30720\n"
      "ds_read_b64_tr_b16 a[122:123], v212 offset:31744\n"
      "ds_read_b64_tr_b16 a[124:125], v213 offset:30720\n"
      "v_mfma_f32_32x32x16_bf16 v[0:15], a[176:179], v[184:187], v[0:15]\n"
      "ds_read_b64_tr_b16 a[126:127], v213 offset:31744\n"
      "v_mfma_f32_32x32x16_bf16 v[16:31], a[180:183], v[184:187], v[16:31]\n"
      "v_mfma_f32_32x32x16_bf16 v[0:15], a[184:187], v[188:191], v[0:15]\n"
      "v_mfma_f32_32x32x16_bf16 v[16:31], a[188:191], v[188:191], v[16:31]\n"
      "s_waitcnt vmcnt(12) lgkmcnt(0)\n"
      "s_barrier\n"
      "v_mfma_f32_32x32x16_bf16 v[32:47], a[64:67], v[128:131], 0\n"
      "ds_read_b64_tr_b16 a[128:129], v214 offset:49152\n"
      "ds_read_b64_tr_b16 a[130:131], v214 offset:50176\n"
      "ds_read_b64_tr_b16 a[132:133], v215 offset:49152\n"
      "v_mfma_f32_32x32x16_bf16 v[48:63], a[68:71], v[128:131], 0\n"
      "ds_read_b64_tr_b16 a[134:135], v215 offset:50176\n"
      "ds_read_b64_tr_b16 a[136:137], v214 offset:51200\n"
      "ds_read_b64_tr_b16 a[138:139], v214 offset:52224\n"
      "v_mfma_f32_32x32x16_bf16 v[32:47], a[72:75], v[132:135], v[32:47]\n"
      "ds_read_b64_tr_b16 a[140:141], v215 offset:51200\n"
      "ds_read_b64_tr_b16 a[142:143], v215 offset:52224\n"
      "v_mfma_f32_32x32x16_bf16 v[48:63], a[76:79], v[132:135], v[48:63]\n"
      "ds_read_b64_tr_b16 a[144:145], v214 offset:53248\n"
      "ds_read_b64_tr_b16 a[146:147], v214 offset:54272\n"
      "ds_read_b64_tr_b16 a[148:149], v215 offset:53248\n"
      "v_mfma_f32_32x32x16_bf16 v[32:47], a[80:83], v[136:139], v[32:47]\n"
      "ds_read_b64_tr_b16 a[150:151], v215 offset:54272\n"
      "ds_read_b64_tr_b16 a[152:153], v214 offset:55296\n"
      "v_cvt_pk_bf16_f32 v192, v0, v1\n"
      "v_cvt_pk_bf16_f32 v193, v2, v3\n"
      "v_mfma_f32_32x32x16_bf16 v[48:63], a[84:87], v[136:139], v[48:63]\n"
      "ds_read_b64_tr_b16 a[154:155], v214 offset:56320\n"
      "ds_read_b64_tr_b16 a[156:157], v215 offset:55296\n"
      "ds_read_b64_tr_b16 a[158:159], v215 offset:56320\n"
      "v_cvt_pk_bf16_f32 v194, v4, v5\n"
      "v_cvt_pk_bf16_f32 v195, v6, v7\n"
      "v_mfma_f32_32x32x16_bf16 v[32:47], a[88:91], v[140:143], v[32:47]\n"
      "ds_read_b64_tr_b16 a[160:161], v214 offset:57344\n"
      "ds_read_b64_tr_b16 a[162:163], v214 offset:58368\n"
      "buffer_store_dwordx4 v[192:195], v227, %6, s49 offen offset:0 sc1\n"
      "v_cvt_pk_bf16_f32 v196, v8, v9\n"
      "v_mfma_f32_32x32x16_bf16 v[48:63], a[92:95], v[140:143], v[48:63]\n"
      "ds_read_b64_tr_b16 a[164:165], v215 offset:57344\n"
      "ds_read_b64_tr_b16 a[166:167], v215 offset:58368\n"
      "ds_read_b64_tr_b16 a[168:169], v214 offset:59392\n"
      "v_cvt_pk_bf16_f32 v197, v10, v11\n"
      "v_cvt_pk_bf16_f32 v198, v12, v13\n"
      "v_mfma_f32_32x32x16_bf16 v[32:47], a[96:99], v[144:147], v[32:47]\n"
      "ds_read_b64_tr_b16 a[170:171], v214 offset:60416\n"
      "ds_read_b64_tr_b16 a[172:173], v215 offset:59392\n"
      "v_cvt_pk_bf16_f32 v199, v14, v15\n"
      "buffer_store_dwordx4 v[196:199], v227, %6, s49 offen offset:1024 sc1\n"
      "v_mfma_f32_32x32x16_bf16 v[48:63], a[100:103], v[144:147], v[48:63]\n"
      "ds_read_b64_tr_b16 a[174:175], v215 offset:60416\n"
      "ds_read_b64_tr_b16 a[176:177], v214 offset:61440\n"
      "ds_read_b64_tr_b16 a[178:179], v214 offset:62464\n"
      "v_cvt_pk_bf16_f32 v200, v16, v17\n"
      "v_cvt_pk_bf16_f32 v201, v18, v19\n"
      "v_mfma_f32_32x32x16_bf16 v[32:47], a[104:107], v[148:151], v[32:47]\n"
      "ds_read_b64_tr_b16 a[180:181], v215 offset:61440\n"
      "ds_read_b64_tr_b16 a[182:183], v215 offset:62464\n"
      "v_cvt_pk_bf16_f32 v202, v20, v21\n"
      "v_cvt_pk_bf16_f32 v203, v22, v23\n"
      "v_mfma_f32_32x32x16_bf16 v[48:63], a[108:111], v[148:151], v[48:63]\n"
      "ds_read_b64_tr_b16 a[184:185], v214 offset:63488\n"
      "ds_read_b64_tr_b16 a[186:187], v214 offset:64512\n"
      "ds_read_b64_tr_b16 a[188:189], v215 offset:63488\n"
      "buffer_store_dwordx4 v[200:203], v227, %6, s49 offen offset:2048 sc1\n"
      "v_cvt_pk_bf16_f32 v204, v24, v25\n"
      "v_mfma_f32_32x32x16_bf16 v[32:47], a[112:115], v[152:155], v[32:47]\n"
      "ds_read_b64_tr_b16 a[190:191], v215 offset:64512\n"
      "v_cvt_pk_bf16_f32 v205, v26, v27\n"
      "v_cvt_pk_bf16_f32 v206, v28, v29\n"
      "v_mfma_f32_32x32x16_bf16 v[48:63], a[116:119], v[152:155], v[48:63]\n"
      "v_cvt_pk_bf16_f32 v207, v30, v31\n"
      "buffer_store_dwordx4 v[204:207], v227, %6, s49 offen offset:3072 sc1\n"
      "v_mfma_f32_32x32x16_bf16 v[32:47], a[120:123], v[156:159], v[32:47]\n"
      "s_add_u32 s49, s49, 4096\n"
      "s_cmp_eq_u32 s49, 28672\n"
      "s_cselect_b32 s49, 0, s49\n"
      "v_mfma_f32_32x32x16_bf16 v[48:63], a[124:127], v[156:159], v[48:63]\n"
      "s_waitcnt lgkmcnt(0)\n"
      "v_mfma_f32_32x32x16_bf16 v[32:47], a[128:131], v[160:163], v[32:47]\n"
      "ds_read_b64_tr_b16 a[64:65], v212 offset:32768\n"
      "ds_read_b64_tr_b16 a[66:67], v212 offset:33792\n"
      "ds_read_b64_tr_b16 a[68:69], v213 offset:32768\n"
      "v_mfma_f32_32x32x16_bf16 v[48:63], a[132:135], v[160:163], v[48:63]\n"
      "ds_read_b64_tr_b16 a[70:71], v213 offset:33792\n"
      "ds_read_b64_tr_b16 a[72:73], v212 offset:34816\n"
      "ds_read_b64_tr_b16 a[74:75], v212 offset:35840\n"
      "v_mfma_f32_32x32x16_bf16 v[32:47], a[136:139], v[164:167], v[32:47]\n"
      "ds_read_b64_tr_b16 a[76:77], v213 offset:34816\n"
      "ds_read_b64_tr_b16 a[78:79], v213 offset:35840\n"
      "v_mfma_f32_32x32x16_bf16 v[48:63], a[140:143], v[164:167], v[48:63]\n"
      "ds_read_b64_tr_b16 a[80:81], v212 offset:36864\n"
      "ds_read_b64_tr_b16 a[82:83], v212 offset:37888\n"
      "ds_read_b64_tr_b16 a[84:85], v213 offset:36864\n"
      "v_mfma_f32_32x32x16_bf16 v[32:47], a[144:147], v[168:171], v[32:47]\n"
      "ds_read_b64_tr_b16 a[86:87], v213 offset:37888\n"
      "ds_read_b64_tr_b16 a[88:89], v212 offset:38912\n"
      "v_mfma_f32_32x32x16_bf16 v[48:63], a[148:151], v[168:171], v[48:63]\n"
      "ds_read_b64_tr_b16 a[90:91], v212 offset:39936\n"
      "ds_read_b64_tr_b16 a[92:93], v213 offset:38912\n"
      "ds_read_b64_tr_b16 a[94:95], v213 offset:39936\n"
      "v_mfma_f32_32x32x16_bf16 v[32:47], a[152:155], v[172:175], v[32:47]\n"
      "ds_read_b64_tr_b16 a[96:97], v212 offset:40960\n"
      "ds_read_b64_tr_b16 a[98:99], v212 offset:41984\n"
      "v_mfma_f32_32x32x16_bf16 v[48:63], a[156:159], v[172:175], v[48:63]\n"
      "ds_read_b64_tr_b16 a[100:101], v213 offset:40960\n"
      "ds_read_b64_tr_b16 a[102:103], v213 offset:41984\n"
      "ds_read_b64_tr_b16 a[104:105], v212 offset:43008\n"
      "v_mfma_f32_32x32x16_bf16 v[32:47], a[160:163], v[176:179], v[32:47]\n"
      "ds_read_b64_tr_b16 a[106:107], v212 offset:44032\n"
      "ds_read_b64_tr_b16 a[108:109], v213 offset:43008\n"
      "v_mfma_f32_32x32x16_bf16 v[48:63], a[164:167], v[176:179], v[48:63]\n"
      "ds_read_b64_tr_b16 a[110:111], v213 offset:44032\n"
      "ds_read_b64_tr_b16 a[112:113], v212 offset:45056\n"
      "ds_read_b64_tr_b16 a[114:115], v212 offset:46080\n"
      "v_mfma_f32_32x32x16_bf16 v[32:47], a[168:171], v[180:183], v[32:47]\n"
      "ds_read_b64_tr_b16 a[116:117], v213 offset:45056\n"
      "ds_read_b64_tr_b16 a[118:119], v213 offset:46080\n"
      "v_mfma_f32_32x32x16_bf16 v[48:63], a[172:175], v[180:183], v[48:63]\n"
      "ds_read_b64_tr_b16 a[120:121], v212 offset:47104\n"
      "ds_read_b64_tr_b16 a[122:123], v212 offset:48128\n"
      "ds_read_b64_tr_b16 a[124:125], v213 offset:47104\n"
      "v_mfma_f32_32x32x16_bf16 v[32:47], a[176:179], v[184:187], v[32:47]\n"
      "ds_read_b64_tr_b16 a[126:127], v213 offset:48128\n"
      "v_mfma_f32_32x32x16_bf16 v[48:63], a[180:183], v[184:187], v[48:63]\n"
      "v_mfma_f32_32x32x16_bf16 v[32:47], a[184:187], v[188:191], v[32:47]\n"
      "v_mfma_f32_32x32x16_bf16 v[48:63], a[188:191], v[188:191], v[48:63]\n"
      "s_waitcnt vmcnt(4)\n"
      "s_waitcnt vmcnt(4) lgkmcnt(0)\n"
      "s_barrier\n"
      "s_cmp_lg_u32 %14, 0\n"
      "s_cbranch_scc1 LcA_ef_%=\n"
      "v_mul_lo_u32 v230, %0, s72\n"
      "v_add_u32 v230, 0x9e3779b9, v230\n"
      "global_store_dword v233, v230, %15 sc1\n"
      "LcA_ef_%=:\n"
      "v_mfma_f32_32x32x16_bf16 v[0:15], a[64:67], v[128:131], 0\n"
      "ds_read_b64_tr_b16 a[128:129], v214 offset:0\n"
      "ds_read_b64_tr_b16 a[130:131], v214 offset:1024\n"
      "ds_read_b64_tr_b16 a[132:133], v215 offset:0\n"
      "v_mfma_f32_32x32x16_bf16 v[16:31], a[68:71], v[128:131], 0\n"
      "ds_read_b64_tr_b16 a[134:135], v215 offset:1024\n"
      "ds_read_b64_tr_b16 a[136:137], v214 offset:2048\n"
      "ds_read_b64_tr_b16 a[138:139], v214 offset:3072\n"
      "v_mfma_f32_32x32x16_bf16 v[0:15], a[72:75], v[132:135], v[0:15]\n"
      "ds_read_b64_tr_b16 a[140:141], v215 offset:2048\n"
      "ds_read_b64_tr_b16 a[142:143], v215 offset:3072\n"
      "v_mfma_f32_32x32x16_bf16 v[16:31], a[76:79], v[132:135], v[16:31]\n"
      "ds_read_b64_tr_b16 a[144:145], v214 offset:4096\n"
      "ds_read_b64_tr_b16 a[146:147], v214 offset:5120\n"
      "ds_read_b64_tr_b16 a[148:149], v215 offset:4096\n"
      "v_mfma_f32_32x32x16_bf16 v[0:15], a[80:83], v[136:139], v[0:15]\n"
      "ds_read_b64_tr_b16 a[150:151], v215 offset:5120\n"
      "ds_read_b64_tr_b16 a[152:153], v214 offset:6144\n"
      "v_cvt_pk_bf16_f32 v192, v32, v33\n"
      "v_cvt_pk_bf16_f32 v193, v34, v35\n"
      "v_mfma_f32_32x32x16_bf16 v[16:31], a[84:87], v[136:139], v[16:31]\n"
      "ds_read_b64_tr_b16 a[154:155], v214 offset:7168\n"
      "ds_read_b64_tr_b16 a[156:157], v215 offset:6144\n"
      "ds_read_b64_tr_b16 a[158:159], v215 offset:7168\n"
      "v_cvt_pk_bf16_f32 v194, v36, v37\n"
      "v_cvt_pk_bf16_f32 v195, v38, v39\n"
      "v_mfma_f32_32x32x16_bf16 v[0:15], a[88:91], v[140:143], v[0:15]\n"
      "ds_read_b64_tr_b16 a[160:161], v214 offset:8192\n"
      "ds_read_b64_tr_b16 a[162:163], v214 offset:9216\n"
      "buffer_store_dwordx4 v[192:195], v227, %6, s49 offen offset:0 sc1\n"
      "v_cvt_pk_bf16_f32 v196, v40, v41\n"
      "v_mfma_f32_32x32x16_bf16 v[16:31], a[92:95], v[140:143], v[16:31]\n"
      "ds_read_b64_tr_b16 a[164:165], v215 offset:8192\n"
      "ds_read_b64_tr_b16 a[166:167], v215 offset:9216\n"
      "ds_read_b64_tr_b16 a[168:169], v214 offset:10240\n"
      "v_cvt_pk_bf16_f32 v197, v42, v43\n"
      "v_cvt_pk_bf16_f32 v198, v44, v45\n"
      "v_mfma_f32_32x32x16_bf16 v[0:15], a[96:99], v[144:147], v[0:15]\n"
      "ds_read_b64_tr_b16 a[170:171], v214 offset:11264\n"
      "ds_read_b64_tr_b16 a[172:173], v215 offset:10240\n"
      "v_cvt_pk_bf16_f32 v199, v46, v47\n"
      "buffer_store_dwordx4 v[196:199], v227, %6, s49 offen offset:1024 sc1\n"
      "v_mfma_f32_32x32x16_bf16 v[16:31], a[100:103], v[144:147], v[16:31]\n"
      "ds_read_b64_tr_b16 a[174:175], v215 offset:11264\n"
      "ds_read_b64_tr_b16 a[176:177], v214 offset:12288\n"
      "ds_read_b64_tr_b16 a[178:179], v214 offset:13312\n"
      "v_cvt_pk_bf16_f32 v200, v48, v49\n"
      "v_cvt_pk_bf16_f32 v201, v50, v51\n"
      "v_mfma_f32_32x32x16_bf16 v[0:15], a[104:107], v[148:151], v[0:15]\n"
      "ds_read_b64_tr_b16 a[180:181], v215 offset:12288\n"
      "ds_read_b64_tr_b16 a[182:183], v215 offset:13312\n"
      "v_cvt_pk_bf16_f32 v202, v52, v53\n"
      "v_cvt_pk_bf16_f32 v203, v54, v55\n"
      "v_mfma_f32_32x32x16_bf16 v[16:31], a[108:111], v[148:151], v[16:31]\n"
      "ds_read_b64_tr_b16 a[184:185], v214 offset:14336\n"
      "ds_read_b64_tr_b16 a[186:187], v214 offset:15360\n"
      "ds_read_b64_tr_b16 a[188:189], v215 offset:14336\n"
      "buffer_store_dwordx4 v[200:203], v227, %6, s49 offen offset:2048 sc1\n"
      "v_cvt_pk_bf16_f32 v204, v56, v57\n"
      "v_mfma_f32_32x32x16_bf16 v[0:15], a[112:115], v[152:155], v[0:15]\n"
      "ds_read_b64_tr_b16 a[190:191], v215 offset:15360\n"
      "v_cvt_pk_bf16_f32 v205, v58, v59\n"
      "v_cvt_pk_bf16_f32 v206, v60, v61\n"
      "v_mfma_f32_32x32x16_bf16 v[16:31], a[116:119], v[152:155], v[16:31]\n"
      "v_cvt_pk_bf16_f32 v207, v62, v63\n"
      "buffer_store_dwordx4 v[204:207], v227, %6, s49 offen offset:3072 sc1\n"
      "v_mfma_f32_32x32x16_bf16 v[0:15], a[120:123], v[156:159], v[0:15]\n"
      "s_add_u32 s49, s49, 4096\n"
      "s_cmp_eq_u32 s49, 28672\n"
      "s_cselect_b32 s49, 0, s49\n"
      "v_mfma_f32_32x32x16_bf16 v[16:31], a[124:127], v[156:159], v[16:31]\n"
      "s_waitcnt lgkmcnt(0)\n"
      "v_mfma_f32_32x32x16_bf16 v[0:15], a[128:131], v[160:163], v[0:15]\n"
      "v_mfma_f32_32x32x16_bf16 v[16:31], a[132:135], v[160:163], v[16:31]\n"
      "v_mfma_f32_32x32x16_bf16 v[0:15], a[136:139], v[164:167], v[0:15]\n"
      "v_mfma_f32_32x32x16_bf16 v[16:31], a[140:143], v[164:167], v[16:31]\n"
      "v_mfma_f32_32x32x16_bf16 v[0:15], a[144:147], v[168:171], v[0:15]\n"
      "v_mfma_f32_32x32x16_bf16 v[16:31], a[148:151], v[168:171], v[16:31]\n"
      "v_mfma_f32_32x32x16_bf16 v[0:15], a[152:155], v[172:175], v[0:15]\n"
      "v_mfma_f32_32x32x16_bf16 v[16:31], a[156:159], v[172:175], v[16:31]\n"
      "v_mfma_f32_32x32x16_bf16 v[0:15], a[160:163], v[176:179], v[0:15]\n"
      "v_mfma_f32_32x32x16_bf16 v[16:31], a[164:167], v[176:179], v[16:31]\n"
      "v_mfma_f32_32x32x16_bf16 v[0:15], a[168:171], v[180:183], v[0:15]\n"
      "v_mfma_f32_32x32x16_bf16 v[16:31], a[172:175], v[180:183], v[16:31]\n"
      "v_mfma_f32_32x32x16_bf16 v[0:15], a[176:179], v[184:187], v[0:15]\n"
      "v_mfma_f32_32x32x16_bf16 v[16:31], a[180:183], v[184:187], v[16:31]\n"
      "v_mfma_f32_32x32x16_bf16 v[0:15], a[184:187], v[188:191], v[0:15]\n"
      "v_mfma_f32_32x32x16_bf16 v[16:31], a[188:191], v[188:191], v[16:31]\n"
      "s_nop 10\n"
      "v_cvt_pk_bf16_f32 v192, v0, v1\n"
      "v_cvt_pk_bf16_f32 v193, v2, v3\n"
      "v_cvt_pk_bf16_f32 v194, v4, v5\n"
      "v_cvt_pk_bf16_f32 v195, v6, v7\n"
      "buffer_store_dwordx4 v[192:195], v227, %6, s49 offen offset:0 sc1\n"
      "v_cvt_pk_bf16_f32 v196, v8, v9\n"
      "v_cvt_pk_bf16_f32 v197, v10, v11\n"
      "v_cvt_pk_bf16_f32 v198, v12, v13\n"
      "v_cvt_pk_bf16_f32 v199, v14, v15\n"
      "buffer_store_dwordx4 v[196:199], v227, %6, s49 offen offset:1024 sc1\n"
      "v_cvt_pk_bf16_f32 v200, v16, v17\n"
      "v_cvt_pk_bf16_f32 v201, v18, v19\n"
      "v_cvt_pk_bf16_f32 v202, v20, v21\n"
      "v_cvt_pk_bf16_f32 v203, v22, v23\n"
      "buffer_store_dwordx4 v[200:203], v227, %6, s49 offen offset:2048 sc1\n"
      "v_cvt_pk_bf16_f32 v204, v24, v25\n"
      "v_cvt_pk_bf16_f32 v205, v26, v27\n"
      "v_cvt_pk_bf16_f32 v206, v28, v29\n"
      "v_cvt_pk_bf16_f32 v207, v30, v31\n"
      "buffer_store_dwordx4 v[204:207], v227, %6, s49 offen offset:3072 sc1\n"
      "s_add_u32 s49, s49, 4096\n"
      "s_cmp_eq_u32 s49, 28672\n"
      "s_cselect_b32 s49, 0, s49\n"
      "s_branch Lend_%=\n"
      "LcA_RA1_%=:\n"
      "v_mov_b32 v196, v194\n"
      "s_nop 1\n"
      "v_permlane32_swap_b32 v196, v194\n"
      "s_nop 1\n"
      "v_max_f32 v194, v194, v196\n"
      "v_max_f32 v196, 0, v194\n"
      "v_sub_f32 v144, v144, v196\n"
      "v_sub_f32 v145, v145, v196\n"
      "v_sub_f32 v146, v146, v196\n"
      "v_sub_f32 v147, v147, v196\n"
      "v_sub_f32 v148, v148, v196\n"
      "v_sub_f32 v149, v149, v196\n"
      "v_sub_f32 v150, v150, v196\n"
      "v_sub_f32 v151, v151, v196\n"
      "v_sub_f32 v152, v152, v196\n"
      "v_sub_f32 v153, v153, v196\n"
      "v_sub_f32 v154, v154, v196\n"
      "v_sub_f32 v155, v155, v196\n"
      "v_sub_f32 v156, v156, v196\n"
      "v_sub_f32 v157, v157, v196\n"
      "v_sub_f32 v158, v158, v196\n"
      "v_sub_f32 v159, v159, v196\n"
      "v_sub_f32 v160, v160, v196\n"
      "v_sub_f32 v161, v161, v196\n"
      "v_sub_f32 v162, v162, v196\n"
      "v_sub_f32 v163, v163, v196\n"
      "v_sub_f32 v164, v164, v196\n"
      "v_sub_f32 v165, v165, v196\n"
      "v_sub_f32 v166, v166, v196\n"
      "v_sub_f32 v167, v167, v196\n"
      "v_sub_f32 v168, v168, v196\n"
      "v_sub_f32 v169, v169, v196\n"
      "v_sub_f32 v170, v170, v196\n"
      "v_sub_f32 v171, v171, v196\n"
      "v_sub_f32 v172, v172, v196\n"
      "v_sub_f32 v173, v173, v196\n"
      "v_sub_f32 v174, v174, v196\n"
      "v_sub_f32 v175, v175, v196\n"
      "v_sub_f32 v196, 0, v196\n"
      "v_exp_f32 v197, v196\n"
      "s_nop 1\n"
      "v_mul_f32 v192, v192, v197\n"
      "v_mul_f32 v193, v193, v197\n"
      "s_mov_b32 s50, 1\n"
      "s_nop 1\n"
      "s_branch LcA_RRA1_%=\n"
      "LcA_RB1_%=:\n"
      "s_nop 15\n"
      "v_pk_mul_f32 v[0:1], v[0:1], v[196:197] op_sel:[0,1] op_sel_hi:[1,1]\n"
      "v_pk_mul_f32 v[2:3], v[2:3], v[196:197] op_sel:[0,1] op_sel_hi:[1,1]\n"
      "v_pk_mul_f32 v[4:5], v[4:5], v[196:197] op_sel:[0,1] op_sel_hi:[1,1]\n"
      "v_pk_mul_f32 v[6:7], v[6:7], v[196:197] op_sel:[0,1] op_sel_hi:[1,1]\n"
      "v_pk_mul_f32 v[8:9], v[8:9], v[196:197] op_sel:[0,1] op_sel_hi:[1,1]\n"
      "v_pk_mul_f32 v[10:11], v[10:11], v[196:197] op_sel:[0,1] op_sel_hi:[1,1]\n"
      "v_pk_mul_f32 v[12:13], v[12:13], v[196:197] op_sel:[0,1] op_sel_hi:[1,1]\n"
      "v_pk_mul_f32 v[14:15], v[14:15], v[196:197] op_sel:[0,1] op_sel_hi:[1,1]\n"
      "v_pk_mul_f32 v[16:17], v[16:17], v[196:197] op_sel:[0,1] op_sel_hi:[1,1]\n"
      "v_pk_mul_f32 v[18:19], v[18:19], v[196:197] op_sel:[0,1] op_sel_hi:[1,1]\n"
      "v_pk_mul_f32 v[20:21], v[20:21], v[196:197] op_sel:[0,1] op_sel_hi:[1,1]\n"
      "v_pk_mul_f32 v[22:23], v[22:23], v[196:197] op_sel:[0,1] op_sel_hi:[1,1]\n"
      "v_pk_mul_f32 v[24:25], v[24:25], v[196:197] op_sel:[0,1] op_sel_hi:[1,1]\n"
      "v_pk_mul_f32 v[26:27], v[26:27], v[196:197] op_sel:[0,1] op_sel_hi:[1,1]\n"
      "v_pk_mul_f32 v[28:29], v[28:29], v[196:197] op_sel:[0,1] op_sel_hi:[1,1]\n"
      "v_pk_mul_f32 v[30:31], v[30:31], v[196:197] op_sel:[0,1] op_sel_hi:[1,1]\n"
      "v_pk_mul_f32 v[32:33], v[32:33], v[196:197] op_sel:[0,1] op_sel_hi:[1,1]\n"
      "v_pk_mul_f32 v[34:35], v[34:35], v[196:197] op_sel:[0,1] op_sel_hi:[1,1]\n"
      "v_pk_mul_f32 v[36:37], v[36:37], v[196:197] op_sel:[0,1] op_sel_hi:[1,1]\n"
      "v_pk_mul_f32 v[38:39], v[38:39], v[196:197] op_sel:[0,1] op_sel_hi:[1,1]\n"
      "v_pk_mul_f32 v[40:41], v[40:41], v[196:197] op_sel:[0,1] op_sel_hi:[1,1]\n"
      "v_pk_mul_f32 v[42:43], v[42:43], v[196:197] op_sel:[0,1] op_sel_hi:[1,1]\n"
      "v_pk_mul_f32 v[44:45], v[44:45], v[196:197] op_sel:[0,1] op_sel_hi:[1,1]\n"
      "v_pk_mul_f32 v[46:47], v[46:47], v[196:197] op_sel:[0,1] op_sel_hi:[1,1]\n"
      "v_pk_mul_f32 v[48:49], v[48:49], v[196:197] op_sel:[0,1] op_sel_hi:[1,1]\n"
      "v_pk_mul_f32 v[50:51], v[50:51], v[196:197] op_sel:[0,1] op_sel_hi:[1,1]\n"
      "v_pk_mul_f32 v[52:53], v[52:53], v[196:197] op_sel:[0,1] op_sel_hi:[1,1]\n"
      "v_pk_mul_f32 v[54:55], v[54:55], v[196:197] op_sel:[0,1] op_sel_hi:[1,1]\n"
      "v_pk_mul_f32 v[56:57], v[56:57], v[196:197] op_sel:[0,1] op_sel_hi:[1,1]\n"
      "v_pk_mul_f32 v[58:59], v[58:59], v[196:197] op_sel:[0,1] op_sel_hi:[1,1]\n"
      "v_pk_mul_f32 v[60:61], v[60:61], v[196:197] op_sel:[0,1] op_sel_hi:[1,1]\n"
      "v_pk_mul_f32 v[62:63], v[62:63], v[196:197] op_sel:[0,1] op_sel_hi:[1,1]\n"
      "v_pk_mul_f32 v[64:65], v[64:65], v[196:197] op_sel:[0,1] op_sel_hi:[1,1]\n"
      "v_pk_mul_f32 v[66:67], v[66:67], v[196:197] op_sel:[0,1] op_sel_hi:[1,1]\n"
      "v_pk_mul_f32 v[68:69], v[68:69], v[196:197] op_sel:[0,1] op_sel_hi:[1,1]\n"
      "v_pk_mul_f32 v[70:71], v[70:71], v[196:197] op_sel:[0,1] op_sel_hi:[1,1]\n"
      "v_pk_mul_f32 v[72:73], v[72:73], v[196:197] op_sel:[0,1] op_sel_hi:[1,1]\n"
      "v_pk_mul_f32 v[74:75], v[74:75], v[196:197] op_sel:[0,1] op_sel_hi:[1,1]\n"
      "v_pk_mul_f32 v[76:77], v[76:77], v[196:197] op_sel:[0,1] op_sel_hi:[1,1]\n"
      "v_pk_mul_f32 v[78:79], v[78:79], v[196:197] op_sel:[0,1] op_sel_hi:[1,1]\n"
      "v_pk_mul_f32 v[80:81], v[80:81], v[196:197] op_sel:[0,1] op_sel_hi:[1,1]\n"
      "v_pk_mul_f32 v[82:83], v[82:83], v[196:197] op_sel:[0,1] op_sel_hi:[1,1]\n"
      "v_pk_mul_f32 v[84:85], v[84:85], v[196:197] op_sel:[0,1] op_sel_hi:[1,1]\n"
      "v_pk_mul_f32 v[86:87], v[86:87], v[196:197] op_sel:[0,1] op_sel_hi:[1,1]\n"
      "v_pk_mul_f32 v[88:89], v[88:89], v[196:197] op_sel:[0,1] op_sel_hi:[1,1]\n"
      "v_pk_mul_f32 v[90:91], v[90:91], v[196:197] op_sel:[0,1] op_sel_hi:[1,1]\n"
      "v_pk_mul_f32 v[92:93], v[92:93], v[196:197] op_sel:[0,1] op_sel_hi:[1,1]\n"
      "v_pk_mul_f32 v[94:95], v[94:95], v[196:197] op_sel:[0,1] op_sel_hi:[1,1]\n"
      "v_pk_mul_f32 v[96:97], v[96:97], v[196:197] op_sel:[0,1] op_sel_hi:[1,1]\n"
      "v_pk_mul_f32 v[98:99], v[98:99], v[196:197] op_sel:[0,1] op_sel_hi:[1,1]\n"
      "v_pk_mul_f32 v[100:101], v[100:101], v[196:197] op_sel:[0,1] op_sel_hi:[1,1]\n"
      "v_pk_mul_f32 v[102:103], v[102:103], v[196:197] op_sel:[0,1] op_sel_hi:[1,1]\n"
      "v_pk_mul_f32 v[104:105], v[104:105], v[196:197] op_sel:[0,1] op_sel_hi:[1,1]\n"
      "v_pk_mul_f32 v[106:107], v[106:107], v[196:197] op_sel:[0,1] op_sel_hi:[1,1]\n"
      "v_pk_mul_f32 v[108:109], v[108:109], v[196:197] op_sel:[0,1] op_sel_hi:[1,1]\n"
      "v_pk_mul_f32 v[110:111], v[110:111], v[196:197] op_sel:[0,1] op_sel_hi:[1,1]\n"
      "v_pk_mul_f32 v[112:113], v[112:113], v[196:197] op_sel:[0,1] op_sel_hi:[1,1]\n"
      "v_pk_mul_f32 v[114:115], v[114:115], v[196:197] op_sel:[0,1] op_sel_hi:[1,1]\n"
      "v_pk_mul_f32 v[116:117], v[116:117], v[196:197] op_sel:[0,1] op_sel_hi:[1,1]\n"
      "v_pk_mul_f32 v[118:119], v[118:119], v[196:197] op_sel:[0,1] op_sel_hi:[1,1]\n"
      "v_pk_mul_f32 v[120:121], v[120:121], v[196:197] op_sel:[0,1] op_sel_hi:[1,1]\n"
      "v_pk_mul_f32 v[122:123], v[122:123], v[196:197] op_sel:[0,1] op_sel_hi:[1,1]\n"
      "v_pk_mul_f32 v[124:125], v[124:125], v[196:197] op_sel:[0,1] op_sel_hi:[1,1]\n"
      "v_pk_mul_f32 v[126:127], v[126:127], v[196:197] op_sel:[0,1] op_sel_hi:[1,1]\n"
      "s_mov_b32 s50, 0\n"
      "s_nop 1\n"
      "s_branch LcA_RRB1_%=\n"
      "LcA_RA2_%=:\n"
      "v_mov_b32 v196, v194\n"
      "s_nop 1\n"
      "v_permlane32_swap_b32 v196, v194\n"
      "s_nop 1\n"
      "v_max_f32 v194, v194, v196\n"
      "v_max_f32 v196, 0, v194\n"
      "v_sub_f32 v128, v128, v196\n"
      "v_sub_f32 v129, v129, v196\n"
      "v_sub_f32 v130, v130, v196\n"
      "v_sub_f32 v131, v131, v196\n"
      "v_sub_f32 v132, v132, v196\n"
      "v_sub_f32 v133, v133, v196\n"
      "v_sub_f32 v134, v134, v196\n"
      "v_sub_f32 v135, v135, v196\n"
      "v_sub_f32 v136, v136, v196\n"
      "v_sub_f32 v137, v137, v196\n"
      "v_sub_f32 v138, v138, v196\n"
      "v_sub_f32 v139, v139, v196\n"
      "v_sub_f32 v140, v140, v196\n"
      "v_sub_f32 v141, v141, v196\n"
      "v_sub_f32 v142, v142, v196\n"
      "v_sub_f32 v143, v143, v196\n"
      "v_sub_f32 v160, v160, v196\n"
      "v_sub_f32 v161, v161, v196\n"
      "v_sub_f32 v162, v162, v196\n"
      "v_sub_f32 v163, v163, v196\n"
      "v_sub_f32 v164, v164, v196\n"
      "v_sub_f32 v165, v165, v196\n"
      "v_sub_f32 v166, v166, v196\n"
      "v_sub_f32 v167, v167, v196\n"
      "v_sub_f32 v168, v168, v196\n"
      "v_sub_f32 v169, v169, v196\n"
      "v_sub_f32 v170, v170, v196\n"
      "v_sub_f32 v171, v171, v196\n"
      "v_sub_f32 v172, v172, v196\n"
      "v_sub_f32 v173, v173, v196\n"
      "v_sub_f32 v174, v174, v196\n"
      "v_sub_f32 v175, v175, v196\n"
      "v_sub_f32 v196, 0, v196\n"
      "v_exp_f32 v197, v196\n"
      "s_nop 1\n"
      "v_mul_f32 v192, v192, v197\n"
      "v_mul_f32 v193, v193, v197\n"
      "s_mov_b32 s50, 1\n"
      "s_nop 1\n"
      "s_branch LcA_RRA2_%=\n"
      "LcA_RB2_%=:\n"
      "s_nop 15\n"
      "v_pk_mul_f32 v[0:1], v[0:1], v[196:197] op_sel:[0,1] op_sel_hi:[1,1]\n"
      "v_pk_mul_f32 v[2:3], v[2:3], v[196:197] op_sel:[0,1] op_sel_hi:[1,1]\n"
      "v_pk_mul_f32 v[4:5], v[4:5], v[196:197] op_sel:[0,1] op_sel_hi:[1,1]\n"
      "v_pk_mul_f32 v[6:7], v[6:7], v[196:197] op_sel:[0,1] op_sel_hi:[1,1]\n"
      "v_pk_mul_f32 v[8:9], v[8:9], v[196:197] op_sel:[0,1] op_sel_hi:[1,1]\n"
      "v_pk_mul_f32 v[10:11], v[10:11], v[196:197] op_sel:[0,1] op_sel_hi:[1,1]\n"
      "v_pk_mul_f32 v[12:13], v[12:13], v[196:197] op_sel:[0,1] op_sel_hi:[1,1]\n"
      "v_pk_mul_f32 v[14:15], v[14:15], v[196:197] op_sel:[0,1] op_sel_hi:[1,1]\n"
      "v_pk_mul_f32 v[16:17], v[16:17], v[196:197] op_sel:[0,1] op_sel_hi:[1,1]\n"
      "v_pk_mul_f32 v[18:19], v[18:19], v[196:197] op_sel:[0,1] op_sel_hi:[1,1]\n"
      "v_pk_mul_f32 v[20:21], v[20:21], v[196:197] op_sel:[0,1] op_sel_hi:[1,1]\n"
      "v_pk_mul_f32 v[22:23], v[22:23], v[196:197] op_sel:[0,1] op_sel_hi:[1,1]\n"
      "v_pk_mul_f32 v[24:25], v[24:25], v[196:197] op_sel:[0,1] op_sel_hi:[1,1]\n"
      "v_pk_mul_f32 v[26:27], v[26:27], v[196:197] op_sel:[0,1] op_sel_hi:[1,1]\n"
      "v_pk_mul_f32 v[28:29], v[28:29], v[196:197] op_sel:[0,1] op_sel_hi:[1,1]\n"
      "v_pk_mul_f32 v[30:31], v[30:31], v[196:197] op_sel:[0,1] op_sel_hi:[1,1]\n"
      "v_pk_mul_f32 v[32:33], v[32:33], v[196:197] op_sel:[0,1] op_sel_hi:[1,1]\n"
      "v_pk_mul_f32 v[34:35], v[34:35], v[196:197] op_sel:[0,1] op_sel_hi:[1,1]\n"
      "v_pk_mul_f32 v[36:37], v[36:37], v[196:197] op_sel:[0,1] op_sel_hi:[1,1]\n"
      "v_pk_mul_f32 v[38:39], v[38:39], v[196:197] op_sel:[0,1] op_sel_hi:[1,1]\n"
      "v_pk_mul_f32 v[40:41], v[40:41], v[196:197] op_sel:[0,1] op_sel_hi:[1,1]\n"
      "v_pk_mul_f32 v[42:43], v[42:43], v[196:197] op_sel:[0,1] op_sel_hi:[1,1]\n"
      "v_pk_mul_f32 v[44:45], v[44:45], v[196:197] op_sel:[0,1] op_sel_hi:[1,1]\n"
      "v_pk_mul_f32 v[46:47], v[46:47], v[196:197] op_sel:[0,1] op_sel_hi:[1,1]\n"
      "v_pk_mul_f32 v[48:49], v[48:49], v[196:197] op_sel:[0,1] op_sel_hi:[1,1]\n"
      "v_pk_mul_f32 v[50:51], v[50:51], v[196:197] op_sel:[0,1] op_sel_hi:[1,1]\n"
      "v_pk_mul_f32 v[52:53], v[52:53], v[196:197] op_sel:[0,1] op_sel_hi:[1,1]\n"
      "v_pk_mul_f32 v[54:55], v[54:55], v[196:197] op_sel:[0,1] op_sel_hi:[1,1]\n"
      "v_pk_mul_f32 v[56:57], v[56:57], v[196:197] op_sel:[0,1] op_sel_hi:[1,1]\n"
      "v_pk_mul_f32 v[58:59], v[58:59], v[196:197] op_sel:[0,1] op_sel_hi:[1,1]\n"
      "v_pk_mul_f32 v[60:61], v[60:61], v[196:197] op_sel:[0,1] op_sel_hi:[1,1]\n"
      "v_pk_mul_f32 v[62:63], v[62:63], v[196:197] op_sel:[0,1] op_sel_hi:[1,1]\n"
      "v_pk_mul_f32 v[64:65], v[64:65], v[196:197] op_sel:[0,1] op_sel_hi:[1,1]\n"
      "v_pk_mul_f32 v[66:67], v[66:67], v[196:197] op_sel:[0,1] op_sel_hi:[1,1]\n"
      "v_pk_mul_f32 v[68:69], v[68:69], v[196:197] op_sel:[0,1] op_sel_hi:[1,1]\n"
      "v_pk_mul_f32 v[70:71], v[70:71], v[196:197] op_sel:[0,1] op_sel_hi:[1,1]\n"
      "v_pk_mul_f32 v[72:73], v[72:73], v[196:197] op_sel:[0,1] op_sel_hi:[1,1]\n"
      "v_pk_mul_f32 v[74:75], v[74:75], v[196:197] op_sel:[0,1] op_sel_hi:[1,1]\n"
      "v_pk_mul_f32 v[76:77], v[76:77], v[196:197] op_sel:[0,1] op_sel_hi:[1,1]\n"
      "v_pk_mul_f32 v[78:79], v[78:79], v[196:197] op_sel:[0,1] op_sel_hi:[1,1]\n"
      "v_pk_mul_f32 v[80:81], v[80:81], v[196:197] op_sel:[0,1] op_sel_hi:[1,1]\n"
      "v_pk_mul_f32 v[82:83], v[82:83], v[196:197] op_sel:[0,1] op_sel_hi:[1,1]\n"
      "v_pk_mul_f32 v[84:85], v[84:85], v[196:197] op_sel:[0,1] op_sel_hi:[1,1]\n"
      "v_pk_mul_f32 v[86:87], v[86:87], v[196:197] op_sel:[0,1] op_sel_hi:[1,1]\n"
      "v_pk_mul_f32 v[88:89], v[88:89], v[196:197] op_sel:[0,1] op_sel_hi:[1,1]\n"
      "v_pk_mul_f32 v[90:91], v[90:91], v[196:197] op_sel:[0,1] op_sel_hi:[1,1]\n"
      "v_pk_mul_f32 v[92:93], v[92:93], v[196:197] op_sel:[0,1] op_sel_hi:[1,1]\n"
      "v_pk_mul_f32 v[94:95], v[94:95], v[196:197] op_sel:[0,1] op_sel_hi:[1,1]\n"
      "v_pk_mul_f32 v[96:97], v[96:97], v[196:197] op_sel:[0,1] op_sel_hi:[1,1]\n"
      "v_pk_mul_f32 v[98:99], v[98:99], v[196:197] op_sel:[0,1] op_sel_hi:[1,1]\n"
      "v_pk_mul_f32 v[100:101], v[100:101], v[196:197] op_sel:[0,1] op_sel_hi:[1,1]\n"
      "v_pk_mul_f32 v[102:103], v[102:103], v[196:197] op_sel:[0,1] op_sel_hi:[1,1]\n"
      "v_pk_mul_f32 v[104:105], v[104:105], v[196:197] op_sel:[0,1] op_sel_hi:[1,1]\n"
      "v_pk_mul_f32 v[106:107], v[106:107], v[196:197] op_sel:[0,1] op_sel_hi:[1,1]\n"
      "v_pk_mul_f32 v[108:109], v[108:109], v[196:197] op_sel:[0,1] op_sel_hi:[1,1]\n"
      "v_pk_mul_f32 v[110:111], v[110:111], v[196:197] op_sel:[0,1] op_sel_hi:[1,1]\n"
      "v_pk_mul_f32 v[112:113], v[112:113], v[196:197] op_sel:[0,1] op_sel_hi:[1,1]\n"
      "v_pk_mul_f32 v[114:115], v[114:115], v[196:197] op_sel:[0,1] op_sel_hi:[1,1]\n"
      "v_pk_mul_f32 v[116:117], v[116:117], v[196:197] op_sel:[0,1] op_sel_hi:[1,1]\n"
      "v_pk_mul_f32 v[118:119], v[118:119], v[196:197] op_sel:[0,1] op_sel_hi:[1,1]\n"
      "v_pk_mul_f32 v[120:121], v[120:121], v[196:197] op_sel:[0,1] op_sel_hi:[1,1]\n"
      "v_pk_mul_f32 v[122:123], v[122:123], v[196:197] op_sel:[0,1] op_sel_hi:[1,1]\n"
      "v_pk_mul_f32 v[124:125], v[124:125], v[196:197] op_sel:[0,1] op_sel_hi:[1,1]\n"
      "v_pk_mul_f32 v[126:127], v[126:127], v[196:197] op_sel:[0,1] op_sel_hi:[1,1]\n"
      "s_mov_b32 s50, 0\n"
      "s_nop 1\n"
      "s_branch LcA_RRB2_%=\n"
      "LcA_RA3_%=:\n"
      "v_mov_b32 v196, v194\n"
      "s_nop 1\n"
      "v_permlane32_swap_b32 v196, v194\n"
      "s_nop 1\n"
      "v_max_f32 v194, v194, v196\n"
      "v_max_f32 v196, 0, v194\n"
      "v_sub_f32 v144, v144, v196\n"
      "v_sub_f32 v145, v145, v196\n"
      "v_sub_f32 v146, v146, v196\n"
      "v_sub_f32 v147, v147, v196\n"
      "v_sub_f32 v148, v148, v196\n"
      "v_sub_f32 v149, v149, v196\n"
      "v_sub_f32 v150, v150, v196\n"
      "v_sub_f32 v151, v151, v196\n"
      "v_sub_f32 v152, v152, v196\n"
      "v_sub_f32 v153, v153, v196\n"
      "v_sub_f32 v154, v154, v196\n"
      "v_sub_f32 v155, v155, v196\n"
      "v_sub_f32 v156, v156, v196\n"
      "v_sub_f32 v157, v157, v196\n"
      "v_sub_f32 v158, v158, v196\n"
      "v_sub_f32 v159, v159, v196\n"
      "v_sub_f32 v160, v160, v196\n"
      "v_sub_f32 v161, v161, v196\n"
      "v_sub_f32 v162, v162, v196\n"
      "v_sub_f32 v163, v163, v196\n"
      "v_sub_f32 v164, v164, v196\n"
      "v_sub_f32 v165, v165, v196\n"
      "v_sub_f32 v166, v166, v196\n"
      "v_sub_f32 v167, v167, v196\n"
      "v_sub_f32 v168, v168, v196\n"
      "v_sub_f32 v169, v169, v196\n"
      "v_sub_f32 v170, v170, v196\n"
      "v_sub_f32 v171, v171, v196\n"
      "v_sub_f32 v172, v172, v196\n"
      "v_sub_f32 v173, v173, v196\n"
      "v_sub_f32 v174, v174, v196\n"
      "v_sub_f32 v175, v175, v196\n"
      "v_sub_f32 v196, 0, v196\n"
      "v_exp_f32 v197, v196\n"
      "s_nop 1\n"
      "v_mul_f32 v192, v192, v197\n"
      "v_mul_f32 v193, v193, v197\n"
      "s_mov_b32 s50, 1\n"
      "s_nop 1\n"
      "s_branch LcA_RRA3_%=\n"
      "LcA_RB3_%=:\n"
      "s_nop 15\n"
      "v_pk_mul_f32 v[0:1], v[0:1], v[196:197] op_sel:[0,1] op_sel_hi:[1,1]\n"
      "v_pk_mul_f32 v[2:3], v[2:3], v[196:197] op_sel:[0,1] op_sel_hi:[1,1]\n"
      "v_pk_mul_f32 v[4:5], v[4:5], v[196:197] op_sel:[0,1] op_sel_hi:[1,1]\n"
      "v_pk_mul_f32 v[6:7], v[6:7], v[196:197] op_sel:[0,1] op_sel_hi:[1,1]\n"
      "v_pk_mul_f32 v[8:9], v[8:9], v[196:197] op_sel:[0,1] op_sel_hi:[1,1]\n"
      "v_pk_mul_f32 v[10:11], v[10:11], v[196:197] op_sel:[0,1] op_sel_hi:[1,1]\n"
      "v_pk_mul_f32 v[12:13], v[12:13], v[196:197] op_sel:[0,1] op_sel_hi:[1,1]\n"
      "v_pk_mul_f32 v[14:15], v[14:15], v[196:197] op_sel:[0,1] op_sel_hi:[1,1]\n"
      "v_pk_mul_f32 v[16:17], v[16:17], v[196:197] op_sel:[0,1] op_sel_hi:[1,1]\n"
      "v_pk_mul_f32 v[18:19], v[18:19], v[196:197] op_sel:[0,1] op_sel_hi:[1,1]\n"
      "v_pk_mul_f32 v[20:21], v[20:21], v[196:197] op_sel:[0,1] op_sel_hi:[1,1]\n"
      "v_pk_mul_f32 v[22:23], v[22:23], v[196:197] op_sel:[0,1] op_sel_hi:[1,1]\n"
      "v_pk_mul_f32 v[24:25], v[24:25], v[196:197] op_sel:[0,1] op_sel_hi:[1,1]\n"
      "v_pk_mul_f32 v[26:27], v[26:27], v[196:197] op_sel:[0,1] op_sel_hi:[1,1]\n"
      "v_pk_mul_f32 v[28:29], v[28:29], v[196:197] op_sel:[0,1] op_sel_hi:[1,1]\n"
      "v_pk_mul_f32 v[30:31], v[30:31], v[196:197] op_sel:[0,1] op_sel_hi:[1,1]\n"
      "v_pk_mul_f32 v[32:33], v[32:33], v[196:197] op_sel:[0,1] op_sel_hi:[1,1]\n"
      "v_pk_mul_f32 v[34:35], v[34:35], v[196:197] op_sel:[0,1] op_sel_hi:[1,1]\n"
      "v_pk_mul_f32 v[36:37], v[36:37], v[196:197] op_sel:[0,1] op_sel_hi:[1,1]\n"
      "v_pk_mul_f32 v[38:39], v[38:39], v[196:197] op_sel:[0,1] op_sel_hi:[1,1]\n"
      "v_pk_mul_f32 v[40:41], v[40:41], v[196:197] op_sel:[0,1] op_sel_hi:[1,1]\n"
      "v_pk_mul_f32 v[42:43], v[42:43], v[196:197] op_sel:[0,1] op_sel_hi:[1,1]\n"
      "v_pk_mul_f32 v[44:45], v[44:45], v[196:197] op_sel:[0,1] op_sel_hi:[1,1]\n"
      "v_pk_mul_f32 v[46:47], v[46:47], v[196:197] op_sel:[0,1] op_sel_hi:[1,1]\n"
      "v_pk_mul_f32 v[48:49], v[48:49], v[196:197] op_sel:[0,1] op_sel_hi:[1,1]\n"
      "v_pk_mul_f32 v[50:51], v[50:51], v[196:197] op_sel:[0,1] op_sel_hi:[1,1]\n"
      "v_pk_mul_f32 v[52:53], v[52:53], v[196:197] op_sel:[0,1] op_sel_hi:[1,1]\n"
      "v_pk_mul_f32 v[54:55], v[54:55], v[196:197] op_sel:[0,1] op_sel_hi:[1,1]\n"
      "v_pk_mul_f32 v[56:57], v[56:57], v[196:197] op_sel:[0,1] op_sel_hi:[1,1]\n"
      "v_pk_mul_f32 v[58:59], v[58:59], v[196:197] op_sel:[0,1] op_sel_hi:[1,1]\n"
      "v_pk_mul_f32 v[60:61], v[60:61], v[196:197] op_sel:[0,1] op_sel_hi:[1,1]\n"
      "v_pk_mul_f32 v[62:63], v[62:63], v[196:197] op_sel:[0,1] op_sel_hi:[1,1]\n"
      "v_pk_mul_f32 v[64:65], v[64:65], v[196:197] op_sel:[0,1] op_sel_hi:[1,1]\n"
      "v_pk_mul_f32 v[66:67], v[66:67], v[196:197] op_sel:[0,1] op_sel_hi:[1,1]\n"
      "v_pk_mul_f32 v[68:69], v[68:69], v[196:197] op_sel:[0,1] op_sel_hi:[1,1]\n"
      "v_pk_mul_f32 v[70:71], v[70:71], v[196:197] op_sel:[0,1] op_sel_hi:[1,1]\n"
      "v_pk_mul_f32 v[72:73], v[72:73], v[196:197] op_sel:[0,1] op_sel_hi:[1,1]\n"
      "v_pk_mul_f32 v[74:75], v[74:75], v[196:197] op_sel:[0,1] op_sel_hi:[1,1]\n"
      "v_pk_mul_f32 v[76:77], v[76:77], v[196:197] op_sel:[0,1] op_sel_hi:[1,1]\n"
      "v_pk_mul_f32 v[78:79], v[78:79], v[196:197] op_sel:[0,1] op_sel_hi:[1,1]\n"
      "v_pk_mul_f32 v[80:81], v[80:81], v[196:197] op_sel:[0,1] op_sel_hi:[1,1]\n"
      "v_pk_mul_f32 v[82:83], v[82:83], v[196:197] op_sel:[0,1] op_sel_hi:[1,1]\n"
      "v_pk_mul_f32 v[84:85], v[84:85], v[196:197] op_sel:[0,1] op_sel_hi:[1,1]\n"
      "v_pk_mul_f32 v[86:87], v[86:87], v[196:197] op_sel:[0,1] op_sel_hi:[1,1]\n"
      "v_pk_mul_f32 v[88:89], v[88:89], v[196:197] op_sel:[0,1] op_sel_hi:[1,1]\n"
      "v_pk_mul_f32 v[90:91], v[90:91], v[196:197] op_sel:[0,1] op_sel_hi:[1,1]\n"
      "v_pk_mul_f32 v[92:93], v[92:93], v[196:197] op_sel:[0,1] op_sel_hi:[1,1]\n"
      "v_pk_mul_f32 v[94:95], v[94:95], v[196:197] op_sel:[0,1] op_sel_hi:[1,1]\n"
      "v_pk_mul_f32 v[96:97], v[96:97], v[196:197] op_sel:[0,1] op_sel_hi:[1,1]\n"
      "v_pk_mul_f32 v[98:99], v[98:99], v[196:197] op_sel:[0,1] op_sel_hi:[1,1]\n"
      "v_pk_mul_f32 v[100:101], v[100:101], v[196:197] op_sel:[0,1] op_sel_hi:[1,1]\n"
      "v_pk_mul_f32 v[102:103], v[102:103], v[196:197] op_sel:[0,1] op_sel_hi:[1,1]\n"
      "v_pk_mul_f32 v[104:105], v[104:105], v[196:197] op_sel:[0,1] op_sel_hi:[1,1]\n"
      "v_pk_mul_f32 v[106:107], v[106:107], v[196:197] op_sel:[0,1] op_sel_hi:[1,1]\n"
      "v_pk_mul_f32 v[108:109], v[108:109], v[196:197] op_sel:[0,1] op_sel_hi:[1,1]\n"
      "v_pk_mul_f32 v[110:111], v[110:111], v[196:197] op_sel:[0,1] op_sel_hi:[1,1]\n"
      "v_pk_mul_f32 v[112:113], v[112:113], v[196:197] op_sel:[0,1] op_sel_hi:[1,1]\n"
      "v_pk_mul_f32 v[114:115], v[114:115], v[196:197] op_sel:[0,1] op_sel_hi:[1,1]\n"
      "v_pk_mul_f32 v[116:117], v[116:117], v[196:197] op_sel:[0,1] op_sel_hi:[1,1]\n"
      "v_pk_mul_f32 v[118:119], v[118:119], v[196:197] op_sel:[0,1] op_sel_hi:[1,1]\n"
      "v_pk_mul_f32 v[120:121], v[120:121], v[196:197] op_sel:[0,1] op_sel_hi:[1,1]\n"
      "v_pk_mul_f32 v[122:123], v[122:123], v[196:197] op_sel:[0,1] op_sel_hi:[1,1]\n"
      "v_pk_mul_f32 v[124:125], v[124:125], v[196:197] op_sel:[0,1] op_sel_hi:[1,1]\n"
      "v_pk_mul_f32 v[126:127], v[126:127], v[196:197] op_sel:[0,1] op_sel_hi:[1,1]\n"
      "s_mov_b32 s50, 0\n"
      "s_nop 1\n"
      "s_branch LcA_RRB3_%=\n"
      "LcA_RA4_%=:\n"
      "v_mov_b32 v196, v194\n"
      "s_nop 1\n"
      "v_permlane32_swap_b32 v196, v194\n"
      "s_nop 1\n"
      "v_max_f32 v194, v194, v196\n"
      "v_max_f32 v196, 0, v194\n"
      "v_sub_f32 v128, v128, v196\n"
      "v_sub_f32 v129, v129, v196\n"
      "v_sub_f32 v130, v130, v196\n"
      "v_sub_f32 v131, v131, v196\n"
      "v_sub_f32 v132, v132, v196\n"
      "v_sub_f32 v133, v133, v196\n"
      "v_sub_f32 v134, v134, v196\n"
      "v_sub_f32 v135, v135, v196\n"
      "v_sub_f32 v136, v136, v196\n"
      "v_sub_f32 v137, v137, v196\n"
      "v_sub_f32 v138, v138, v196\n"
      "v_sub_f32 v139, v139, v196\n"
      "v_sub_f32 v140, v140, v196\n"
      "v_sub_f32 v141, v141, v196\n"
      "v_sub_f32 v142, v142, v196\n"
      "v_sub_f32 v143, v143, v196\n"
      "v_sub_f32 v160, v160, v196\n"
      "v_sub_f32 v161, v161, v196\n"
      "v_sub_f32 v162, v162, v196\n"
      "v_sub_f32 v163, v163, v196\n"
      "v_sub_f32 v164, v164, v196\n"
      "v_sub_f32 v165, v165, v196\n"
      "v_sub_f32 v166, v166, v196\n"
      "v_sub_f32 v167, v167, v196\n"
      "v_sub_f32 v168, v168, v196\n"
      "v_sub_f32 v169, v169, v196\n"
      "v_sub_f32 v170, v170, v196\n"
      "v_sub_f32 v171, v171, v196\n"
      "v_sub_f32 v172, v172, v196\n"
      "v_sub_f32 v173, v173, v196\n"
      "v_sub_f32 v174, v174, v196\n"
      "v_sub_f32 v175, v175, v196\n"
      "v_sub_f32 v196, 0, v196\n"
      "v_exp_f32 v197, v196\n"
      "s_nop 1\n"
      "v_mul_f32 v192, v192, v197\n"
      "v_mul_f32 v193, v193, v197\n"
      "s_mov_b32 s50, 1\n"
      "s_nop 1\n"
      "s_branch LcA_RRA4_%=\n"
      "LcA_RB4_%=:\n"
      "s_nop 15\n"
      "v_pk_mul_f32 v[0:1], v[0:1], v[196:197] op_sel:[0,1] op_sel_hi:[1,1]\n"
      "v_pk_mul_f32 v[2:3], v[2:3], v[196:197] op_sel:[0,1] op_sel_hi:[1,1]\n"
      "v_pk_mul_f32 v[4:5], v[4:5], v[196:197] op_sel:[0,1] op_sel_hi:[1,1]\n"
      "v_pk_mul_f32 v[6:7], v[6:7], v[196:197] op_sel:[0,1] op_sel_hi:[1,1]\n"
      "v_pk_mul_f32 v[8:9], v[8:9], v[196:197] op_sel:[0,1] op_sel_hi:[1,1]\n"
      "v_pk_mul_f32 v[10:11], v[10:11], v[196:197] op_sel:[0,1] op_sel_hi:[1,1]\n"
      "v_pk_mul_f32 v[12:13], v[12:13], v[196:197] op_sel:[0,1] op_sel_hi:[1,1]\n"
      "v_pk_mul_f32 v[14:15], v[14:15], v[196:197] op_sel:[0,1] op_sel_hi:[1,1]\n"
      "v_pk_mul_f32 v[16:17], v[16:17], v[196:197] op_sel:[0,1] op_sel_hi:[1,1]\n"
      "v_pk_mul_f32 v[18:19], v[18:19], v[196:197] op_sel:[0,1] op_sel_hi:[1,1]\n"
      "v_pk_mul_f32 v[20:21], v[20:21], v[196:197] op_sel:[0,1] op_sel_hi:[1,1]\n"
      "v_pk_mul_f32 v[22:23], v[22:23], v[196:197] op_sel:[0,1] op_sel_hi:[1,1]\n"
      "v_pk_mul_f32 v[24:25], v[24:25], v[196:197] op_sel:[0,1] op_sel_hi:[1,1]\n"
      "v_pk_mul_f32 v[26:27], v[26:27], v[196:197] op_sel:[0,1] op_sel_hi:[1,1]\n"
      "v_pk_mul_f32 v[28:29], v[28:29], v[196:197] op_sel:[0,1] op_sel_hi:[1,1]\n"
      "v_pk_mul_f32 v[30:31], v[30:31], v[196:197] op_sel:[0,1] op_sel_hi:[1,1]\n"
      "v_pk_mul_f32 v[32:33], v[32:33], v[196:197] op_sel:[0,1] op_sel_hi:[1,1]\n"
      "v_pk_mul_f32 v[34:35], v[34:35], v[196:197] op_sel:[0,1] op_sel_hi:[1,1]\n"
      "v_pk_mul_f32 v[36:37], v[36:37], v[196:197] op_sel:[0,1] op_sel_hi:[1,1]\n"
      "v_pk_mul_f32 v[38:39], v[38:39], v[196:197] op_sel:[0,1] op_sel_hi:[1,1]\n"
      "v_pk_mul_f32 v[40:41], v[40:41], v[196:197] op_sel:[0,1] op_sel_hi:[1,1]\n"
      "v_pk_mul_f32 v[42:43], v[42:43], v[196:197] op_sel:[0,1] op_sel_hi:[1,1]\n"
      "v_pk_mul_f32 v[44:45], v[44:45], v[196:197] op_sel:[0,1] op_sel_hi:[1,1]\n"
      "v_pk_mul_f32 v[46:47], v[46:47], v[196:197] op_sel:[0,1] op_sel_hi:[1,1]\n"
      "v_pk_mul_f32 v[48:49], v[48:49], v[196:197] op_sel:[0,1] op_sel_hi:[1,1]\n"
      "v_pk_mul_f32 v[50:51], v[50:51], v[196:197] op_sel:[0,1] op_sel_hi:[1,1]\n"
      "v_pk_mul_f32 v[52:53], v[52:53], v[196:197] op_sel:[0,1] op_sel_hi:[1,1]\n"
      "v_pk_mul_f32 v[54:55], v[54:55], v[196:197] op_sel:[0,1] op_sel_hi:[1,1]\n"
      "v_pk_mul_f32 v[56:57], v[56:57], v[196:197] op_sel:[0,1] op_sel_hi:[1,1]\n"
      "v_pk_mul_f32 v[58:59], v[58:59], v[196:197] op_sel:[0,1] op_sel_hi:[1,1]\n"
      "v_pk_mul_f32 v[60:61], v[60:61], v[196:197] op_sel:[0,1] op_sel_hi:[1,1]\n"
      "v_pk_mul_f32 v[62:63], v[62:63], v[196:197] op_sel:[0,1] op_sel_hi:[1,1]\n"
      "v_pk_mul_f32 v[64:65], v[64:65], v[196:197] op_sel:[0,1] op_sel_hi:[1,1]\n"
      "v_pk_mul_f32 v[66:67], v[66:67], v[196:197] op_sel:[0,1] op_sel_hi:[1,1]\n"
      "v_pk_mul_f32 v[68:69], v[68:69], v[196:197] op_sel:[0,1] op_sel_hi:[1,1]\n"
      "v_pk_mul_f32 v[70:71], v[70:71], v[196:197] op_sel:[0,1] op_sel_hi:[1,1]\n"
      "v_pk_mul_f32 v[72:73], v[72:73], v[196:197] op_sel:[0,1] op_sel_hi:[1,1]\n"
      "v_pk_mul_f32 v[74:75], v[74:75], v[196:197] op_sel:[0,1] op_sel_hi:[1,1]\n"
      "v_pk_mul_f32 v[76:77], v[76:77], v[196:197] op_sel:[0,1] op_sel_hi:[1,1]\n"
      "v_pk_mul_f32 v[78:79], v[78:79], v[196:197] op_sel:[0,1] op_sel_hi:[1,1]\n"
      "v_pk_mul_f32 v[80:81], v[80:81], v[196:197] op_sel:[0,1] op_sel_hi:[1,1]\n"
      "v_pk_mul_f32 v[82:83], v[82:83], v[196:197] op_sel:[0,1] op_sel_hi:[1,1]\n"
      "v_pk_mul_f32 v[84:85], v[84:85], v[196:197] op_sel:[0,1] op_sel_hi:[1,1]\n"
      "v_pk_mul_f32 v[86:87], v[86:87], v[196:197] op_sel:[0,1] op_sel_hi:[1,1]\n"
      "v_pk_mul_f32 v[88:89], v[88:89], v[196:197] op_sel:[0,1] op_sel_hi:[1,1]\n"
      "v_pk_mul_f32 v[90:91], v[90:91], v[196:197] op_sel:[0,1] op_sel_hi:[1,1]\n"
      "v_pk_mul_f32 v[92:93], v[92:93], v[196:197] op_sel:[0,1] op_sel_hi:[1,1]\n"
      "v_pk_mul_f32 v[94:95], v[94:95], v[196:197] op_sel:[0,1] op_sel_hi:[1,1]\n"
      "v_pk_mul_f32 v[96:97], v[96:97], v[196:197] op_sel:[0,1] op_sel_hi:[1,1]\n"
      "v_pk_mul_f32 v[98:99], v[98:99], v[196:197] op_sel:[0,1] op_sel_hi:[1,1]\n"
      "v_pk_mul_f32 v[100:101], v[100:101], v[196:197] op_sel:[0,1] op_sel_hi:[1,1]\n"
      "v_pk_mul_f32 v[102:103], v[102:103], v[196:197] op_sel:[0,1] op_sel_hi:[1,1]\n"
      "v_pk_mul_f32 v[104:105], v[104:105], v[196:197] op_sel:[0,1] op_sel_hi:[1,1]\n"
      "v_pk_mul_f32 v[106:107], v[106:107], v[196:197] op_sel:[0,1] op_sel_hi:[1,1]\n"
      "v_pk_mul_f32 v[108:109], v[108:109], v[196:197] op_sel:[0,1] op_sel_hi:[1,1]\n"
      "v_pk_mul_f32 v[110:111], v[110:111], v[196:197] op_sel:[0,1] op_sel_hi:[1,1]\n"
      "v_pk_mul_f32 v[112:113], v[112:113], v[196:197] op_sel:[0,1] op_sel_hi:[1,1]\n"
      "v_pk_mul_f32 v[114:115], v[114:115], v[196:197] op_sel:[0,1] op_sel_hi:[1,1]\n"
      "v_pk_mul_f32 v[116:117], v[116:117], v[196:197] op_sel:[0,1] op_sel_hi:[1,1]\n"
      "v_pk_mul_f32 v[118:119], v[118:119], v[196:197] op_sel:[0,1] op_sel_hi:[1,1]\n"
      "v_pk_mul_f32 v[120:121], v[120:121], v[196:197] op_sel:[0,1] op_sel_hi:[1,1]\n"
      "v_pk_mul_f32 v[122:123], v[122:123], v[196:197] op_sel:[0,1] op_sel_hi:[1,1]\n"
      "v_pk_mul_f32 v[124:125], v[124:125], v[196:197] op_sel:[0,1] op_sel_hi:[1,1]\n"
      "v_pk_mul_f32 v[126:127], v[126:127], v[196:197] op_sel:[0,1] op_sel_hi:[1,1]\n"
      "s_mov_b32 s50, 0\n"
      "s_nop 1\n"
      "s_branch LcA_RRB4_%=\n"
      "LcA_RA5_%=:\n"
      "v_mov_b32 v196, v194\n"
      "s_nop 1\n"
      "v_permlane32_swap_b32 v196, v194\n"
      "s_nop 1\n"
      "v_max_f32 v194, v194, v196\n"
      "v_max_f32 v196, 0, v194\n"
      "v_sub_f32 v144, v144, v196\n"
      "v_sub_f32 v145, v145, v196\n"
      "v_sub_f32 v146, v146, v196\n"
      "v_sub_f32 v147, v147, v196\n"
      "v_sub_f32 v148, v148, v196\n"
      "v_sub_f32 v149, v149, v196\n"
      "v_sub_f32 v150, v150, v196\n"
      "v_sub_f32 v151, v151, v196\n"
      "v_sub_f32 v152, v152, v196\n"
      "v_sub_f32 v153, v153, v196\n"
      "v_sub_f32 v154, v154, v196\n"
      "v_sub_f32 v155, v155, v196\n"
      "v_sub_f32 v156, v156, v196\n"
      "v_sub_f32 v157, v157, v196\n"
      "v_sub_f32 v158, v158, v196\n"
      "v_sub_f32 v159, v159, v196\n"
      "v_sub_f32 v160, v160, v196\n"
      "v_sub_f32 v161, v161, v196\n"
      "v_sub_f32 v162, v162, v196\n"
      "v_sub_f32 v163, v163, v196\n"
      "v_sub_f32 v164, v164, v196\n"
      "v_sub_f32 v165, v165, v196\n"
      "v_sub_f32 v166, v166, v196\n"
      "v_sub_f32 v167, v167, v196\n"
      "v_sub_f32 v168, v168, v196\n"
      "v_sub_f32 v169, v169, v196\n"
      "v_sub_f32 v170, v170, v196\n"
      "v_sub_f32 v171, v171, v196\n"
      "v_sub_f32 v172, v172, v196\n"
      "v_sub_f32 v173, v173, v196\n"
      "v_sub_f32 v174, v174, v196\n"
      "v_sub_f32 v175, v175, v196\n"
      "v_sub_f32 v196, 0, v196\n"
      "v_exp_f32 v197, v196\n"
      "s_nop 1\n"
      "v_mul_f32 v192, v192, v197\n"
      "v_mul_f32 v193, v193, v197\n"
      "s_mov_b32 s50, 1\n"
      "s_nop 1\n"
      "s_branch LcA_RRA5_%=\n"
      "LcA_RB5_%=:\n"
      "s_nop 15\n"
      "v_pk_mul_f32 v[0:1], v[0:1], v[196:197] op_sel:[0,1] op_sel_hi:[1,1]\n"
      "v_pk_mul_f32 v[2:3], v[2:3], v[196:197] op_sel:[0,1] op_sel_hi:[1,1]\n"
      "v_pk_mul_f32 v[4:5], v[4:5], v[196:197] op_sel:[0,1] op_sel_hi:[1,1]\n"
      "v_pk_mul_f32 v[6:7], v[6:7], v[196:197] op_sel:[0,1] op_sel_hi:[1,1]\n"
      "v_pk_mul_f32 v[8:9], v[8:9], v[196:197] op_sel:[0,1] op_sel_hi:[1,1]\n"
      "v_pk_mul_f32 v[10:11], v[10:11], v[196:197] op_sel:[0,1] op_sel_hi:[1,1]\n"
      "v_pk_mul_f32 v[12:13], v[12:13], v[196:197] op_sel:[0,1] op_sel_hi:[1,1]\n"
      "v_pk_mul_f32 v[14:15], v[14:15], v[196:197] op_sel:[0,1] op_sel_hi:[1,1]\n"
      "v_pk_mul_f32 v[16:17], v[16:17], v[196:197] op_sel:[0,1] op_sel_hi:[1,1]\n"
      "v_pk_mul_f32 v[18:19], v[18:19], v[196:197] op_sel:[0,1] op_sel_hi:[1,1]\n"
      "v_pk_mul_f32 v[20:21], v[20:21], v[196:197] op_sel:[0,1] op_sel_hi:[1,1]\n"
      "v_pk_mul_f32 v[22:23], v[22:23], v[196:197] op_sel:[0,1] op_sel_hi:[1,1]\n"
      "v_pk_mul_f32 v[24:25], v[24:25], v[196:197] op_sel:[0,1] op_sel_hi:[1,1]\n"
      "v_pk_mul_f32 v[26:27], v[26:27], v[196:197] op_sel:[0,1] op_sel_hi:[1,1]\n"
      "v_pk_mul_f32 v[28:29], v[28:29], v[196:197] op_sel:[0,1] op_sel_hi:[1,1]\n"
      "v_pk_mul_f32 v[30:31], v[30:31], v[196:197] op_sel:[0,1] op_sel_hi:[1,1]\n"
      "v_pk_mul_f32 v[32:33], v[32:33], v[196:197] op_sel:[0,1] op_sel_hi:[1,1]\n"
      "v_pk_mul_f32 v[34:35], v[34:35], v[196:197] op_sel:[0,1] op_sel_hi:[1,1]\n"
      "v_pk_mul_f32 v[36:37], v[36:37], v[196:197] op_sel:[0,1] op_sel_hi:[1,1]\n"
      "v_pk_mul_f32 v[38:39], v[38:39], v[196:197] op_sel:[0,1] op_sel_hi:[1,1]\n"
      "v_pk_mul_f32 v[40:41], v[40:41], v[196:197] op_sel:[0,1] op_sel_hi:[1,1]\n"
      "v_pk_mul_f32 v[42:43], v[42:43], v[196:197] op_sel:[0,1] op_sel_hi:[1,1]\n"
      "v_pk_mul_f32 v[44:45], v[44:45], v[196:197] op_sel:[0,1] op_sel_hi:[1,1]\n"
      "v_pk_mul_f32 v[46:47], v[46:47], v[196:197] op_sel:[0,1] op_sel_hi:[1,1]\n"
      "v_pk_mul_f32 v[48:49], v[48:49], v[196:197] op_sel:[0,1] op_sel_hi:[1,1]\n"
      "v_pk_mul_f32 v[50:51], v[50:51], v[196:197] op_sel:[0,1] op_sel_hi:[1,1]\n"
      "v_pk_mul_f32 v[52:53], v[52:53], v[196:197] op_sel:[0,1] op_sel_hi:[1,1]\n"
      "v_pk_mul_f32 v[54:55], v[54:55], v[196:197] op_sel:[0,1] op_sel_hi:[1,1]\n"
      "v_pk_mul_f32 v[56:57], v[56:57], v[196:197] op_sel:[0,1] op_sel_hi:[1,1]\n"
      "v_pk_mul_f32 v[58:59], v[58:59], v[196:197] op_sel:[0,1] op_sel_hi:[1,1]\n"
      "v_pk_mul_f32 v[60:61], v[60:61], v[196:197] op_sel:[0,1] op_sel_hi:[1,1]\n"
      "v_pk_mul_f32 v[62:63], v[62:63], v[196:197] op_sel:[0,1] op_sel_hi:[1,1]\n"
      "v_pk_mul_f32 v[64:65], v[64:65], v[196:197] op_sel:[0,1] op_sel_hi:[1,1]\n"
      "v_pk_mul_f32 v[66:67], v[66:67], v[196:197] op_sel:[0,1] op_sel_hi:[1,1]\n"
      "v_pk_mul_f32 v[68:69], v[68:69], v[196:197] op_sel:[0,1] op_sel_hi:[1,1]\n"
      "v_pk_mul_f32 v[70:71], v[70:71], v[196:197] op_sel:[0,1] op_sel_hi:[1,1]\n"
      "v_pk_mul_f32 v[72:73], v[72:73], v[196:197] op_sel:[0,1] op_sel_hi:[1,1]\n"
      "v_pk_mul_f32 v[74:75], v[74:75], v[196:197] op_sel:[0,1] op_sel_hi:[1,1]\n"
      "v_pk_mul_f32 v[76:77], v[76:77], v[196:197] op_sel:[0,1] op_sel_hi:[1,1]\n"
      "v_pk_mul_f32 v[78:79], v[78:79], v[196:197] op_sel:[0,1] op_sel_hi:[1,1]\n"
      "v_pk_mul_f32 v[80:81], v[80:81], v[196:197] op_sel:[0,1] op_sel_hi:[1,1]\n"
      "v_pk_mul_f32 v[82:83], v[82:83], v[196:197] op_sel:[0,1] op_sel_hi:[1,1]\n"
      "v_pk_mul_f32 v[84:85], v[84:85], v[196:197] op_sel:[0,1] op_sel_hi:[1,1]\n"
      "v_pk_mul_f32 v[86:87], v[86:87], v[196:197] op_sel:[0,1] op_sel_hi:[1,1]\n"
      "v_pk_mul_f32 v[88:89], v[88:89], v[196:197] op_sel:[0,1] op_sel_hi:[1,1]\n"
      "v_pk_mul_f32 v[90:91], v[90:91], v[196:197] op_sel:[0,1] op_sel_hi:[1,1]\n"
      "v_pk_mul_f32 v[92:93], v[92:93], v[196:197] op_sel:[0,1] op_sel_hi:[1,1]\n"
      "v_pk_mul_f32 v[94:95], v[94:95], v[196:197] op_sel:[0,1] op_sel_hi:[1,1]\n"
      "v_pk_mul_f32 v[96:97], v[96:97], v[196:197] op_sel:[0,1] op_sel_hi:[1,1]\n"
      "v_pk_mul_f32 v[98:99], v[98:99], v[196:197] op_sel:[0,1] op_sel_hi:[1,1]\n"
      "v_pk_mul_f32 v[100:101], v[100:101], v[196:197] op_sel:[0,1] op_sel_hi:[1,1]\n"
      "v_pk_mul_f32 v[102:103], v[102:103], v[196:197] op_sel:[0,1] op_sel_hi:[1,1]\n"
      "v_pk_mul_f32 v[104:105], v[104:105], v[196:197] op_sel:[0,1] op_sel_hi:[1,1]\n"
      "v_pk_mul_f32 v[106:107], v[106:107], v[196:197] op_sel:[0,1] op_sel_hi:[1,1]\n"
      "v_pk_mul_f32 v[108:109], v[108:109], v[196:197] op_sel:[0,1] op_sel_hi:[1,1]\n"
      "v_pk_mul_f32 v[110:111], v[110:111], v[196:197] op_sel:[0,1] op_sel_hi:[1,1]\n"
      "v_pk_mul_f32 v[112:113], v[112:113], v[196:197] op_sel:[0,1] op_sel_hi:[1,1]\n"
      "v_pk_mul_f32 v[114:115], v[114:115], v[196:197] op_sel:[0,1] op_sel_hi:[1,1]\n"
      "v_pk_mul_f32 v[116:117], v[116:117], v[196:197] op_sel:[0,1] op_sel_hi:[1,1]\n"
      "v_pk_mul_f32 v[118:119], v[118:119], v[196:197] op_sel:[0,1] op_sel_hi:[1,1]\n"
      "v_pk_mul_f32 v[120:121], v[120:121], v[196:197] op_sel:[0,1] op_sel_hi:[1,1]\n"
      "v_pk_mul_f32 v[122:123], v[122:123], v[196:197] op_sel:[0,1] op_sel_hi:[1,1]\n"
      "v_pk_mul_f32 v[124:125], v[124:125], v[196:197] op_sel:[0,1] op_sel_hi:[1,1]\n"
      "v_pk_mul_f32 v[126:127], v[126:127], v[196:197] op_sel:[0,1] op_sel_hi:[1,1]\n"
      "s_mov_b32 s50, 0\n"
      "s_nop 1\n"
      "s_branch LcA_RRB5_%=\n"
      "LcA_RA6_%=:\n"
      "v_mov_b32 v196, v194\n"
      "s_nop 1\n"
      "v_permlane32_swap_b32 v196, v194\n"
      "s_nop 1\n"
      "v_max_f32 v194, v194, v196\n"
      "v_max_f32 v196, 0, v194\n"
      "v_sub_f32 v128, v128, v196\n"
      "v_sub_f32 v129, v129, v196\n"
      "v_sub_f32 v130, v130, v196\n"
      "v_sub_f32 v131, v131, v196\n"
      "v_sub_f32 v132, v132, v196\n"
      "v_sub_f32 v133, v133, v196\n"
      "v_sub_f32 v134, v134, v196\n"
      "v_sub_f32 v135, v135, v196\n"
      "v_sub_f32 v136, v136, v196\n"
      "v_sub_f32 v137, v137, v196\n"
      "v_sub_f32 v138, v138, v196\n"
      "v_sub_f32 v139, v139, v196\n"
      "v_sub_f32 v140, v140, v196\n"
      "v_sub_f32 v141, v141, v196\n"
      "v_sub_f32 v142, v142, v196\n"
      "v_sub_f32 v143, v143, v196\n"
      "v_sub_f32 v160, v160, v196\n"
      "v_sub_f32 v161, v161, v196\n"
      "v_sub_f32 v162, v162, v196\n"
      "v_sub_f32 v163, v163, v196\n"
      "v_sub_f32 v164, v164, v196\n"
      "v_sub_f32 v165, v165, v196\n"
      "v_sub_f32 v166, v166, v196\n"
      "v_sub_f32 v167, v167, v196\n"
      "v_sub_f32 v168, v168, v196\n"
      "v_sub_f32 v169, v169, v196\n"
      "v_sub_f32 v170, v170, v196\n"
      "v_sub_f32 v171, v171, v196\n"
      "v_sub_f32 v172, v172, v196\n"
      "v_sub_f32 v173, v173, v196\n"
      "v_sub_f32 v174, v174, v196\n"
      "v_sub_f32 v175, v175, v196\n"
      "v_sub_f32 v196, 0, v196\n"
      "v_exp_f32 v197, v196\n"
      "s_nop 1\n"
      "v_mul_f32 v192, v192, v197\n"
      "v_mul_f32 v193, v193, v197\n"
      "s_mov_b32 s50, 1\n"
      "s_nop 1\n"
      "s_branch LcA_RRA6_%=\n"
      "LcA_RB6_%=:\n"
      "s_nop 15\n"
      "v_pk_mul_f32 v[0:1], v[0:1], v[196:197] op_sel:[0,1] op_sel_hi:[1,1]\n"
      "v_pk_mul_f32 v[2:3], v[2:3], v[196:197] op_sel:[0,1] op_sel_hi:[1,1]\n"
      "v_pk_mul_f32 v[4:5], v[4:5], v[196:197] op_sel:[0,1] op_sel_hi:[1,1]\n"
      "v_pk_mul_f32 v[6:7], v[6:7], v[196:197] op_sel:[0,1] op_sel_hi:[1,1]\n"
      "v_pk_mul_f32 v[8:9], v[8:9], v[196:197] op_sel:[0,1] op_sel_hi:[1,1]\n"
      "v_pk_mul_f32 v[10:11], v[10:11], v[196:197] op_sel:[0,1] op_sel_hi:[1,1]\n"
      "v_pk_mul_f32 v[12:13], v[12:13], v[196:197] op_sel:[0,1] op_sel_hi:[1,1]\n"
      "v_pk_mul_f32 v[14:15], v[14:15], v[196:197] op_sel:[0,1] op_sel_hi:[1,1]\n"
      "v_pk_mul_f32 v[16:17], v[16:17], v[196:197] op_sel:[0,1] op_sel_hi:[1,1]\n"
      "v_pk_mul_f32 v[18:19], v[18:19], v[196:197] op_sel:[0,1] op_sel_hi:[1,1]\n"
      "v_pk_mul_f32 v[20:21], v[20:21], v[196:197] op_sel:[0,1] op_sel_hi:[1,1]\n"
      "v_pk_mul_f32 v[22:23], v[22:23], v[196:197] op_sel:[0,1] op_sel_hi:[1,1]\n"
      "v_pk_mul_f32 v[24:25], v[24:25], v[196:197] op_sel:[0,1] op_sel_hi:[1,1]\n"
      "v_pk_mul_f32 v[26:27], v[26:27], v[196:197] op_sel:[0,1] op_sel_hi:[1,1]\n"
      "v_pk_mul_f32 v[28:29], v[28:29], v[196:197] op_sel:[0,1] op_sel_hi:[1,1]\n"
      "v_pk_mul_f32 v[30:31], v[30:31], v[196:197] op_sel:[0,1] op_sel_hi:[1,1]\n"
      "v_pk_mul_f32 v[32:33], v[32:33], v[196:197] op_sel:[0,1] op_sel_hi:[1,1]\n"
      "v_pk_mul_f32 v[34:35], v[34:35], v[196:197] op_sel:[0,1] op_sel_hi:[1,1]\n"
      "v_pk_mul_f32 v[36:37], v[36:37], v[196:197] op_sel:[0,1] op_sel_hi:[1,1]\n"
      "v_pk_mul_f32 v[38:39], v[38:39], v[196:197] op_sel:[0,1] op_sel_hi:[1,1]\n"
      "v_pk_mul_f32 v[40:41], v[40:41], v[196:197] op_sel:[0,1] op_sel_hi:[1,1]\n"
      "v_pk_mul_f32 v[42:43], v[42:43], v[196:197] op_sel:[0,1] op_sel_hi:[1,1]\n"
      "v_pk_mul_f32 v[44:45], v[44:45], v[196:197] op_sel:[0,1] op_sel_hi:[1,1]\n"
      "v_pk_mul_f32 v[46:47], v[46:47], v[196:197] op_sel:[0,1] op_sel_hi:[1,1]\n"
      "v_pk_mul_f32 v[48:49], v[48:49], v[196:197] op_sel:[0,1] op_sel_hi:[1,1]\n"
      "v_pk_mul_f32 v[50:51], v[50:51], v[196:197] op_sel:[0,1] op_sel_hi:[1,1]\n"
      "v_pk_mul_f32 v[52:53], v[52:53], v[196:197] op_sel:[0,1] op_sel_hi:[1,1]\n"
      "v_pk_mul_f32 v[54:55], v[54:55], v[196:197] op_sel:[0,1] op_sel_hi:[1,1]\n"
      "v_pk_mul_f32 v[56:57], v[56:57], v[196:197] op_sel:[0,1] op_sel_hi:[1,1]\n"
      "v_pk_mul_f32 v[58:59], v[58:59], v[196:197] op_sel:[0,1] op_sel_hi:[1,1]\n"
      "v_pk_mul_f32 v[60:61], v[60:61], v[196:197] op_sel:[0,1] op_sel_hi:[1,1]\n"
      "v_pk_mul_f32 v[62:63], v[62:63], v[196:197] op_sel:[0,1] op_sel_hi:[1,1]\n"
      "v_pk_mul_f32 v[64:65], v[64:65], v[196:197] op_sel:[0,1] op_sel_hi:[1,1]\n"
      "v_pk_mul_f32 v[66:67], v[66:67], v[196:197] op_sel:[0,1] op_sel_hi:[1,1]\n"
      "v_pk_mul_f32 v[68:69], v[68:69], v[196:197] op_sel:[0,1] op_sel_hi:[1,1]\n"
      "v_pk_mul_f32 v[70:71], v[70:71], v[196:197] op_sel:[0,1] op_sel_hi:[1,1]\n"
      "v_pk_mul_f32 v[72:73], v[72:73], v[196:197] op_sel:[0,1] op_sel_hi:[1,1]\n"
      "v_pk_mul_f32 v[74:75], v[74:75], v[196:197] op_sel:[0,1] op_sel_hi:[1,1]\n"
      "v_pk_mul_f32 v[76:77], v[76:77], v[196:197] op_sel:[0,1] op_sel_hi:[1,1]\n"
      "v_pk_mul_f32 v[78:79], v[78:79], v[196:197] op_sel:[0,1] op_sel_hi:[1,1]\n"
      "v_pk_mul_f32 v[80:81], v[80:81], v[196:197] op_sel:[0,1] op_sel_hi:[1,1]\n"
      "v_pk_mul_f32 v[82:83], v[82:83], v[196:197] op_sel:[0,1] op_sel_hi:[1,1]\n"
      "v_pk_mul_f32 v[84:85], v[84:85], v[196:197] op_sel:[0,1] op_sel_hi:[1,1]\n"
      "v_pk_mul_f32 v[86:87], v[86:87], v[196:197] op_sel:[0,1] op_sel_hi:[1,1]\n"
      "v_pk_mul_f32 v[88:89], v[88:89], v[196:197] op_sel:[0,1] op_sel_hi:[1,1]\n"
      "v_pk_mul_f32 v[90:91], v[90:91], v[196:197] op_sel:[0,1] op_sel_hi:[1,1]\n"
      "v_pk_mul_f32 v[92:93], v[92:93], v[196:197] op_sel:[0,1] op_sel_hi:[1,1]\n"
      "v_pk_mul_f32 v[94:95], v[94:95], v[196:197] op_sel:[0,1] op_sel_hi:[1,1]\n"
      "v_pk_mul_f32 v[96:97], v[96:97], v[196:197] op_sel:[0,1] op_sel_hi:[1,1]\n"
      "v_pk_mul_f32 v[98:99], v[98:99], v[196:197] op_sel:[0,1] op_sel_hi:[1,1]\n"
      "v_pk_mul_f32 v[100:101], v[100:101], v[196:197] op_sel:[0,1] op_sel_hi:[1,1]\n"
      "v_pk_mul_f32 v[102:103], v[102:103], v[196:197] op_sel:[0,1] op_sel_hi:[1,1]\n"
      "v_pk_mul_f32 v[104:105], v[104:105], v[196:197] op_sel:[0,1] op_sel_hi:[1,1]\n"
      "v_pk_mul_f32 v[106:107], v[106:107], v[196:197] op_sel:[0,1] op_sel_hi:[1,1]\n"
      "v_pk_mul_f32 v[108:109], v[108:109], v[196:197] op_sel:[0,1] op_sel_hi:[1,1]\n"
      "v_pk_mul_f32 v[110:111], v[110:111], v[196:197] op_sel:[0,1] op_sel_hi:[1,1]\n"
      "v_pk_mul_f32 v[112:113], v[112:113], v[196:197] op_sel:[0,1] op_sel_hi:[1,1]\n"
      "v_pk_mul_f32 v[114:115], v[114:115], v[196:197] op_sel:[0,1] op_sel_hi:[1,1]\n"
      "v_pk_mul_f32 v[116:117], v[116:117], v[196:197] op_sel:[0,1] op_sel_hi:[1,1]\n"
      "v_pk_mul_f32 v[118:119], v[118:119], v[196:197] op_sel:[0,1] op_sel_hi:[1,1]\n"
      "v_pk_mul_f32 v[120:121], v[120:121], v[196:197] op_sel:[0,1] op_sel_hi:[1,1]\n"
      "v_pk_mul_f32 v[122:123], v[122:123], v[196:197] op_sel:[0,1] op_sel_hi:[1,1]\n"
      "v_pk_mul_f32 v[124:125], v[124:125], v[196:197] op_sel:[0,1] op_sel_hi:[1,1]\n"
      "v_pk_mul_f32 v[126:127], v[126:127], v[196:197] op_sel:[0,1] op_sel_hi:[1,1]\n"
      "s_mov_b32 s50, 0\n"
      "s_nop 1\n"
      "s_branch LcA_RRB6_%=\n"
      "LcA_RA7_%=:\n"
      "v_mov_b32 v196, v194\n"
      "s_nop 1\n"
      "v_permlane32_swap_b32 v196, v194\n"
      "s_nop 1\n"
      "v_max_f32 v194, v194, v196\n"
      "v_max_f32 v196, 0, v194\n"
      "v_sub_f32 v144, v144, v196\n"
      "v_sub_f32 v145, v145, v196\n"
      "v_sub_f32 v146, v146, v196\n"
      "v_sub_f32 v147, v147, v196\n"
      "v_sub_f32 v148, v148, v196\n"
      "v_sub_f32 v149, v149, v196\n"
      "v_sub_f32 v150, v150, v196\n"
      "v_sub_f32 v151, v151, v196\n"
      "v_sub_f32 v152, v152, v196\n"
      "v_sub_f32 v153, v153, v196\n"
      "v_sub_f32 v154, v154, v196\n"
      "v_sub_f32 v155, v155, v196\n"
      "v_sub_f32 v156, v156, v196\n"
      "v_sub_f32 v157, v157, v196\n"
      "v_sub_f32 v158, v158, v196\n"
      "v_sub_f32 v159, v159, v196\n"
      "v_sub_f32 v160, v160, v196\n"
      "v_sub_f32 v161, v161, v196\n"
      "v_sub_f32 v162, v162, v196\n"
      "v_sub_f32 v163, v163, v196\n"
      "v_sub_f32 v164, v164, v196\n"
      "v_sub_f32 v165, v165, v196\n"
      "v_sub_f32 v166, v166, v196\n"
      "v_sub_f32 v167, v167, v196\n"
      "v_sub_f32 v168, v168, v196\n"
      "v_sub_f32 v169, v169, v196\n"
      "v_sub_f32 v170, v170, v196\n"
      "v_sub_f32 v171, v171, v196\n"
      "v_sub_f32 v172, v172, v196\n"
      "v_sub_f32 v173, v173, v196\n"
      "v_sub_f32 v174, v174, v196\n"
      "v_sub_f32 v175, v175, v196\n"
      "v_sub_f32 v196, 0, v196\n"
      "v_exp_f32 v197, v196\n"
      "s_nop 1\n"
      "v_mul_f32 v192, v192, v197\n"
      "v_mul_f32 v193, v193, v197\n"
      "s_mov_b32 s50, 1\n"
      "s_nop 1\n"
      "s_branch LcA_RRA7_%=\n"
      "LcA_RB7_%=:\n"
      "s_nop 15\n"
      "v_pk_mul_f32 v[0:1], v[0:1], v[196:197] op_sel:[0,1] op_sel_hi:[1,1]\n"
      "v_pk_mul_f32 v[2:3], v[2:3], v[196:197] op_sel:[0,1] op_sel_hi:[1,1]\n"
      "v_pk_mul_f32 v[4:5], v[4:5], v[196:197] op_sel:[0,1] op_sel_hi:[1,1]\n"
      "v_pk_mul_f32 v[6:7], v[6:7], v[196:197] op_sel:[0,1] op_sel_hi:[1,1]\n"
      "v_pk_mul_f32 v[8:9], v[8:9], v[196:197] op_sel:[0,1] op_sel_hi:[1,1]\n"
      "v_pk_mul_f32 v[10:11], v[10:11], v[196:197] op_sel:[0,1] op_sel_hi:[1,1]\n"
      "v_pk_mul_f32 v[12:13], v[12:13], v[196:197] op_sel:[0,1] op_sel_hi:[1,1]\n"
      "v_pk_mul_f32 v[14:15], v[14:15], v[196:197] op_sel:[0,1] op_sel_hi:[1,1]\n"
      "v_pk_mul_f32 v[16:17], v[16:17], v[196:197] op_sel:[0,1] op_sel_hi:[1,1]\n"
      "v_pk_mul_f32 v[18:19], v[18:19], v[196:197] op_sel:[0,1] op_sel_hi:[1,1]\n"
      "v_pk_mul_f32 v[20:21], v[20:21], v[196:197] op_sel:[0,1] op_sel_hi:[1,1]\n"
      "v_pk_mul_f32 v[22:23], v[22:23], v[196:197] op_sel:[0,1] op_sel_hi:[1,1]\n"
      "v_pk_mul_f32 v[24:25], v[24:25], v[196:197] op_sel:[0,1] op_sel_hi:[1,1]\n"
      "v_pk_mul_f32 v[26:27], v[26:27], v[196:197] op_sel:[0,1] op_sel_hi:[1,1]\n"
      "v_pk_mul_f32 v[28:29], v[28:29], v[196:197] op_sel:[0,1] op_sel_hi:[1,1]\n"
      "v_pk_mul_f32 v[30:31], v[30:31], v[196:197] op_sel:[0,1] op_sel_hi:[1,1]\n"
      "v_pk_mul_f32 v[32:33], v[32:33], v[196:197] op_sel:[0,1] op_sel_hi:[1,1]\n"
      "v_pk_mul_f32 v[34:35], v[34:35], v[196:197] op_sel:[0,1] op_sel_hi:[1,1]\n"
      "v_pk_mul_f32 v[36:37], v[36:37], v[196:197] op_sel:[0,1] op_sel_hi:[1,1]\n"
      "v_pk_mul_f32 v[38:39], v[38:39], v[196:197] op_sel:[0,1] op_sel_hi:[1,1]\n"
      "v_pk_mul_f32 v[40:41], v[40:41], v[196:197] op_sel:[0,1] op_sel_hi:[1,1]\n"
      "v_pk_mul_f32 v[42:43], v[42:43], v[196:197] op_sel:[0,1] op_sel_hi:[1,1]\n"
      "v_pk_mul_f32 v[44:45], v[44:45], v[196:197] op_sel:[0,1] op_sel_hi:[1,1]\n"
      "v_pk_mul_f32 v[46:47], v[46:47], v[196:197] op_sel:[0,1] op_sel_hi:[1,1]\n"
      "v_pk_mul_f32 v[48:49], v[48:49], v[196:197] op_sel:[0,1] op_sel_hi:[1,1]\n"
      "v_pk_mul_f32 v[50:51], v[50:51], v[196:197] op_sel:[0,1] op_sel_hi:[1,1]\n"
      "v_pk_mul_f32 v[52:53], v[52:53], v[196:197] op_sel:[0,1] op_sel_hi:[1,1]\n"
      "v_pk_mul_f32 v[54:55], v[54:55], v[196:197] op_sel:[0,1] op_sel_hi:[1,1]\n"
      "v_pk_mul_f32 v[56:57], v[56:57], v[196:197] op_sel:[0,1] op_sel_hi:[1,1]\n"
      "v_pk_mul_f32 v[58:59], v[58:59], v[196:197] op_sel:[0,1] op_sel_hi:[1,1]\n"
      "v_pk_mul_f32 v[60:61], v[60:61], v[196:197] op_sel:[0,1] op_sel_hi:[1,1]\n"
      "v_pk_mul_f32 v[62:63], v[62:63], v[196:197] op_sel:[0,1] op_sel_hi:[1,1]\n"
      "v_pk_mul_f32 v[64:65], v[64:65], v[196:197] op_sel:[0,1] op_sel_hi:[1,1]\n"
      "v_pk_mul_f32 v[66:67], v[66:67], v[196:197] op_sel:[0,1] op_sel_hi:[1,1]\n"
      "v_pk_mul_f32 v[68:69], v[68:69], v[196:197] op_sel:[0,1] op_sel_hi:[1,1]\n"
      "v_pk_mul_f32 v[70:71], v[70:71], v[196:197] op_sel:[0,1] op_sel_hi:[1,1]\n"
      "v_pk_mul_f32 v[72:73], v[72:73], v[196:197] op_sel:[0,1] op_sel_hi:[1,1]\n"
      "v_pk_mul_f32 v[74:75], v[74:75], v[196:197] op_sel:[0,1] op_sel_hi:[1,1]\n"
      "v_pk_mul_f32 v[76:77], v[76:77], v[196:197] op_sel:[0,1] op_sel_hi:[1,1]\n"
      "v_pk_mul_f32 v[78:79], v[78:79], v[196:197] op_sel:[0,1] op_sel_hi:[1,1]\n"
      "v_pk_mul_f32 v[80:81], v[80:81], v[196:197] op_sel:[0,1] op_sel_hi:[1,1]\n"
      "v_pk_mul_f32 v[82:83], v[82:83], v[196:197] op_sel:[0,1] op_sel_hi:[1,1]\n"
      "v_pk_mul_f32 v[84:85], v[84:85], v[196:197] op_sel:[0,1] op_sel_hi:[1,1]\n"
      "v_pk_mul_f32 v[86:87], v[86:87], v[196:197] op_sel:[0,1] op_sel_hi:[1,1]\n"
      "v_pk_mul_f32 v[88:89], v[88:89], v[196:197] op_sel:[0,1] op_sel_hi:[1,1]\n"
      "v_pk_mul_f32 v[90:91], v[90:91], v[196:197] op_sel:[0,1] op_sel_hi:[1,1]\n"
      "v_pk_mul_f32 v[92:93], v[92:93], v[196:197] op_sel:[0,1] op_sel_hi:[1,1]\n"
      "v_pk_mul_f32 v[94:95], v[94:95], v[196:197] op_sel:[0,1] op_sel_hi:[1,1]\n"
      "v_pk_mul_f32 v[96:97], v[96:97], v[196:197] op_sel:[0,1] op_sel_hi:[1,1]\n"
      "v_pk_mul_f32 v[98:99], v[98:99], v[196:197] op_sel:[0,1] op_sel_hi:[1,1]\n"
      "v_pk_mul_f32 v[100:101], v[100:101], v[196:197] op_sel:[0,1] op_sel_hi:[1,1]\n"
      "v_pk_mul_f32 v[102:103], v[102:103], v[196:197] op_sel:[0,1] op_sel_hi:[1,1]\n"
      "v_pk_mul_f32 v[104:105], v[104:105], v[196:197] op_sel:[0,1] op_sel_hi:[1,1]\n"
      "v_pk_mul_f32 v[106:107], v[106:107], v[196:197] op_sel:[0,1] op_sel_hi:[1,1]\n"
      "v_pk_mul_f32 v[108:109], v[108:109], v[196:197] op_sel:[0,1] op_sel_hi:[1,1]\n"
      "v_pk_mul_f32 v[110:111], v[110:111], v[196:197] op_sel:[0,1] op_sel_hi:[1,1]\n"
      "v_pk_mul_f32 v[112:113], v[112:113], v[196:197] op_sel:[0,1] op_sel_hi:[1,1]\n"
      "v_pk_mul_f32 v[114:115], v[114:115], v[196:197] op_sel:[0,1] op_sel_hi:[1,1]\n"
      "v_pk_mul_f32 v[116:117], v[116:117], v[196:197] op_sel:[0,1] op_sel_hi:[1,1]\n"
      "v_pk_mul_f32 v[118:119], v[118:119], v[196:197] op_sel:[0,1] op_sel_hi:[1,1]\n"
      "v_pk_mul_f32 v[120:121], v[120:121], v[196:197] op_sel:[0,1] op_sel_hi:[1,1]\n"
      "v_pk_mul_f32 v[122:123], v[122:123], v[196:197] op_sel:[0,1] op_sel_hi:[1,1]\n"
      "v_pk_mul_f32 v[124:125], v[124:125], v[196:197] op_sel:[0,1] op_sel_hi:[1,1]\n"
      "v_pk_mul_f32 v[126:127], v[126:127], v[196:197] op_sel:[0,1] op_sel_hi:[1,1]\n"
      "s_mov_b32 s50, 0\n"
      "s_nop 1\n"
      "s_branch LcA_RRB7_%=\n"
      "LcA_RA28_%=:\n"
      "v_mov_b32 v196, v194\n"
      "s_nop 1\n"
      "v_permlane32_swap_b32 v196, v194\n"
      "s_nop 1\n"
      "v_max_f32 v194, v194, v196\n"
      "v_max_f32 v196, 0, v194\n"
      "v_sub_f32 v128, v128, v196\n"
      "v_sub_f32 v129, v129, v196\n"
      "v_sub_f32 v130, v130, v196\n"
      "v_sub_f32 v131, v131, v196\n"
      "v_sub_f32 v132, v132, v196\n"
      "v_sub_f32 v133, v133, v196\n"
      "v_sub_f32 v134, v134, v196\n"
      "v_sub_f32 v135, v135, v196\n"
      "v_sub_f32 v136, v136, v196\n"
      "v_sub_f32 v137, v137, v196\n"
      "v_sub_f32 v138, v138, v196\n"
      "v_sub_f32 v139, v139, v196\n"
      "v_sub_f32 v140, v140, v196\n"
      "v_sub_f32 v141, v141, v196\n"
      "v_sub_f32 v142, v142, v196\n"
      "v_sub_f32 v143, v143, v196\n"
      "v_sub_f32 v160, v160, v196\n"
      "v_sub_f32 v161, v161, v196\n"
      "v_sub_f32 v162, v162, v196\n"
      "v_sub_f32 v163, v163, v196\n"
      "v_sub_f32 v164, v164, v196\n"
      "v_sub_f32 v165, v165, v196\n"
      "v_sub_f32 v166, v166, v196\n"
      "v_sub_f32 v167, v167, v196\n"
      "v_sub_f32 v168, v168, v196\n"
      "v_sub_f32 v169, v169, v196\n"
      "v_sub_f32 v170, v170, v196\n"
      "v_sub_f32 v171, v171, v196\n"
      "v_sub_f32 v172, v172, v196\n"
      "v_sub_f32 v173, v173, v196\n"
      "v_sub_f32 v174, v174, v196\n"
      "v_sub_f32 v175, v175, v196\n"
      "v_sub_f32 v196, 0, v196\n"
      "v_exp_f32 v197, v196\n"
      "s_nop 1\n"
      "v_mul_f32 v192, v192, v197\n"
      "v_mul_f32 v193, v193, v197\n"
      "s_mov_b32 s50, 1\n"
      "s_nop 1\n"
      "s_branch LcA_RRA28_%=\n"
      "LcA_RB28_%=:\n"
      "s_nop 15\n"
      "v_pk_mul_f32 v[0:1], v[0:1], v[196:197] op_sel:[0,1] op_sel_hi:[1,1]\n"
      "v_pk_mul_f32 v[2:3], v[2:3], v[196:197] op_sel:[0,1] op_sel_hi:[1,1]\n"
      "v_pk_mul_f32 v[4:5], v[4:5], v[196:197] op_sel:[0,1] op_sel_hi:[1,1]\n"
      "v_pk_mul_f32 v[6:7], v[6:7], v[196:197] op_sel:[0,1] op_sel_hi:[1,1]\n"
      "v_pk_mul_f32 v[8:9], v[8:9], v[196:197] op_sel:[0,1] op_sel_hi:[1,1]\n"
      "v_pk_mul_f32 v[10:11], v[10:11], v[196:197] op_sel:[0,1] op_sel_hi:[1,1]\n"
      "v_pk_mul_f32 v[12:13], v[12:13], v[196:197] op_sel:[0,1] op_sel_hi:[1,1]\n"
      "v_pk_mul_f32 v[14:15], v[14:15], v[196:197] op_sel:[0,1] op_sel_hi:[1,1]\n"
      "v_pk_mul_f32 v[16:17], v[16:17], v[196:197] op_sel:[0,1] op_sel_hi:[1,1]\n"
      "v_pk_mul_f32 v[18:19], v[18:19], v[196:197] op_sel:[0,1] op_sel_hi:[1,1]\n"
      "v_pk_mul_f32 v[20:21], v[20:21], v[196:197] op_sel:[0,1] op_sel_hi:[1,1]\n"
      "v_pk_mul_f32 v[22:23], v[22:23], v[196:197] op_sel:[0,1] op_sel_hi:[1,1]\n"
      "v_pk_mul_f32 v[24:25], v[24:25], v[196:197] op_sel:[0,1] op_sel_hi:[1,1]\n"
      "v_pk_mul_f32 v[26:27], v[26:27], v[196:197] op_sel:[0,1] op_sel_hi:[1,1]\n"
      "v_pk_mul_f32 v[28:29], v[28:29], v[196:197] op_sel:[0,1] op_sel_hi:[1,1]\n"
      "v_pk_mul_f32 v[30:31], v[30:31], v[196:197] op_sel:[0,1] op_sel_hi:[1,1]\n"
      "v_pk_mul_f32 v[32:33], v[32:33], v[196:197] op_sel:[0,1] op_sel_hi:[1,1]\n"
      "v_pk_mul_f32 v[34:35], v[34:35], v[196:197] op_sel:[0,1] op_sel_hi:[1,1]\n"
      "v_pk_mul_f32 v[36:37], v[36:37], v[196:197] op_sel:[0,1] op_sel_hi:[1,1]\n"
      "v_pk_mul_f32 v[38:39], v[38:39], v[196:197] op_sel:[0,1] op_sel_hi:[1,1]\n"
      "v_pk_mul_f32 v[40:41], v[40:41], v[196:197] op_sel:[0,1] op_sel_hi:[1,1]\n"
      "v_pk_mul_f32 v[42:43], v[42:43], v[196:197] op_sel:[0,1] op_sel_hi:[1,1]\n"
      "v_pk_mul_f32 v[44:45], v[44:45], v[196:197] op_sel:[0,1] op_sel_hi:[1,1]\n"
      "v_pk_mul_f32 v[46:47], v[46:47], v[196:197] op_sel:[0,1] op_sel_hi:[1,1]\n"
      "v_pk_mul_f32 v[48:49], v[48:49], v[196:197] op_sel:[0,1] op_sel_hi:[1,1]\n"
      "v_pk_mul_f32 v[50:51], v[50:51], v[196:197] op_sel:[0,1] op_sel_hi:[1,1]\n"
      "v_pk_mul_f32 v[52:53], v[52:53], v[196:197] op_sel:[0,1] op_sel_hi:[1,1]\n"
      "v_pk_mul_f32 v[54:55], v[54:55], v[196:197] op_sel:[0,1] op_sel_hi:[1,1]\n"
      "v_pk_mul_f32 v[56:57], v[56:57], v[196:197] op_sel:[0,1] op_sel_hi:[1,1]\n"
      "v_pk_mul_f32 v[58:59], v[58:59], v[196:197] op_sel:[0,1] op_sel_hi:[1,1]\n"
      "v_pk_mul_f32 v[60:61], v[60:61], v[196:197] op_sel:[0,1] op_sel_hi:[1,1]\n"
      "v_pk_mul_f32 v[62:63], v[62:63], v[196:197] op_sel:[0,1] op_sel_hi:[1,1]\n"
      "v_pk_mul_f32 v[64:65], v[64:65], v[196:197] op_sel:[0,1] op_sel_hi:[1,1]\n"
      "v_pk_mul_f32 v[66:67], v[66:67], v[196:197] op_sel:[0,1] op_sel_hi:[1,1]\n"
      "v_pk_mul_f32 v[68:69], v[68:69], v[196:197] op_sel:[0,1] op_sel_hi:[1,1]\n"
      "v_pk_mul_f32 v[70:71], v[70:71], v[196:197] op_sel:[0,1] op_sel_hi:[1,1]\n"
      "v_pk_mul_f32 v[72:73], v[72:73], v[196:197] op_sel:[0,1] op_sel_hi:[1,1]\n"
      "v_pk_mul_f32 v[74:75], v[74:75], v[196:197] op_sel:[0,1] op_sel_hi:[1,1]\n"
      "v_pk_mul_f32 v[76:77], v[76:77], v[196:197] op_sel:[0,1] op_sel_hi:[1,1]\n"
      "v_pk_mul_f32 v[78:79], v[78:79], v[196:197] op_sel:[0,1] op_sel_hi:[1,1]\n"
      "v_pk_mul_f32 v[80:81], v[80:81], v[196:197] op_sel:[0,1] op_sel_hi:[1,1]\n"
      "v_pk_mul_f32 v[82:83], v[82:83], v[196:197] op_sel:[0,1] op_sel_hi:[1,1]\n"
      "v_pk_mul_f32 v[84:85], v[84:85], v[196:197] op_sel:[0,1] op_sel_hi:[1,1]\n"
      "v_pk_mul_f32 v[86:87], v[86:87], v[196:197] op_sel:[0,1] op_sel_hi:[1,1]\n"
      "v_pk_mul_f32 v[88:89], v[88:89], v[196:197] op_sel:[0,1] op_sel_hi:[1,1]\n"
      "v_pk_mul_f32 v[90:91], v[90:91], v[196:197] op_sel:[0,1] op_sel_hi:[1,1]\n"
      "v_pk_mul_f32 v[92:93], v[92:93], v[196:197] op_sel:[0,1] op_sel_hi:[1,1]\n"
      "v_pk_mul_f32 v[94:95], v[94:95], v[196:197] op_sel:[0,1] op_sel_hi:[1,1]\n"
      "v_pk_mul_f32 v[96:97], v[96:97], v[196:197] op_sel:[0,1] op_sel_hi:[1,1]\n"
      "v_pk_mul_f32 v[98:99], v[98:99], v[196:197] op_sel:[0,1] op_sel_hi:[1,1]\n"
      "v_pk_mul_f32 v[100:101], v[100:101], v[196:197] op_sel:[0,1] op_sel_hi:[1,1]\n"
      "v_pk_mul_f32 v[102:103], v[102:103], v[196:197] op_sel:[0,1] op_sel_hi:[1,1]\n"
      "v_pk_mul_f32 v[104:105], v[104:105], v[196:197] op_sel:[0,1] op_sel_hi:[1,1]\n"
      "v_pk_mul_f32 v[106:107], v[106:107], v[196:197] op_sel:[0,1] op_sel_hi:[1,1]\n"
      "v_pk_mul_f32 v[108:109], v[108:109], v[196:197] op_sel:[0,1] op_sel_hi:[1,1]\n"
      "v_pk_mul_f32 v[110:111], v[110:111], v[196:197] op_sel:[0,1] op_sel_hi:[1,1]\n"
      "v_pk_mul_f32 v[112:113], v[112:113], v[196:197] op_sel:[0,1] op_sel_hi:[1,1]\n"
      "v_pk_mul_f32 v[114:115], v[114:115], v[196:197] op_sel:[0,1] op_sel_hi:[1,1]\n"
      "v_pk_mul_f32 v[116:117], v[116:117], v[196:197] op_sel:[0,1] op_sel_hi:[1,1]\n"
      "v_pk_mul_f32 v[118:119], v[118:119], v[196:197] op_sel:[0,1] op_sel_hi:[1,1]\n"
      "v_pk_mul_f32 v[120:121], v[120:121], v[196:197] op_sel:[0,1] op_sel_hi:[1,1]\n"
      "v_pk_mul_f32 v[122:123], v[122:123], v[196:197] op_sel:[0,1] op_sel_hi:[1,1]\n"
      "v_pk_mul_f32 v[124:125], v[124:125], v[196:197] op_sel:[0,1] op_sel_hi:[1,1]\n"
      "v_pk_mul_f32 v[126:127], v[126:127], v[196:197] op_sel:[0,1] op_sel_hi:[1,1]\n"
      "s_mov_b32 s50, 0\n"
      "s_nop 1\n"
      "s_branch LcA_RRB28_%=\n"
      "LcA_RA29_%=:\n"
      "v_mov_b32 v196, v194\n"
      "s_nop 1\n"
      "v_permlane32_swap_b32 v196, v194\n"
      "s_nop 1\n"
      "v_max_f32 v194, v194, v196\n"
      "v_max_f32 v196, 0, v194\n"
      "v_sub_f32 v144, v144, v196\n"
      "v_sub_f32 v145, v145, v196\n"
      "v_sub_f32 v146, v146, v196\n"
      "v_sub_f32 v147, v147, v196\n"
      "v_sub_f32 v148, v148, v196\n"
      "v_sub_f32 v149, v149, v196\n"
      "v_sub_f32 v150, v150, v196\n"
      "v_sub_f32 v151, v151, v196\n"
      "v_sub_f32 v152, v152, v196\n"
      "v_sub_f32 v153, v153, v196\n"
      "v_sub_f32 v154, v154, v196\n"
      "v_sub_f32 v155, v155, v196\n"
      "v_sub_f32 v156, v156, v196\n"
      "v_sub_f32 v157, v157, v196\n"
      "v_sub_f32 v158, v158, v196\n"
      "v_sub_f32 v159, v159, v196\n"
      "v_sub_f32 v160, v160, v196\n"
      "v_sub_f32 v161, v161, v196\n"
      "v_sub_f32 v162, v162, v196\n"
      "v_sub_f32 v163, v163, v196\n"
      "v_sub_f32 v164, v164, v196\n"
      "v_sub_f32 v165, v165, v196\n"
      "v_sub_f32 v166, v166, v196\n"
      "v_sub_f32 v167, v167, v196\n"
      "v_sub_f32 v168, v168, v196\n"
      "v_sub_f32 v169, v169, v196\n"
      "v_sub_f32 v170, v170, v196\n"
      "v_sub_f32 v171, v171, v196\n"
      "v_sub_f32 v172, v172, v196\n"
      "v_sub_f32 v173, v173, v196\n"
      "v_sub_f32 v174, v174, v196\n"
      "v_sub_f32 v175, v175, v196\n"
      "v_sub_f32 v196, 0, v196\n"
      "v_exp_f32 v197, v196\n"
      "s_nop 1\n"
      "v_mul_f32 v192, v192, v197\n"
      "v_mul_f32 v193, v193, v197\n"
      "s_mov_b32 s50, 1\n"
      "s_nop 1\n"
      "s_branch LcA_RRA29_%=\n"
      "LcA_RB29_%=:\n"
      "s_nop 15\n"
      "v_pk_mul_f32 v[0:1], v[0:1], v[196:197] op_sel:[0,1] op_sel_hi:[1,1]\n"
      "v_pk_mul_f32 v[2:3], v[2:3], v[196:197] op_sel:[0,1] op_sel_hi:[1,1]\n"
      "v_pk_mul_f32 v[4:5], v[4:5], v[196:197] op_sel:[0,1] op_sel_hi:[1,1]\n"
      "v_pk_mul_f32 v[6:7], v[6:7], v[196:197] op_sel:[0,1] op_sel_hi:[1,1]\n"
      "v_pk_mul_f32 v[8:9], v[8:9], v[196:197] op_sel:[0,1] op_sel_hi:[1,1]\n"
      "v_pk_mul_f32 v[10:11], v[10:11], v[196:197] op_sel:[0,1] op_sel_hi:[1,1]\n"
      "v_pk_mul_f32 v[12:13], v[12:13], v[196:197] op_sel:[0,1] op_sel_hi:[1,1]\n"
      "v_pk_mul_f32 v[14:15], v[14:15], v[196:197] op_sel:[0,1] op_sel_hi:[1,1]\n"
      "v_pk_mul_f32 v[16:17], v[16:17], v[196:197] op_sel:[0,1] op_sel_hi:[1,1]\n"
      "v_pk_mul_f32 v[18:19], v[18:19], v[196:197] op_sel:[0,1] op_sel_hi:[1,1]\n"
      "v_pk_mul_f32 v[20:21], v[20:21], v[196:197] op_sel:[0,1] op_sel_hi:[1,1]\n"
      "v_pk_mul_f32 v[22:23], v[22:23], v[196:197] op_sel:[0,1] op_sel_hi:[1,1]\n"
      "v_pk_mul_f32 v[24:25], v[24:25], v[196:197] op_sel:[0,1] op_sel_hi:[1,1]\n"
      "v_pk_mul_f32 v[26:27], v[26:27], v[196:197] op_sel:[0,1] op_sel_hi:[1,1]\n"
      "v_pk_mul_f32 v[28:29], v[28:29], v[196:197] op_sel:[0,1] op_sel_hi:[1,1]\n"
      "v_pk_mul_f32 v[30:31], v[30:31], v[196:197] op_sel:[0,1] op_sel_hi:[1,1]\n"
      "v_pk_mul_f32 v[32:33], v[32:33], v[196:197] op_sel:[0,1] op_sel_hi:[1,1]\n"
      "v_pk_mul_f32 v[34:35], v[34:35], v[196:197] op_sel:[0,1] op_sel_hi:[1,1]\n"
      "v_pk_mul_f32 v[36:37], v[36:37], v[196:197] op_sel:[0,1] op_sel_hi:[1,1]\n"
      "v_pk_mul_f32 v[38:39], v[38:39], v[196:197] op_sel:[0,1] op_sel_hi:[1,1]\n"
      "v_pk_mul_f32 v[40:41], v[40:41], v[196:197] op_sel:[0,1] op_sel_hi:[1,1]\n"
      "v_pk_mul_f32 v[42:43], v[42:43], v[196:197] op_sel:[0,1] op_sel_hi:[1,1]\n"
      "v_pk_mul_f32 v[44:45], v[44:45], v[196:197] op_sel:[0,1] op_sel_hi:[1,1]\n"
      "v_pk_mul_f32 v[46:47], v[46:47], v[196:197] op_sel:[0,1] op_sel_hi:[1,1]\n"
      "v_pk_mul_f32 v[48:49], v[48:49], v[196:197] op_sel:[0,1] op_sel_hi:[1,1]\n"
      "v_pk_mul_f32 v[50:51], v[50:51], v[196:197] op_sel:[0,1] op_sel_hi:[1,1]\n"
      "v_pk_mul_f32 v[52:53], v[52:53], v[196:197] op_sel:[0,1] op_sel_hi:[1,1]\n"
      "v_pk_mul_f32 v[54:55], v[54:55], v[196:197] op_sel:[0,1] op_sel_hi:[1,1]\n"
      "v_pk_mul_f32 v[56:57], v[56:57], v[196:197] op_sel:[0,1] op_sel_hi:[1,1]\n"
      "v_pk_mul_f32 v[58:59], v[58:59], v[196:197] op_sel:[0,1] op_sel_hi:[1,1]\n"
      "v_pk_mul_f32 v[60:61], v[60:61], v[196:197] op_sel:[0,1] op_sel_hi:[1,1]\n"
      "v_pk_mul_f32 v[62:63], v[62:63], v[196:197] op_sel:[0,1] op_sel_hi:[1,1]\n"
      "v_pk_mul_f32 v[64:65], v[64:65], v[196:197] op_sel:[0,1] op_sel_hi:[1,1]\n"
      "v_pk_mul_f32 v[66:67], v[66:67], v[196:197] op_sel:[0,1] op_sel_hi:[1,1]\n"
      "v_pk_mul_f32 v[68:69], v[68:69], v[196:197] op_sel:[0,1] op_sel_hi:[1,1]\n"
      "v_pk_mul_f32 v[70:71], v[70:71], v[196:197] op_sel:[0,1] op_sel_hi:[1,1]\n"
      "v_pk_mul_f32 v[72:73], v[72:73], v[196:197] op_sel:[0,1] op_sel_hi:[1,1]\n"
      "v_pk_mul_f32 v[74:75], v[74:75], v[196:197] op_sel:[0,1] op_sel_hi:[1,1]\n"
      "v_pk_mul_f32 v[76:77], v[76:77], v[196:197] op_sel:[0,1] op_sel_hi:[1,1]\n"
      "v_pk_mul_f32 v[78:79], v[78:79], v[196:197] op_sel:[0,1] op_sel_hi:[1,1]\n"
      "v_pk_mul_f32 v[80:81], v[80:81], v[196:197] op_sel:[0,1] op_sel_hi:[1,1]\n"
      "v_pk_mul_f32 v[82:83], v[82:83], v[196:197] op_sel:[0,1] op_sel_hi:[1,1]\n"
      "v_pk_mul_f32 v[84:85], v[84:85], v[196:197] op_sel:[0,1] op_sel_hi:[1,1]\n"
      "v_pk_mul_f32 v[86:87], v[86:87], v[196:197] op_sel:[0,1] op_sel_hi:[1,1]\n"
      "v_pk_mul_f32 v[88:89], v[88:89], v[196:197] op_sel:[0,1] op_sel_hi:[1,1]\n"
      "v_pk_mul_f32 v[90:91], v[90:91], v[196:197] op_sel:[0,1] op_sel_hi:[1,1]\n"
      "v_pk_mul_f32 v[92:93], v[92:93], v[196:197] op_sel:[0,1] op_sel_hi:[1,1]\n"
      "v_pk_mul_f32 v[94:95], v[94:95], v[196:197] op_sel:[0,1] op_sel_hi:[1,1]\n"
      "v_pk_mul_f32 v[96:97], v[96:97], v[196:197] op_sel:[0,1] op_sel_hi:[1,1]\n"
      "v_pk_mul_f32 v[98:99], v[98:99], v[196:197] op_sel:[0,1] op_sel_hi:[1,1]\n"
      "v_pk_mul_f32 v[100:101], v[100:101], v[196:197] op_sel:[0,1] op_sel_hi:[1,1]\n"
      "v_pk_mul_f32 v[102:103], v[102:103], v[196:197] op_sel:[0,1] op_sel_hi:[1,1]\n"
      "v_pk_mul_f32 v[104:105], v[104:105], v[196:197] op_sel:[0,1] op_sel_hi:[1,1]\n"
      "v_pk_mul_f32 v[106:107], v[106:107], v[196:197] op_sel:[0,1] op_sel_hi:[1,1]\n"
      "v_pk_mul_f32 v[108:109], v[108:109], v[196:197] op_sel:[0,1] op_sel_hi:[1,1]\n"
      "v_pk_mul_f32 v[110:111], v[110:111], v[196:197] op_sel:[0,1] op_sel_hi:[1,1]\n"
      "v_pk_mul_f32 v[112:113], v[112:113], v[196:197] op_sel:[0,1] op_sel_hi:[1,1]\n"
      "v_pk_mul_f32 v[114:115], v[114:115], v[196:197] op_sel:[0,1] op_sel_hi:[1,1]\n"
      "v_pk_mul_f32 v[116:117], v[116:117], v[196:197] op_sel:[0,1] op_sel_hi:[1,1]\n"
      "v_pk_mul_f32 v[118:119], v[118:119], v[196:197] op_sel:[0,1] op_sel_hi:[1,1]\n"
      "v_pk_mul_f32 v[120:121], v[120:121], v[196:197] op_sel:[0,1] op_sel_hi:[1,1]\n"
      "v_pk_mul_f32 v[122:123], v[122:123], v[196:197] op_sel:[0,1] op_sel_hi:[1,1]\n"
      "v_pk_mul_f32 v[124:125], v[124:125], v[196:197] op_sel:[0,1] op_sel_hi:[1,1]\n"
      "v_pk_mul_f32 v[126:127], v[126:127], v[196:197] op_sel:[0,1] op_sel_hi:[1,1]\n"
      "s_mov_b32 s50, 0\n"
      "s_nop 1\n"
      "s_branch LcA_RRB29_%=\n"
      "LcA_RA30_%=:\n"
      "v_mov_b32 v196, v194\n"
      "s_nop 1\n"
      "v_permlane32_swap_b32 v196, v194\n"
      "s_nop 1\n"
      "v_max_f32 v194, v194, v196\n"
      "v_max_f32 v196, 0, v194\n"
      "v_sub_f32 v128, v128, v196\n"
      "v_sub_f32 v129, v129, v196\n"
      "v_sub_f32 v130, v130, v196\n"
      "v_sub_f32 v131, v131, v196\n"
      "v_sub_f32 v132, v132, v196\n"
      "v_sub_f32 v133, v133, v196\n"
      "v_sub_f32 v134, v134, v196\n"
      "v_sub_f32 v135, v135, v196\n"
      "v_sub_f32 v136, v136, v196\n"
      "v_sub_f32 v137, v137, v196\n"
      "v_sub_f32 v138, v138, v196\n"
      "v_sub_f32 v139, v139, v196\n"
      "v_sub_f32 v140, v140, v196\n"
      "v_sub_f32 v141, v141, v196\n"
      "v_sub_f32 v142, v142, v196\n"
      "v_sub_f32 v143, v143, v196\n"
      "v_sub_f32 v160, v160, v196\n"
      "v_sub_f32 v161, v161, v196\n"
      "v_sub_f32 v162, v162, v196\n"
      "v_sub_f32 v163, v163, v196\n"
      "v_sub_f32 v164, v164, v196\n"
      "v_sub_f32 v165, v165, v196\n"
      "v_sub_f32 v166, v166, v196\n"
      "v_sub_f32 v167, v167, v196\n"
      "v_sub_f32 v168, v168, v196\n"
      "v_sub_f32 v169, v169, v196\n"
      "v_sub_f32 v170, v170, v196\n"
      "v_sub_f32 v171, v171, v196\n"
      "v_sub_f32 v172, v172, v196\n"
      "v_sub_f32 v173, v173, v196\n"
      "v_sub_f32 v174, v174, v196\n"
      "v_sub_f32 v175, v175, v196\n"
      "v_sub_f32 v196, 0, v196\n"
      "v_exp_f32 v197, v196\n"
      "s_nop 1\n"
      "v_mul_f32 v192, v192, v197\n"
      "v_mul_f32 v193, v193, v197\n"
      "s_mov_b32 s50, 1\n"
      "s_nop 1\n"
      "s_branch LcA_RRA30_%=\n"
      "LcA_RB30_%=:\n"
      "s_nop 15\n"
      "v_pk_mul_f32 v[0:1], v[0:1], v[196:197] op_sel:[0,1] op_sel_hi:[1,1]\n"
      "v_pk_mul_f32 v[2:3], v[2:3], v[196:197] op_sel:[0,1] op_sel_hi:[1,1]\n"
      "v_pk_mul_f32 v[4:5], v[4:5], v[196:197] op_sel:[0,1] op_sel_hi:[1,1]\n"
      "v_pk_mul_f32 v[6:7], v[6:7], v[196:197] op_sel:[0,1] op_sel_hi:[1,1]\n"
      "v_pk_mul_f32 v[8:9], v[8:9], v[196:197] op_sel:[0,1] op_sel_hi:[1,1]\n"
      "v_pk_mul_f32 v[10:11], v[10:11], v[196:197] op_sel:[0,1] op_sel_hi:[1,1]\n"
      "v_pk_mul_f32 v[12:13], v[12:13], v[196:197] op_sel:[0,1] op_sel_hi:[1,1]\n"
      "v_pk_mul_f32 v[14:15], v[14:15], v[196:197] op_sel:[0,1] op_sel_hi:[1,1]\n"
      "v_pk_mul_f32 v[16:17], v[16:17], v[196:197] op_sel:[0,1] op_sel_hi:[1,1]\n"
      "v_pk_mul_f32 v[18:19], v[18:19], v[196:197] op_sel:[0,1] op_sel_hi:[1,1]\n"
      "v_pk_mul_f32 v[20:21], v[20:21], v[196:197] op_sel:[0,1] op_sel_hi:[1,1]\n"
      "v_pk_mul_f32 v[22:23], v[22:23], v[196:197] op_sel:[0,1] op_sel_hi:[1,1]\n"
      "v_pk_mul_f32 v[24:25], v[24:25], v[196:197] op_sel:[0,1] op_sel_hi:[1,1]\n"
      "v_pk_mul_f32 v[26:27], v[26:27], v[196:197] op_sel:[0,1] op_sel_hi:[1,1]\n"
      "v_pk_mul_f32 v[28:29], v[28:29], v[196:197] op_sel:[0,1] op_sel_hi:[1,1]\n"
      "v_pk_mul_f32 v[30:31], v[30:31], v[196:197] op_sel:[0,1] op_sel_hi:[1,1]\n"
      "v_pk_mul_f32 v[32:33], v[32:33], v[196:197] op_sel:[0,1] op_sel_hi:[1,1]\n"
      "v_pk_mul_f32 v[34:35], v[34:35], v[196:197] op_sel:[0,1] op_sel_hi:[1,1]\n"
      "v_pk_mul_f32 v[36:37], v[36:37], v[196:197] op_sel:[0,1] op_sel_hi:[1,1]\n"
      "v_pk_mul_f32 v[38:39], v[38:39], v[196:197] op_sel:[0,1] op_sel_hi:[1,1]\n"
      "v_pk_mul_f32 v[40:41], v[40:41], v[196:197] op_sel:[0,1] op_sel_hi:[1,1]\n"
      "v_pk_mul_f32 v[42:43], v[42:43], v[196:197] op_sel:[0,1] op_sel_hi:[1,1]\n"
      "v_pk_mul_f32 v[44:45], v[44:45], v[196:197] op_sel:[0,1] op_sel_hi:[1,1]\n"
      "v_pk_mul_f32 v[46:47], v[46:47], v[196:197] op_sel:[0,1] op_sel_hi:[1,1]\n"
      "v_pk_mul_f32 v[48:49], v[48:49], v[196:197] op_sel:[0,1] op_sel_hi:[1,1]\n"
      "v_pk_mul_f32 v[50:51], v[50:51], v[196:197] op_sel:[0,1] op_sel_hi:[1,1]\n"
      "v_pk_mul_f32 v[52:53], v[52:53], v[196:197] op_sel:[0,1] op_sel_hi:[1,1]\n"
      "v_pk_mul_f32 v[54:55], v[54:55], v[196:197] op_sel:[0,1] op_sel_hi:[1,1]\n"
      "v_pk_mul_f32 v[56:57], v[56:57], v[196:197] op_sel:[0,1] op_sel_hi:[1,1]\n"
      "v_pk_mul_f32 v[58:59], v[58:59], v[196:197] op_sel:[0,1] op_sel_hi:[1,1]\n"
      "v_pk_mul_f32 v[60:61], v[60:61], v[196:197] op_sel:[0,1] op_sel_hi:[1,1]\n"
      "v_pk_mul_f32 v[62:63], v[62:63], v[196:197] op_sel:[0,1] op_sel_hi:[1,1]\n"
      "v_pk_mul_f32 v[64:65], v[64:65], v[196:197] op_sel:[0,1] op_sel_hi:[1,1]\n"
      "v_pk_mul_f32 v[66:67], v[66:67], v[196:197] op_sel:[0,1] op_sel_hi:[1,1]\n"
      "v_pk_mul_f32 v[68:69], v[68:69], v[196:197] op_sel:[0,1] op_sel_hi:[1,1]\n"
      "v_pk_mul_f32 v[70:71], v[70:71], v[196:197] op_sel:[0,1] op_sel_hi:[1,1]\n"
      "v_pk_mul_f32 v[72:73], v[72:73], v[196:197] op_sel:[0,1] op_sel_hi:[1,1]\n"
      "v_pk_mul_f32 v[74:75], v[74:75], v[196:197] op_sel:[0,1] op_sel_hi:[1,1]\n"
      "v_pk_mul_f32 v[76:77], v[76:77], v[196:197] op_sel:[0,1] op_sel_hi:[1,1]\n"
      "v_pk_mul_f32 v[78:79], v[78:79], v[196:197] op_sel:[0,1] op_sel_hi:[1,1]\n"
      "v_pk_mul_f32 v[80:81], v[80:81], v[196:197] op_sel:[0,1] op_sel_hi:[1,1]\n"
      "v_pk_mul_f32 v[82:83], v[82:83], v[196:197] op_sel:[0,1] op_sel_hi:[1,1]\n"
      "v_pk_mul_f32 v[84:85], v[84:85], v[196:197] op_sel:[0,1] op_sel_hi:[1,1]\n"
      "v_pk_mul_f32 v[86:87], v[86:87], v[196:197] op_sel:[0,1] op_sel_hi:[1,1]\n"
      "v_pk_mul_f32 v[88:89], v[88:89], v[196:197] op_sel:[0,1] op_sel_hi:[1,1]\n"
      "v_pk_mul_f32 v[90:91], v[90:91], v[196:197] op_sel:[0,1] op_sel_hi:[1,1]\n"
      "v_pk_mul_f32 v[92:93], v[92:93], v[196:197] op_sel:[0,1] op_sel_hi:[1,1]\n"
      "v_pk_mul_f32 v[94:95], v[94:95], v[196:197] op_sel:[0,1] op_sel_hi:[1,1]\n"
      "v_pk_mul_f32 v[96:97], v[96:97], v[196:197] op_sel:[0,1] op_sel_hi:[1,1]\n"
      "v_pk_mul_f32 v[98:99], v[98:99], v[196:197] op_sel:[0,1] op_sel_hi:[1,1]\n"
      "v_pk_mul_f32 v[100:101], v[100:101], v[196:197] op_sel:[0,1] op_sel_hi:[1,1]\n"
      "v_pk_mul_f32 v[102:103], v[102:103], v[196:197] op_sel:[0,1] op_sel_hi:[1,1]\n"
      "v_pk_mul_f32 v[104:105], v[104:105], v[196:197] op_sel:[0,1] op_sel_hi:[1,1]\n"
      "v_pk_mul_f32 v[106:107], v[106:107], v[196:197] op_sel:[0,1] op_sel_hi:[1,1]\n"
      "v_pk_mul_f32 v[108:109], v[108:109], v[196:197] op_sel:[0,1] op_sel_hi:[1,1]\n"
      "v_pk_mul_f32 v[110:111], v[110:111], v[196:197] op_sel:[0,1] op_sel_hi:[1,1]\n"
      "v_pk_mul_f32 v[112:113], v[112:113], v[196:197] op_sel:[0,1] op_sel_hi:[1,1]\n"
      "v_pk_mul_f32 v[114:115], v[114:115], v[196:197] op_sel:[0,1] op_sel_hi:[1,1]\n"
      "v_pk_mul_f32 v[116:117], v[116:117], v[196:197] op_sel:[0,1] op_sel_hi:[1,1]\n"
      "v_pk_mul_f32 v[118:119], v[118:119], v[196:197] op_sel:[0,1] op_sel_hi:[1,1]\n"
      "v_pk_mul_f32 v[120:121], v[120:121], v[196:197] op_sel:[0,1] op_sel_hi:[1,1]\n"
      "v_pk_mul_f32 v[122:123], v[122:123], v[196:197] op_sel:[0,1] op_sel_hi:[1,1]\n"
      "v_pk_mul_f32 v[124:125], v[124:125], v[196:197] op_sel:[0,1] op_sel_hi:[1,1]\n"
      "v_pk_mul_f32 v[126:127], v[126:127], v[196:197] op_sel:[0,1] op_sel_hi:[1,1]\n"
      "s_mov_b32 s50, 0\n"
      "s_nop 1\n"
      "s_branch LcA_RRB30_%=\n"
      "LcA_RA31_%=:\n"
      "v_mov_b32 v196, v194\n"
      "s_nop 1\n"
      "v_permlane32_swap_b32 v196, v194\n"
      "s_nop 1\n"
      "v_max_f32 v194, v194, v196\n"
      "v_max_f32 v196, 0, v194\n"
      "v_sub_f32 v144, v144, v196\n"
      "v_sub_f32 v145, v145, v196\n"
      "v_sub_f32 v146, v146, v196\n"
      "v_sub_f32 v147, v147, v196\n"
      "v_sub_f32 v148, v148, v196\n"
      "v_sub_f32 v149, v149, v196\n"
      "v_sub_f32 v150, v150, v196\n"
      "v_sub_f32 v151, v151, v196\n"
      "v_sub_f32 v152, v152, v196\n"
      "v_sub_f32 v153, v153, v196\n"
      "v_sub_f32 v154, v154, v196\n"
      "v_sub_f32 v155, v155, v196\n"
      "v_sub_f32 v156, v156, v196\n"
      "v_sub_f32 v157, v157, v196\n"
      "v_sub_f32 v158, v158, v196\n"
      "v_sub_f32 v159, v159, v196\n"
      "v_sub_f32 v160, v160, v196\n"
      "v_sub_f32 v161, v161, v196\n"
      "v_sub_f32 v162, v162, v196\n"
      "v_sub_f32 v163, v163, v196\n"
      "v_sub_f32 v164, v164, v196\n"
      "v_sub_f32 v165, v165, v196\n"
      "v_sub_f32 v166, v166, v196\n"
      "v_sub_f32 v167, v167, v196\n"
      "v_sub_f32 v168, v168, v196\n"
      "v_sub_f32 v169, v169, v196\n"
      "v_sub_f32 v170, v170, v196\n"
      "v_sub_f32 v171, v171, v196\n"
      "v_sub_f32 v172, v172, v196\n"
      "v_sub_f32 v173, v173, v196\n"
      "v_sub_f32 v174, v174, v196\n"
      "v_sub_f32 v175, v175, v196\n"
      "v_sub_f32 v196, 0, v196\n"
      "v_exp_f32 v197, v196\n"
      "s_nop 1\n"
      "v_mul_f32 v192, v192, v197\n"
      "v_mul_f32 v193, v193, v197\n"
      "s_mov_b32 s50, 1\n"
      "s_nop 1\n"
      "s_branch LcA_RRA31_%=\n"
      "LcA_RB31_%=:\n"
      "s_nop 15\n"
      "v_pk_mul_f32 v[0:1], v[0:1], v[196:197] op_sel:[0,1] op_sel_hi:[1,1]\n"
      "v_pk_mul_f32 v[2:3], v[2:3], v[196:197] op_sel:[0,1] op_sel_hi:[1,1]\n"
      "v_pk_mul_f32 v[4:5], v[4:5], v[196:197] op_sel:[0,1] op_sel_hi:[1,1]\n"
      "v_pk_mul_f32 v[6:7], v[6:7], v[196:197] op_sel:[0,1] op_sel_hi:[1,1]\n"
      "v_pk_mul_f32 v[8:9], v[8:9], v[196:197] op_sel:[0,1] op_sel_hi:[1,1]\n"
      "v_pk_mul_f32 v[10:11], v[10:11], v[196:197] op_sel:[0,1] op_sel_hi:[1,1]\n"
      "v_pk_mul_f32 v[12:13], v[12:13], v[196:197] op_sel:[0,1] op_sel_hi:[1,1]\n"
      "v_pk_mul_f32 v[14:15], v[14:15], v[196:197] op_sel:[0,1] op_sel_hi:[1,1]\n"
      "v_pk_mul_f32 v[16:17], v[16:17], v[196:197] op_sel:[0,1] op_sel_hi:[1,1]\n"
      "v_pk_mul_f32 v[18:19], v[18:19], v[196:197] op_sel:[0,1] op_sel_hi:[1,1]\n"
      "v_pk_mul_f32 v[20:21], v[20:21], v[196:197] op_sel:[0,1] op_sel_hi:[1,1]\n"
      "v_pk_mul_f32 v[22:23], v[22:23], v[196:197] op_sel:[0,1] op_sel_hi:[1,1]\n"
      "v_pk_mul_f32 v[24:25], v[24:25], v[196:197] op_sel:[0,1] op_sel_hi:[1,1]\n"
      "v_pk_mul_f32 v[26:27], v[26:27], v[196:197] op_sel:[0,1] op_sel_hi:[1,1]\n"
      "v_pk_mul_f32 v[28:29], v[28:29], v[196:197] op_sel:[0,1] op_sel_hi:[1,1]\n"
      "v_pk_mul_f32 v[30:31], v[30:31], v[196:197] op_sel:[0,1] op_sel_hi:[1,1]\n"
      "v_pk_mul_f32 v[32:33], v[32:33], v[196:197] op_sel:[0,1] op_sel_hi:[1,1]\n"
      "v_pk_mul_f32 v[34:35], v[34:35], v[196:197] op_sel:[0,1] op_sel_hi:[1,1]\n"
      "v_pk_mul_f32 v[36:37], v[36:37], v[196:197] op_sel:[0,1] op_sel_hi:[1,1]\n"
      "v_pk_mul_f32 v[38:39], v[38:39], v[196:197] op_sel:[0,1] op_sel_hi:[1,1]\n"
      "v_pk_mul_f32 v[40:41], v[40:41], v[196:197] op_sel:[0,1] op_sel_hi:[1,1]\n"
      "v_pk_mul_f32 v[42:43], v[42:43], v[196:197] op_sel:[0,1] op_sel_hi:[1,1]\n"
      "v_pk_mul_f32 v[44:45], v[44:45], v[196:197] op_sel:[0,1] op_sel_hi:[1,1]\n"
      "v_pk_mul_f32 v[46:47], v[46:47], v[196:197] op_sel:[0,1] op_sel_hi:[1,1]\n"
      "v_pk_mul_f32 v[48:49], v[48:49], v[196:197] op_sel:[0,1] op_sel_hi:[1,1]\n"
      "v_pk_mul_f32 v[50:51], v[50:51], v[196:197] op_sel:[0,1] op_sel_hi:[1,1]\n"
      "v_pk_mul_f32 v[52:53], v[52:53], v[196:197] op_sel:[0,1] op_sel_hi:[1,1]\n"
      "v_pk_mul_f32 v[54:55], v[54:55], v[196:197] op_sel:[0,1] op_sel_hi:[1,1]\n"
      "v_pk_mul_f32 v[56:57], v[56:57], v[196:197] op_sel:[0,1] op_sel_hi:[1,1]\n"
      "v_pk_mul_f32 v[58:59], v[58:59], v[196:197] op_sel:[0,1] op_sel_hi:[1,1]\n"
      "v_pk_mul_f32 v[60:61], v[60:61], v[196:197] op_sel:[0,1] op_sel_hi:[1,1]\n"
      "v_pk_mul_f32 v[62:63], v[62:63], v[196:197] op_sel:[0,1] op_sel_hi:[1,1]\n"
      "v_pk_mul_f32 v[64:65], v[64:65], v[196:197] op_sel:[0,1] op_sel_hi:[1,1]\n"
      "v_pk_mul_f32 v[66:67], v[66:67], v[196:197] op_sel:[0,1] op_sel_hi:[1,1]\n"
      "v_pk_mul_f32 v[68:69], v[68:69], v[196:197] op_sel:[0,1] op_sel_hi:[1,1]\n"
      "v_pk_mul_f32 v[70:71], v[70:71], v[196:197] op_sel:[0,1] op_sel_hi:[1,1]\n"
      "v_pk_mul_f32 v[72:73], v[72:73], v[196:197] op_sel:[0,1] op_sel_hi:[1,1]\n"
      "v_pk_mul_f32 v[74:75], v[74:75], v[196:197] op_sel:[0,1] op_sel_hi:[1,1]\n"
      "v_pk_mul_f32 v[76:77], v[76:77], v[196:197] op_sel:[0,1] op_sel_hi:[1,1]\n"
      "v_pk_mul_f32 v[78:79], v[78:79], v[196:197] op_sel:[0,1] op_sel_hi:[1,1]\n"
      "v_pk_mul_f32 v[80:81], v[80:81], v[196:197] op_sel:[0,1] op_sel_hi:[1,1]\n"
      "v_pk_mul_f32 v[82:83], v[82:83], v[196:197] op_sel:[0,1] op_sel_hi:[1,1]\n"
      "v_pk_mul_f32 v[84:85], v[84:85], v[196:197] op_sel:[0,1] op_sel_hi:[1,1]\n"
      "v_pk_mul_f32 v[86:87], v[86:87], v[196:197] op_sel:[0,1] op_sel_hi:[1,1]\n"
      "v_pk_mul_f32 v[88:89], v[88:89], v[196:197] op_sel:[0,1] op_sel_hi:[1,1]\n"
      "v_pk_mul_f32 v[90:91], v[90:91], v[196:197] op_sel:[0,1] op_sel_hi:[1,1]\n"
      "v_pk_mul_f32 v[92:93], v[92:93], v[196:197] op_sel:[0,1] op_sel_hi:[1,1]\n"
      "v_pk_mul_f32 v[94:95], v[94:95], v[196:197] op_sel:[0,1] op_sel_hi:[1,1]\n"
      "v_pk_mul_f32 v[96:97], v[96:97], v[196:197] op_sel:[0,1] op_sel_hi:[1,1]\n"
      "v_pk_mul_f32 v[98:99], v[98:99], v[196:197] op_sel:[0,1] op_sel_hi:[1,1]\n"
      "v_pk_mul_f32 v[100:101], v[100:101], v[196:197] op_sel:[0,1] op_sel_hi:[1,1]\n"
      "v_pk_mul_f32 v[102:103], v[102:103], v[196:197] op_sel:[0,1] op_sel_hi:[1,1]\n"
      "v_pk_mul_f32 v[104:105], v[104:105], v[196:197] op_sel:[0,1] op_sel_hi:[1,1]\n"
      "v_pk_mul_f32 v[106:107], v[106:107], v[196:197] op_sel:[0,1] op_sel_hi:[1,1]\n"
      "v_pk_mul_f32 v[108:109], v[108:109], v[196:197] op_sel:[0,1] op_sel_hi:[1,1]\n"
      "v_pk_mul_f32 v[110:111], v[110:111], v[196:197] op_sel:[0,1] op_sel_hi:[1,1]\n"
      "v_pk_mul_f32 v[112:113], v[112:113], v[196:197] op_sel:[0,1] op_sel_hi:[1,1]\n"
      "v_pk_mul_f32 v[114:115], v[114:115], v[196:197] op_sel:[0,1] op_sel_hi:[1,1]\n"
      "v_pk_mul_f32 v[116:117], v[116:117], v[196:197] op_sel:[0,1] op_sel_hi:[1,1]\n"
      "v_pk_mul_f32 v[118:119], v[118:119], v[196:197] op_sel:[0,1] op_sel_hi:[1,1]\n"
      "v_pk_mul_f32 v[120:121], v[120:121], v[196:197] op_sel:[0,1] op_sel_hi:[1,1]\n"
      "v_pk_mul_f32 v[122:123], v[122:123], v[196:197] op_sel:[0,1] op_sel_hi:[1,1]\n"
      "v_pk_mul_f32 v[124:125], v[124:125], v[196:197] op_sel:[0,1] op_sel_hi:[1,1]\n"
      "v_pk_mul_f32 v[126:127], v[126:127], v[196:197] op_sel:[0,1] op_sel_hi:[1,1]\n"
      "s_mov_b32 s50, 0\n"
      "s_nop 1\n"
      "s_branch LcA_RRB31_%=\n"
      "Lp1_%=:\n"
      "s_mov_b32 s40, %7\n"
      "s_add_u32 s41, %7, 65536\n"
      "s_add_u32 s42, %7, 131072\n"
      "s_mov_b32 s43, %8\n"
      "s_mov_b32 s44, %8\n"
      "s_lshl_b32 s45, %9, 19\n"
      "s_lshl_b32 s46, %9, 7\n"
      "s_lshl_b32 s47, %10, 7\n"
      "s_add_u32 s48, s47, 114688\n"
      "s_lshl_b32 s49, %10, 12\n"
      "s_mov_b32 s50, 0\n"
      "s_mov_b32 s52, 0x3db8aa3b\n"
      "s_mov_b32 s53, 0x3db8aa3b\n"
      "s_mov_b32 s72, 0x9e3779b1\n"
      "v_mov_b32 v218, %18\n"
      "v_mov_b32 v219, %19\n"
      "v_xor_b32 v220, 64, %19\n"
      "v_add_u32 v220, 7168, v220\n"
      "v_mov_b32 v221, %19\n"
      "v_add_u32 v221, 14336, v221\n"
      "v_xor_b32 v222, 64, %19\n"
      "v_add_u32 v222, 21504, v222\n"
      "s_add_u32 s46, s46, 128\n"
      "s_cmp_eq_u32 s46, 896\n"
      "s_cselect_b32 s46, 0, s46\n"
      "s_add_u32 s45, s45, 0x40000\n"
      "s_cmp_eq_u32 s45, 0x380000\n"
      "s_cselect_b32 s45, 0, s45\n"
      "s_add_u32 s45, s45, 0x40000\n"
      "s_cmp_eq_u32 s45, 0x380000\n"
      "s_cselect_b32 s45, 0, s45\n"
      "s_add_u32 s46, s46, 128\n"
      "s_cmp_eq_u32 s46, 896\n"
      "s_cselect_b32 s46, 0, s46\n"
      "v_mov_b32 v233, 0\n"
      "v_mov_b32 v216, %16\n"
      "v_mov_b32 v217, %17\n"
      "v_mov_b32 v223, %20\n"
      "v_add_u32 v224, 7168, %20\n"
      "v_add_u32 v225, 14336, %20\n"
      "v_add_u32 v226, 21504, %20\n"
      "s_waitcnt vmcnt(8) lgkmcnt(0)\n"
      "s_barrier\n"
      "s_waitcnt vmcnt(4) lgkmcnt(0)\n"
      "s_barrier\n"
      "s_add_u32 m0, s41, 32768\n"
      "s_nop 0\n"
      "buffer_load_dwordx4 v218, %4, s45 offen offset:0 lds\n"
      "s_add_u32 m0, s41, 33664\n"
      "s_nop 0\n"
      "buffer_load_dwordx4 v218, %4, s45 offen offset:128 lds\n"
      "s_add_u32 m0, s41, 34560\n"
      "s_nop 0\n"
      "buffer_load_dwordx4 v218, %4, s45 offen offset:256 lds\n"
      "s_add_u32 m0, s41, 35456\n"
      "s_nop 0\n"
      "buffer_load_dwordx4 v218, %4, s45 offen offset:384 lds\n"
      "s_add_u32 s45, s45, 0x40000\n"
      "s_cmp_eq_u32 s45, 0x380000\n"
      "s_cselect_b32 s45, 0, s45\n"
      "s_add_u32 m0, s41, 49152\n"
      "s_nop 0\n"
      "buffer_load_dwordx4 v218, %4, s45 offen offset:0 lds\n"
      "s_add_u32 m0, s41, 50048\n"
      "s_nop 0\n"
      "buffer_load_dwordx4 v218, %4, s45 offen offset:128 lds\n"
      "s_add_u32 m0, s41, 50944\n"
      "s_nop 0\n"
      "buffer_load_dwordx4 v218, %4, s45 offen offset:256 lds\n"
      "s_add_u32 m0, s41, 51840\n"
      "s_nop 0\n"
      "buffer_load_dwordx4 v218, %4, s45 offen offset:384 lds\n"
      "s_add_u32 s45, s45, 0x40000\n"
      "s_cmp_eq_u32 s45, 0x380000\n"
      "s_cselect_b32 s45, 0, s45\n"
      "s_waitcnt vmcnt(4) lgkmcnt(0)\n"
      "s_barrier\n"
      "s_add_u32 m0, s41, 0\n"
      "s_nop 0\n"
      "buffer_load_dwordx4 v218, %4, s45 offen offset:0 lds\n"
      "s_add_u32 m0, s41, 896\n"
      "s_nop 0\n"
      "buffer_load_dwordx4 v218, %4, s45 offen offset:128 lds\n"
      "s_add_u32 m0, s41, 1792\n"
      "s_nop 0\n"
      "buffer_load_dwordx4 v218, %4, s45 offen offset:256 lds\n"
      "s_add_u32 m0, s41, 2688\n"
      "s_nop 0\n"
      "buffer_load_dwordx4 v218, %4, s45 offen offset:384 lds\n"
      "s_add_u32 s45, s45, 0x40000\n"
      "s_cmp_eq_u32 s45, 0x380000\n"
      "s_cselect_b32 s45, 0, s45\n"
      "s_add_u32 m0, s42, 0\n"
      "s_nop 0\n"
      "buffer_load_dwordx4 v219, %3, s46 offen lds\n"
      "s_add_u32 m0, s42, 1024\n"
      "s_nop 0\n"
      "buffer_load_dwordx4 v220, %3, s46 offen lds\n"
      "s_add_u32 m0, s42, 2048\n"
      "s_nop 0\n"
      "buffer_load_dwordx4 v221, %3, s46 offen lds\n"
      "s_add_u32 m0, s42, 3072\n"
      "s_nop 0\n"
      "buffer_load_dwordx4 v222, %3, s46 offen lds\n"
      "s_add_u32 s46, s46, 128\n"
      "s_cmp_eq_u32 s46, 896\n"
      "s_cselect_b32 s46, 0, s46\n"
      "s_waitcnt vmcnt(8) lgkmcnt(0)\n"
      "s_barrier\n"
      "s_add_u32 m0, s41, 16384\n"
      "s_nop 0\n"
      "buffer_load_dwordx4 v218, %4, s45 offen offset:0 lds\n"
      "s_add_u32 m0, s41, 17280\n"
      "s_nop 0\n"
      "buffer_load_dwordx4 v218, %4, s45 offen offset:128 lds\n"
      "s_add_u32 m0, s41, 18176\n"
      "s_nop 0\n"
      "buffer_load_dwordx4 v218, %4, s45 offen offset:256 lds\n"
      "s_add_u32 m0, s41, 19072\n"
      "s_nop 0\n"
      "buffer_load_dwordx4 v218, %4, s45 offen offset:384 lds\n"
      "s_add_u32 s45, s45, 0x40000\n"
      "s_cmp_eq_u32 s45, 0x380000\n"
      "s_cselect_b32 s45, 0, s45\n"
      "s_add_u32 m0, s41, 32768\n"
      "s_nop 0\n"
      "buffer_load_dwordx4 v218, %4, s45 offen offset:0 lds\n"
      "s_add_u32 m0, s41, 33664\n"
      "s_nop 0\n"
      "buffer_load_dwordx4 v218, %4, s45 offen offset:128 lds\n"
      "s_add_u32 m0, s41, 34560\n"
      "s_nop 0\n"
      "buffer_load_dwordx4 v218, %4, s45 offen offset:256 lds\n"
      "s_add_u32 m0, s41, 35456\n"
      "s_nop 0\n"
      "buffer_load_dwordx4 v218, %4, s45 offen offset:384 lds\n"
      "s_add_u32 s45, s45, 0x40000\n"
      "s_cmp_eq_u32 s45, 0x380000\n"
      "s_cselect_b32 s45, 0, s45\n"
      "s_add_u32 m0, s40, 0\n"
      "s_nop 0\n"
      "buffer_load_dwordx4 v216, %1, s43 offen offset:0 lds\n"
      "s_add_u32 m0, s40, 896\n"
      "s_nop 0\n"
      "buffer_load_dwordx4 v216, %1, s43 offen offset:128 lds\n"
      "s_add_u32 m0, s40, 1792\n"
      "s_nop 0\n"
      "buffer_load_dwordx4 v216, %1, s43 offen offset:256 lds\n"
      "s_add_u32 m0, s40, 2688\n"
      "s_nop 0\n"
      "buffer_load_dwordx4 v216, %1, s43 offen offset:384 lds\n"
      "s_add_u32 s43, s43, 0x4000\n"
      "s_waitcnt vmcnt(12) lgkmcnt(0)\n"
      "s_barrier\n"
      "s_add_u32 m0, s41, 49152\n"
      "s_nop 0\n"
      "buffer_load_dwordx4 v218, %4, s45 offen offset:0 lds\n"
      "s_add_u32 m0, s41, 50048\n"
      "s_nop 0\n"
      "buffer_load_dwordx4 v218, %4, s45 offen offset:128 lds\n"
      "s_add_u32 m0, s41, 50944\n"
      "s_nop 0\n"
      "buffer_load_dwordx4 v218, %4, s45 offen offset:256 lds\n"
      "s_add_u32 m0, s41, 51840\n"
      "s_nop 0\n"
      "buffer_load_dwordx4 v218, %4, s45 offen offset:384 lds\n"
      "s_add_u32 s45, s45, 0x40000\n"
      "s_cmp_eq_u32 s45, 0x380000\n"
      "s_cselect_b32 s45, 0, s45\n"
      "s_add_u32 m0, s42, 16384\n"
      "s_nop 0\n"
      "buffer_load_dwordx4 v219, %3, s46 offen lds\n"
      "s_add_u32 m0, s42, 17408\n"
      "s_nop 0\n"
      "buffer_load_dwordx4 v220, %3, s46 offen lds\n"
      "s_add_u32 m0, s42, 18432\n"
      "s_nop 0\n"
      "buffer_load_dwordx4 v221, %3, s46 offen lds\n"
      "s_add_u32 m0, s42, 19456\n"
      "s_nop 0\n"
      "buffer_load_dwordx4 v222, %3, s46 offen lds\n"
      "s_add_u32 s46, s46, 128\n"
      "s_cmp_eq_u32 s46, 896\n"
      "s_cselect_b32 s46, 0, s46\n"
      "global_load_dword %0, v233, %12 sc1\n"
      "s_waitcnt vmcnt(17) lgkmcnt(0)\n"
      "s_barrier\n"
      "s_add_u32 m0, s41, 0\n"
      "s_nop 0\n"
      "buffer_load_dwordx4 v218, %4, s45 offen offset:0 lds\n"
      "s_add_u32 m0, s41, 896\n"
      "s_nop 0\n"
      "buffer_load_dwordx4 v218, %4, s45 offen offset:128 lds\n"
      "s_add_u32 m0, s41, 1792\n"
      "s_nop 0\n"
      "buffer_load_dwordx4 v218, %4, s45 offen offset:256 lds\n"
      "s_add_u32 m0, s41, 2688\n"
      "s_nop 0\n"
      "buffer_load_dwordx4 v218, %4, s45 offen offset:384 lds\n"
      "s_add_u32 s45, s45, 0x40000\n"
      "s_cmp_eq_u32 s45, 0x380000\n"
      "s_cselect_b32 s45, 0, s45\n"
      "s_add_u32 m0, s40, 16384\n"
      "s_nop 0\n"
      "buffer_load_dwordx4 v216, %1, s43 offen offset:0 lds\n"
      "s_add_u32 m0, s40, 17280\n"
      "s_nop 0\n"
      "buffer_load_dwordx4 v216, %1, s43 offen offset:128 lds\n"
      "s_add_u32 m0, s40, 18176\n"
      "s_nop 0\n"
      "buffer_load_dwordx4 v216, %1, s43 offen offset:256 lds\n"
      "s_add_u32 m0, s40, 19072\n"
      "s_nop 0\n"
      "buffer_load_dwordx4 v216, %1, s43 offen offset:384 lds\n"
      "s_add_u32 s43, s43, 0x4000\n"
      "s_waitcnt vmcnt(9) lgkmcnt(0)\n"
      "s_barrier\n"
      "s_add_u32 m0, s41, 16384\n"
      "s_nop 0\n"
      "buffer_load_dwordx4 v218, %4, s45 offen offset:0 lds\n"
      "s_add_u32 m0, s41, 17280\n"
      "s_nop 0\n"
      "buffer_load_dwordx4 v218, %4, s45 offen offset:128 lds\n"
      "s_add_u32 m0, s41, 18176\n"
      "s_nop 0\n"
      "buffer_load_dwordx4 v218, %4, s45 offen offset:256 lds\n"
      "s_add_u32 m0, s41, 19072\n"
      "s_nop 0\n"
      "buffer_load_dwordx4 v218, %4, s45 offen offset:384 lds\n"
      "s_add_u32 s45, s45, 0x40000\n"
      "s_cmp_eq_u32 s45, 0x380000\n"
      "s_cselect_b32 s45, 0, s45\n"
      "s_add_u32 m0, s42, 0\n"
      "s_nop 0\n"
      "buffer_load_dwordx4 v219, %3, s46 offen lds\n"
      "s_add_u32 m0, s42, 1024\n"
      "s_nop 0\n"
      "buffer_load_dwordx4 v220, %3, s46 offen lds\n"
      "s_add_u32 m0, s42, 2048\n"
      "s_nop 0\n"
      "buffer_load_dwordx4 v221, %3, s46 offen lds\n"
      "s_add_u32 m0, s42, 3072\n"
      "s_nop 0\n"
      "buffer_load_dwordx4 v222, %3, s46 offen lds\n"
      "s_add_u32 s46, s46, 128\n"
      "s_cmp_eq_u32 s46, 896\n"
      "s_cselect_b32 s46, 0, s46\n"
      "s_waitcnt vmcnt(17) lgkmcnt(0)\n"
      "s_barrier\n"
      "s_add_u32 m0, s41, 32768\n"
      "s_nop 0\n"
      "buffer_load_dwordx4 v218, %4, s45 offen offset:0 lds\n"
      "s_add_u32 m0, s41, 33664\n"
      "s_nop 0\n"
      "buffer_load_dwordx4 v218, %4, s45 offen offset:128 lds\n"
      "s_add_u32 m0, s41, 34560\n"
      "s_nop 0\n"
      "buffer_load_dwordx4 v218, %4, s45 offen offset:256 lds\n"
      "s_add_u32 m0, s41, 35456\n"
      "s_nop 0\n"
      "buffer_load_dwordx4 v218, %4, s45 offen offset:384 lds\n"
      "s_add_u32 s45, s45, 0x40000\n"
      "s_cmp_eq_u32 s45, 0x380000\n"
      "s_cselect_b32 s45, 0, s45\n"
      "s_add_u32 m0, s40, 32768\n"
      "s_nop 0\n"
      "buffer_load_dwordx4 v216, %1, s43 offen offset:0 lds\n"
      "s_add_u32 m0, s40, 33664\n"
      "s_nop 0\n"
      "buffer_load_dwordx4 v216, %1, s43 offen offset:128 lds\n"
      "s_add_u32 m0, s40, 34560\n"
      "s_nop 0\n"
      "buffer_load_dwordx4 v216, %1, s43 offen offset:256 lds\n"
      "s_add_u32 m0, s40, 35456\n"
      "s_nop 0\n"
      "buffer_load_dwordx4 v216, %1, s43 offen offset:384 lds\n"
      "s_add_u32 s43, s43, 0x4000\n"
      "s_waitcnt vmcnt(8) lgkmcnt(0)\n"
      "s_barrier\n"
      "s_add_u32 m0, s41, 49152\n"
      "s_nop 0\n"
      "buffer_load_dwordx4 v218, %4, s45 offen offset:0 lds\n"
      "s_add_u32 m0, s41, 50048\n"
      "s_nop 0\n"
      "buffer_load_dwordx4 v218, %4, s45 offen offset:128 lds\n"
      "s_add_u32 m0, s41, 50944\n"
      "s_nop 0\n"
      "buffer_load_dwordx4 v218, %4, s45 offen offset:256 lds\n"
      "s_add_u32 m0, s41, 51840\n"
      "s_nop 0\n"
      "buffer_load_dwordx4 v218, %4, s45 offen offset:384 lds\n"
      "s_add_u32 s45, s45, 0x40000\n"
      "s_cmp_eq_u32 s45, 0x380000\n"
      "s_cselect_b32 s45, 0, s45\n"
      "s_add_u32 m0, s42, 16384\n"
      "s_nop 0\n"
      "buffer_load_dwordx4 v219, %3, s46 offen lds\n"
      "s_add_u32 m0, s42, 17408\n"
      "s_nop 0\n"
      "buffer_load_dwordx4 v220, %3, s46 offen lds\n"
      "s_add_u32 m0, s42, 18432\n"
      "s_nop 0\n"
      "buffer_load_dwordx4 v221, %3, s46 offen lds\n"
      "s_add_u32 m0, s42, 19456\n"
      "s_nop 0\n"
      "buffer_load_dwordx4 v222, %3, s46 offen lds\n"
      "s_add_u32 s46, s46, 128\n"
      "s_cmp_eq_u32 s46, 896\n"
      "s_cselect_b32 s46, 0, s46\n"
      "s_waitcnt vmcnt(16) lgkmcnt(0)\n"
      "s_barrier\n"
      "s_add_u32 m0, s41, 0\n"
      "s_nop 0\n"
      "buffer_load_dwordx4 v218, %4, s45 offen offset:0 lds\n"
      "s_add_u32 m0, s41, 896\n"
      "s_nop 0\n"
      "buffer_load_dwordx4 v218, %4, s45 offen offset:128 lds\n"
      "s_add_u32 m0, s41, 1792\n"
      "s_nop 0\n"
      "buffer_load_dwordx4 v218, %4, s45 offen offset:256 lds\n"
      "s_add_u32 m0, s41, 2688\n"
      "s_nop 0\n"
      "buffer_load_dwordx4 v218, %4, s45 offen offset:384 lds\n"
      "s_add_u32 s45, s45, 0x40000\n"
      "s_cmp_eq_u32 s45, 0x380000\n"
      "s_cselect_b32 s45, 0, s45\n"
      "s_add_u32 m0, s40, 49152\n"
      "s_nop 0\n"
      "buffer_load_dwordx4 v216, %1, s43 offen offset:0 lds\n"
      "s_add_u32 m0, s40, 50048\n"
      "s_nop 0\n"
      "buffer_load_dwordx4 v216, %1, s43 offen offset:128 lds\n"
      "s_add_u32 m0, s40, 50944\n"
      "s_nop 0\n"
      "buffer_load_dwordx4 v216, %1, s43 offen offset:256 lds\n"
      "s_add_u32 m0, s40, 51840\n"
      "s_nop 0\n"
      "buffer_load_dwordx4 v216, %1, s43 offen offset:384 lds\n"
      "s_add_u32 s43, s43, 0x4000\n"
      "s_and_b32 s43, s43, 0x7ffff\n"
      "global_load_dword v235, v229, s[74:75]\n"
      "s_waitcnt vmcnt(9) lgkmcnt(0)\n"
      "s_barrier\n"
      "s_add_u32 m0, s41, 16384\n"
      "s_nop 0\n"
      "buffer_load_dwordx4 v218, %4, s45 offen offset:0 lds\n"
      "s_add_u32 m0, s41, 17280\n"
      "s_nop 0\n"
      "buffer_load_dwordx4 v218, %4, s45 offen offset:128 lds\n"
      "s_add_u32 m0, s41, 18176\n"
      "s_nop 0\n"
      "buffer_load_dwordx4 v218, %4, s45 offen offset:256 lds\n"
      "s_add_u32 m0, s41, 19072\n"
      "s_nop 0\n"
      "buffer_load_dwordx4 v218, %4, s45 offen offset:384 lds\n"
      "s_add_u32 s45, s45, 0x40000\n"
      "s_cmp_eq_u32 s45, 0x380000\n"
      "s_cselect_b32 s45, 0, s45\n"
      "s_add_u32 m0, s42, 0\n"
      "s_nop 0\n"
      "buffer_load_dwordx4 v219, %3, s46 offen lds\n"
      "s_add_u32 m0, s42, 1024\n"
      "s_nop 0\n"
      "buffer_load_dwordx4 v220, %3, s46 offen lds\n"
      "s_add_u32 m0, s42, 2048\n"
      "s_nop 0\n"
      "buffer_load_dwordx4 v221, %3, s46 offen lds\n"
      "s_add_u32 m0, s42, 3072\n"
      "s_nop 0\n"
      "buffer_load_dwordx4 v222, %3, s46 offen lds\n"
      "s_add_u32 s46, s46, 128\n"
      "s_cmp_eq_u32 s46, 896\n"
      "s_cselect_b32 s46, 0, s46\n"
      "s_waitcnt vmcnt(17) lgkmcnt(0)\n"
      "s_barrier\n"
      "s_add_u32 m0, s41, 32768\n"
      "s_nop 0\n"
      "buffer_load_dwordx4 v217, %2, s44 offen offset:0 lds\n"
      "s_add_u32 m0, s41, 33664\n"
      "s_nop 0\n"
      "buffer_load_dwordx4 v217, %2, s44 offen offset:128 lds\n"
      "s_add_u32 m0, s41, 34560\n"
      "s_nop 0\n"
      "buffer_load_dwordx4 v217, %2, s44 offen offset:256 lds\n"
      "s_add_u32 m0, s41, 35456\n"
      "s_nop 0\n"
      "buffer_load_dwordx4 v217, %2, s44 offen offset:384 lds\n"
      "s_add_u32 s44, s44, 0x4000\n"
      "s_waitcnt vmcnt(4) lgkmcnt(0)\n"
      "s_barrier\n"
      "s_add_u32 m0, s41, 49152\n"
      "s_nop 0\n"
      "buffer_load_dwordx4 v217, %2, s44 offen offset:0 lds\n"
      "s_add_u32 m0, s41, 50048\n"
      "s_nop 0\n"
      "buffer_load_dwordx4 v217, %2, s44 offen offset:128 lds\n"
      "s_add_u32 m0, s41, 50944\n"
      "s_nop 0\n"
      "buffer_load_dwordx4 v217, %2, s44 offen offset:256 lds\n"
      "s_add_u32 m0, s41, 51840\n"
      "s_nop 0\n"
      "buffer_load_dwordx4 v217, %2, s44 offen offset:384 lds\n"
      "s_add_u32 s44, s44, 0x4000\n"
      "s_waitcnt vmcnt(8) lgkmcnt(0)\n"
      "s_barrier\n"
      "s_add_u32 m0, s41, 0\n"
      "s_nop 0\n"
      "buffer_load_dwordx4 v217, %2, s44 offen offset:0 lds\n"
      "s_add_u32 m0, s41, 896\n"
      "s_nop 0\n"
      "buffer_load_dwordx4 v217, %2, s44 offen offset:128 lds\n"
      "s_add_u32 m0, s41, 1792\n"
      "s_nop 0\n"
      "buffer_load_dwordx4 v217, %2, s44 offen offset:256 lds\n"
      "s_add_u32 m0, s41, 2688\n"
      "s_nop 0\n"
      "buffer_load_dwordx4 v217, %2, s44 offen offset:384 lds\n"
      "s_add_u32 s44, s44, 0x4000\n"
      "s_waitcnt vmcnt(12) lgkmcnt(0)\n"
      "s_barrier\n"
      "s_cmp_lg_u32 %14, 0\n"
      "s_cbranch_scc1 LdA_cf_%=\n"
      "LdA_cf_%=:\n"
      "s_waitcnt vmcnt(8) lgkmcnt(0)\n"
      "s_barrier\n"
      "s_add_u32 m0, s40, 0\n"
      "s_nop 0\n"
      "buffer_load_dwordx4 v216, %1, s43 offen offset:0 lds\n"
      "s_add_u32 m0, s40, 896\n"
      "s_nop 0\n"
      "buffer_load_dwordx4 v216, %1, s43 offen offset:128 lds\n"
      "s_add_u32 m0, s40, 1792\n"
      "s_nop 0\n"
      "buffer_load_dwordx4 v216, %1, s43 offen offset:256 lds\n"
      "s_add_u32 m0, s40, 2688\n"
      "s_nop 0\n"
      "buffer_load_dwordx4 v216, %1, s43 offen offset:384 lds\n"
      "s_add_u32 s43, s43, 0x4000\n"
      "s_add_u32 m0, s41, 16384\n"
      "s_nop 0\n"
      "buffer_load_dwordx4 v217, %2, s44 offen offset:0 lds\n"
      "s_add_u32 m0, s41, 17280\n"
      "s_nop 0\n"
      "buffer_load_dwordx4 v217, %2, s44 offen offset:128 lds\n"
      "s_add_u32 m0, s41, 18176\n"
      "s_nop 0\n"
      "buffer_load_dwordx4 v217, %2, s44 offen offset:256 lds\n"
      "s_add_u32 m0, s41, 19072\n"
      "s_nop 0\n"
      "buffer_load_dwordx4 v217, %2, s44 offen offset:384 lds\n"
      "s_add_u32 s44, s44, 0x4000\n"
      "s_and_b32 s44, s44, 0x7ffff\n"
      "s_waitcnt vmcnt(12) lgkmcnt(0)\n"
      "s_barrier\n"
      "s_add_u32 m0, s40, 16384\n"
      "s_nop 0\n"
      "buffer_load_dwordx4 v216, %1, s43 offen offset:0 lds\n"
      "global_load_dword v235, v231, s[70:71]\n"
      "s_add_u32 m0, s40, 17280\n"
      "s_nop 0\n"
      "buffer_load_dwordx4 v216, %1, s43 offen offset:128 lds\n"
      "s_add_u32 m0, s40, 18176\n"
      "s_nop 0\n"
      "buffer_load_dwordx4 v216, %1, s43 offen offset:256 lds\n"
      "s_add_u32 m0, s40, 19072\n"
      "s_nop 0\n"
      "buffer_load_dwordx4 v216, %1, s43 offen offset:384 lds\n"
      "s_add_u32 s43, s43, 0x4000\n"
      "s_add_u32 m0, s41, 32768\n"
      "s_nop 0\n"
      "buffer_load_dwordx4 v217, %2, s44 offen offset:0 lds\n"
      "s_add_u32 m0, s41, 33664\n"
      "s_nop 0\n"
      "buffer_load_dwordx4 v217, %2, s44 offen offset:128 lds\n"
      "s_add_u32 m0, s41, 34560\n"
      "s_nop 0\n"
      "buffer_load_dwordx4 v217, %2, s44 offen offset:256 lds\n"
      "s_add_u32 m0, s41, 35456\n"
      "s_nop 0\n"
      "buffer_load_dwordx4 v217, %2, s44 offen offset:384 lds\n"
      "s_add_u32 s44, s44, 0x4000\n"
      "s_waitcnt vmcnt(17) lgkmcnt(0)\n"
      "s_barrier\n"
      "s_add_u32 m0, s40, 32768\n"
      "s_nop 0\n"
      "buffer_load_dwordx4 v216, %1, s43 offen offset:0 lds\n"
      "s_add_u32 m0, s40, 33664\n"
      "s_nop 0\n"
      "buffer_load_dwordx4 v216, %1, s43 offen offset:128 lds\n"
      "s_add_u32 m0, s40, 34560\n"
      "s_nop 0\n"
      "buffer_load_dwordx4 v216, %1, s43 offen offset:256 lds\n"
      "s_add_u32 m0, s40, 35456\n"
      "s_nop 0\n"
      "buffer_load_dwordx4 v216, %1, s43 offen offset:384 lds\n"
      "s_add_u32 s43, s43, 0x4000\n"
      "s_add_u32 m0, s41, 49152\n"
      "s_nop 0\n"
      "buffer_load_dwordx4 v217, %2, s44 offen offset:0 lds\n"
      "s_add_u32 m0, s41, 50048\n"
      "s_nop 0\n"
      "buffer_load_dwordx4 v217, %2, s44 offen offset:128 lds\n"
      "s_add_u32 m0, s41, 50944\n"
      "s_nop 0\n"
      "buffer_load_dwordx4 v217, %2, s44 offen offset:256 lds\n"
      "s_add_u32 m0, s41, 51840\n"
      "s_nop 0\n"
      "buffer_load_dwordx4 v217, %2, s44 offen offset:384 lds\n"
      "s_add_u32 s44, s44, 0x4000\n"
      "s_waitcnt vmcnt(17) lgkmcnt(0)\n"
      "s_barrier\n"
      "s_add_u32 m0, s40, 49152\n"
      "s_nop 0\n"
      "buffer_load_dwordx4 v216, %1, s43 offen offset:0 lds\n"
      "s_add_u32 m0, s40, 50048\n"
      "s_nop 0\n"
      "buffer_load_dwordx4 v216, %1, s43 offen offset:128 lds\n"
      "s_add_u32 m0, s40, 50944\n"
      "s_nop 0\n"
      "buffer_load_dwordx4 v216, %1, s43 offen offset:256 lds\n"
      "s_mov_b32 s51, 6\n"
      "LdA_loop_%=:\n"
      "s_add_u32 m0, s40, 51840\n"
      "s_nop 0\n"
      "buffer_load_dwordx4 v216, %1, s43 offen offset:384 lds\n"
      "s_add_u32 s43, s43, 0x4000\n"
      "s_and_b32 s43, s43, 0x7ffff\n"
      "s_add_u32 m0, s41, 0\n"
      "s_nop 0\n"
      "buffer_load_dwordx4 v217, %2, s44 offen offset:0 lds\n"
      "s_add_u32 m0, s41, 896\n"
      "s_nop 0\n"
      "buffer_load_dwordx4 v217, %2, s44 offen offset:128 lds\n"
      "s_add_u32 m0, s41, 1792\n"
      "s_nop 0\n"
      "buffer_load_dwordx4 v217, %2, s44 offen offset:256 lds\n"
      "s_add_u32 m0, s41, 2688\n"
      "s_nop 0\n"
      "buffer_load_dwordx4 v217, %2, s44 offen offset:384 lds\n"
      "s_add_u32 s44, s44, 0x4000\n"
      "s_waitcnt vmcnt(16) lgkmcnt(0)\n"
      "s_barrier\n"
      "s_add_u32 m0, s40, 0\n"
      "s_nop 0\n"
      "buffer_load_dwordx4 v216, %1, s43 offen offset:0 lds\n"
      "s_add_u32 m0, s40, 896\n"
      "s_nop 0\n"
      "buffer_load_dwordx4 v216, %1, s43 offen offset:128 lds\n"
      "s_add_u32 m0, s40, 1792\n"
      "s_nop 0\n"
      "buffer_load_dwordx4 v216, %1, s43 offen offset:256 lds\n"
      "s_add_u32 m0, s40, 2688\n"
      "s_nop 0\n"
      "buffer_load_dwordx4 v216, %1, s43 offen offset:384 lds\n"
      "s_add_u32 s43, s43, 0x4000\n"
      "s_add_u32 m0, s41, 16384\n"
      "s_nop 0\n"
      "buffer_load_dwordx4 v217, %2, s44 offen offset:0 lds\n"
      "s_add_u32 m0, s41, 17280\n"
      "s_nop 0\n"
      "buffer_load_dwordx4 v217, %2, s44 offen offset:128 lds\n"
      "s_add_u32 m0, s41, 18176\n"
      "s_nop 0\n"
      "buffer_load_dwordx4 v217, %2, s44 offen offset:256 lds\n"
      "s_add_u32 m0, s41, 19072\n"
      "s_nop 0\n"
      "buffer_load_dwordx4 v217, %2, s44 offen offset:384 lds\n"
      "s_add_u32 s44, s44, 0x4000\n"
      "s_and_b32 s44, s44, 0x7ffff\n"
      "s_waitcnt vmcnt(16) lgkmcnt(0)\n"
      "s_barrier\n"
      "s_add_u32 m0, s40, 16384\n"
      "s_nop 0\n"
      "buffer_load_dwordx4 v216, %1, s43 offen offset:0 lds\n"
      "global_load_dword v235, v231, s[70:71]\n"
      "s_add_u32 m0, s40, 17280\n"
      "s_nop 0\n"
      "buffer_load_dwordx4 v216, %1, s43 offen offset:128 lds\n"
      "s_add_u32 m0, s40, 18176\n"
      "s_nop 0\n"
      "buffer_load_dwordx4 v216, %1, s43 offen offset:256 lds\n"
      "s_add_u32 m0, s40, 19072\n"
      "s_nop 0\n"
      "buffer_load_dwordx4 v216, %1, s43 offen offset:384 lds\n"
      "s_add_u32 s43, s43, 0x4000\n"
      "s_add_u32 m0, s41, 32768\n"
      "s_nop 0\n"
      "buffer_load_dwordx4 v217, %2, s44 offen offset:0 lds\n"
      "s_add_u32 m0, s41, 33664\n"
      "s_nop 0\n"
      "buffer_load_dwordx4 v217, %2, s44 offen offset:128 lds\n"
      "s_add_u32 m0, s41, 34560\n"
      "s_nop 0\n"
      "buffer_load_dwordx4 v217, %2, s44 offen offset:256 lds\n"
      "s_add_u32 m0, s41, 35456\n"
      "s_nop 0\n"
      "buffer_load_dwordx4 v217, %2, s44 offen offset:384 lds\n"
      "s_add_u32 s44, s44, 0x4000\n"
      "s_waitcnt vmcnt(17) lgkmcnt(0)\n"
      "s_barrier\n"
      "s_add_u32 m0, s40, 32768\n"
      "s_nop 0\n"
      "buffer_load_dwordx4 v216, %1, s43 offen offset:0 lds\n"
      "s_add_u32 m0, s40, 33664\n"
      "s_nop 0\n"
      "buffer_load_dwordx4 v216, %1, s43 offen offset:128 lds\n"
      "s_add_u32 m0, s40, 34560\n"
      "s_nop 0\n"
      "buffer_load_dwordx4 v216, %1, s43 offen offset:256 lds\n"
      "s_add_u32 m0, s40, 35456\n"
      "s_nop 0\n"
      "buffer_load_dwordx4 v216, %1, s43 offen offset:384 lds\n"
      "s_add_u32 s43, s43, 0x4000\n"
      "s_add_u32 m0, s41, 49152\n"
      "s_nop 0\n"
      "buffer_load_dwordx4 v217, %2, s44 offen offset:0 lds\n"
      "s_add_u32 m0, s41, 50048\n"
      "s_nop 0\n"
      "buffer_load_dwordx4 v217, %2, s44 offen offset:128 lds\n"
      "s_add_u32 m0, s41, 50944\n"
      "s_nop 0\n"
      "buffer_load_dwordx4 v217, %2, s44 offen offset:256 lds\n"
      "s_add_u32 m0, s41, 51840\n"
      "s_nop 0\n"
      "buffer_load_dwordx4 v217, %2, s44 offen offset:384 lds\n"
      "s_add_u32 s44, s44, 0x4000\n"
      "s_waitcnt vmcnt(17) lgkmcnt(0)\n"
      "s_barrier\n"
      "s_add_u32 m0, s40, 49152\n"
      "s_nop 0\n"
      "buffer_load_dwordx4 v216, %1, s43 offen offset:0 lds\n"
      "s_add_u32 m0, s40, 50048\n"
      "s_nop 0\n"
      "buffer_load_dwordx4 v216, %1, s43 offen offset:128 lds\n"
      "s_add_u32 m0, s40, 50944\n"
      "s_nop 0\n"
      "buffer_load_dwordx4 v216, %1, s43 offen offset:256 lds\n"
      "s_sub_u32 s51, s51, 1\n"
      "s_cmp_lg_u32 s51, 0\n"
      "s_cbranch_scc1 LdA_loop_%=\n"
      "s_add_u32 m0, s40, 51840\n"
      "s_nop 0\n"
      "buffer_load_dwordx4 v216, %1, s43 offen offset:384 lds\n"
      "s_add_u32 s43, s43, 0x4000\n"
      "s_and_b32 s43, s43, 0x7ffff\n"
      "s_add_u32 m0, s41, 0\n"
      "s_nop 0\n"
      "buffer_load_dwordx4 v217, %2, s44 offen offset:0 lds\n"
      "s_add_u32 m0, s41, 896\n"
      "s_nop 0\n"
      "buffer_load_dwordx4 v217, %2, s44 offen offset:128 lds\n"
      "s_add_u32 m0, s41, 1792\n"
      "s_nop 0\n"
      "buffer_load_dwordx4 v217, %2, s44 offen offset:256 lds\n"
      "s_add_u32 m0, s41, 2688\n"
      "s_nop 0\n"
      "buffer_load_dwordx4 v217, %2, s44 offen offset:384 lds\n"
      "s_add_u32 s44, s44, 0x4000\n"
      "s_waitcnt vmcnt(16) lgkmcnt(0)\n"
      "s_barrier\n"
      "s_add_u32 m0, s40, 0\n"
      "s_nop 0\n"
      "buffer_load_dwordx4 v223, %5, s47 offen lds\n"
      "s_add_u32 m0, s40, 1024\n"
      "s_nop 0\n"
      "buffer_load_dwordx4 v224, %5, s47 offen lds\n"
      "s_add_u32 m0, s40, 2048\n"
      "s_nop 0\n"
      "buffer_load_dwordx4 v225, %5, s47 offen lds\n"
      "s_add_u32 m0, s40, 3072\n"
      "s_nop 0\n"
      "buffer_load_dwordx4 v226, %5, s47 offen lds\n"
      "s_add_u32 s47, s47, 128\n"
      "s_cmp_eq_u32 s47, 896\n"
      "s_cselect_b32 s47, 0, s47\n"
      "s_add_u32 m0, s41, 16384\n"
      "s_nop 0\n"
      "buffer_load_dwordx4 v217, %2, s44 offen offset:0 lds\n"
      "s_add_u32 m0, s41, 17280\n"
      "s_nop 0\n"
      "buffer_load_dwordx4 v217, %2, s44 offen offset:128 lds\n"
      "s_add_u32 m0, s41, 18176\n"
      "s_nop 0\n"
      "buffer_load_dwordx4 v217, %2, s44 offen offset:256 lds\n"
      "s_add_u32 m0, s41, 19072\n"
      "s_nop 0\n"
      "buffer_load_dwordx4 v217, %2, s44 offen offset:384 lds\n"
      "s_add_u32 s44, s44, 0x4000\n"
      "s_and_b32 s44, s44, 0x7ffff\n"
      "s_waitcnt vmcnt(16) lgkmcnt(0)\n"
      "s_barrier\n"
      "s_add_u32 m0, s40, 16384\n"
      "s_nop 0\n"
      "buffer_load_dwordx4 v223, %5, s47 offen lds\n"
      "s_add_u32 m0, s40, 17408\n"
      "s_nop 0\n"
      "buffer_load_dwordx4 v224, %5, s47 offen lds\n"
      "s_add_u32 m0, s40, 18432\n"
      "s_nop 0\n"
      "buffer_load_dwordx4 v225, %5, s47 offen lds\n"
      "s_add_u32 m0, s40, 19456\n"
      "s_nop 0\n"
      "buffer_load_dwordx4 v226, %5, s47 offen lds\n"
      "s_add_u32 s47, s47, 128\n"
      "s_cmp_eq_u32 s47, 896\n"
      "s_cselect_b32 s47, 0, s47\n"
      "s_add_u32 m0, s41, 32768\n"
      "s_nop 0\n"
      "buffer_load_dwordx4 v223, %5, s48 offen lds\n"
      "s_add_u32 m0, s41, 33792\n"
      "s_nop 0\n"
      "buffer_load_dwordx4 v224, %5, s48 offen lds\n"
      "s_add_u32 m0, s41, 34816\n"
      "s_nop 0\n"
      "buffer_load_dwordx4 v225, %5, s48 offen lds\n"
      "s_add_u32 m0, s41, 35840\n"
      "s_nop 0\n"
      "buffer_load_dwordx4 v226, %5, s48 offen lds\n"
      "s_add_u32 s48, s48, 128\n"
      "s_cmp_eq_u32 s48, 115584\n"
      "s_cselect_b32 s48, 114688, s48\n"
      "s_waitcnt vmcnt(16) lgkmcnt(0)\n"
      "s_barrier\n"
      "s_add_u32 m0, s40, 32768\n"
      "s_nop 0\n"
      "buffer_load_dwordx4 v223, %5, s47 offen lds\n"
      "s_add_u32 m0, s40, 33792\n"
      "s_nop 0\n"
      "buffer_load_dwordx4 v224, %5, s47 offen lds\n"
      "s_add_u32 m0, s40, 34816\n"
      "s_nop 0\n"
      "buffer_load_dwordx4 v225, %5, s47 offen lds\n"
      "s_add_u32 m0, s40, 35840\n"
      "s_nop 0\n"
      "buffer_load_dwordx4 v226, %5, s47 offen lds\n"
      "s_add_u32 s47, s47, 128\n"
      "s_cmp_eq_u32 s47, 896\n"
      "s_cselect_b32 s47, 0, s47\n"
      "s_add_u32 m0, s41, 49152\n"
      "s_nop 0\n"
      "buffer_load_dwordx4 v223, %5, s48 offen lds\n"
      "s_add_u32 m0, s41, 50176\n"
      "s_nop 0\n"
      "buffer_load_dwordx4 v224, %5, s48 offen lds\n"
      "s_add_u32 m0, s41, 51200\n"
      "s_nop 0\n"
      "buffer_load_dwordx4 v225, %5, s48 offen lds\n"
      "s_add_u32 m0, s41, 52224\n"
      "s_nop 0\n"
      "buffer_load_dwordx4 v226, %5, s48 offen lds\n"
      "s_add_u32 s48, s48, 128\n"
      "s_cmp_eq_u32 s48, 115584\n"
      "s_cselect_b32 s48, 114688, s48\n"
      "s_waitcnt vmcnt(16) lgkmcnt(0)\n"
      "s_barrier\n"
      "s_add_u32 m0, s40, 49152\n"
      "s_nop 0\n"
      "buffer_load_dwordx4 v223, %5, s47 offen lds\n"
      "s_add_u32 m0, s40, 50176\n"
      "s_nop 0\n"
      "buffer_load_dwordx4 v224, %5, s47 offen lds\n"
      "s_add_u32 m0, s40, 51200\n"
      "s_nop 0\n"
      "buffer_load_dwordx4 v225, %5, s47 offen lds\n"
      "s_add_u32 m0, s40, 52224\n"
      "s_nop 0\n"
      "buffer_load_dwordx4 v226, %5, s47 offen lds\n"
      "s_add_u32 s47, s47, 128\n"
      "s_cmp_eq_u32 s47, 896\n"
      "s_cselect_b32 s47, 0, s47\n"
      "s_add_u32 m0, s41, 0\n"
      "s_nop 0\n"
      "buffer_load_dwordx4 v223, %5, s48 offen lds\n"
      "s_add_u32 m0, s41, 1024\n"
      "s_nop 0\n"
      "buffer_load_dwordx4 v224, %5, s48 offen lds\n"
      "s_add_u32 m0, s41, 2048\n"
      "s_nop 0\n"
      "buffer_load_dwordx4 v225, %5, s48 offen lds\n"
      "s_add_u32 m0, s41, 3072\n"
      "s_nop 0\n"
      "buffer_load_dwordx4 v226, %5, s48 offen lds\n"
      "s_add_u32 s48, s48, 128\n"
      "s_cmp_eq_u32 s48, 115584\n"
      "s_cselect_b32 s48, 114688, s48\n"
      "s_waitcnt vmcnt(24) lgkmcnt(0)\n"
      "s_barrier\n"
      "s_add_u32 m0, s41, 16384\n"
      "s_nop 0\n"
      "buffer_load_dwordx4 v223, %5, s48 offen lds\n"
      "s_add_u32 m0, s41, 17408\n"
      "s_nop 0\n"
      "buffer_load_dwordx4 v224, %5, s48 offen lds\n"
      "s_add_u32 m0, s41, 18432\n"
      "s_nop 0\n"
      "buffer_load_dwordx4 v225, %5, s48 offen lds\n"
      "s_add_u32 m0, s41, 19456\n"
      "s_nop 0\n"
      "buffer_load_dwordx4 v226, %5, s48 offen lds\n"
      "s_add_u32 s48, s48, 128\n"
      "s_cmp_eq_u32 s48, 115584\n"
      "s_cselect_b32 s48, 114688, s48\n"
      "s_waitcnt vmcnt(20) lgkmcnt(0)\n"
      "s_barrier\n"
      "s_add_u32 m0, s40, 0\n"
      "s_nop 0\n"
      "buffer_load_dwordx4 v223, %5, s47 offen lds\n"
      "s_add_u32 m0, s40, 1024\n"
      "s_nop 0\n"
      "buffer_load_dwordx4 v224, %5, s47 offen lds\n"
      "s_add_u32 m0, s40, 2048\n"
      "s_nop 0\n"
      "buffer_load_dwordx4 v225, %5, s47 offen lds\n"
      "s_add_u32 m0, s40, 3072\n"
      "s_nop 0\n"
      "buffer_load_dwordx4 v226, %5, s47 offen lds\n"
      "s_add_u32 s47, s47, 128\n"
      "s_cmp_eq_u32 s47, 896\n"
      "s_cselect_b32 s47, 0, s47\n"
      "s_waitcnt vmcnt(16) lgkmcnt(0)\n"
      "s_barrier\n"
      "s_add_u32 m0, s41, 32768\n"
      "s_nop 0\n"
      "buffer_load_dwordx4 v223, %5, s48 offen lds\n"
      "s_add_u32 m0, s41, 33792\n"
      "s_nop 0\n"
      "buffer_load_dwordx4 v224, %5, s48 offen lds\n"
      "s_add_u32 m0, s41, 34816\n"
      "s_nop 0\n"
      "buffer_load_dwordx4 v225, %5, s48 offen lds\n"
      "s_add_u32 m0, s41, 35840\n"
      "s_nop 0\n"
      "buffer_load_dwordx4 v226, %5, s48 offen lds\n"
      "s_add_u32 s48, s48, 128\n"
      "s_cmp_eq_u32 s48, 115584\n"
      "s_cselect_b32 s48, 114688, s48\n"
      "s_add_u32 m0, s40, 16384\n"
      "s_nop 0\n"
      "buffer_load_dwordx4 v223, %5, s47 offen lds\n"
      "s_add_u32 m0, s40, 17408\n"
      "s_nop 0\n"
      "buffer_load_dwordx4 v224, %5, s47 offen lds\n"
      "s_add_u32 m0, s40, 18432\n"
      "s_nop 0\n"
      "buffer_load_dwordx4 v225, %5, s47 offen lds\n"
      "s_add_u32 m0, s40, 19456\n"
      "s_nop 0\n"
      "buffer_load_dwordx4 v226, %5, s47 offen lds\n"
      "s_add_u32 s47, s47, 128\n"
      "s_cmp_eq_u32 s47, 896\n"
      "s_cselect_b32 s47, 0, s47\n"
      "s_waitcnt vmcnt(16) lgkmcnt(0)\n"
      "s_barrier\n"
      "s_add_u32 m0, s41, 49152\n"
      "s_nop 0\n"
      "buffer_load_dwordx4 v223, %5, s48 offen lds\n"
      "s_add_u32 m0, s41, 50176\n"
      "s_nop 0\n"
      "buffer_load_dwordx4 v224, %5, s48 offen lds\n"
      "s_add_u32 m0, s41, 51200\n"
      "s_nop 0\n"
      "buffer_load_dwordx4 v225, %5, s48 offen lds\n"
      "s_add_u32 m0, s41, 52224\n"
      "s_nop 0\n"
      "buffer_load_dwordx4 v226, %5, s48 offen lds\n"
      "s_add_u32 s48, s48, 128\n"
      "s_cmp_eq_u32 s48, 115584\n"
      "s_cselect_b32 s48, 114688, s48\n"
      "s_add_u32 m0, s40, 32768\n"
      "s_nop 0\n"
      "buffer_load_dwordx4 v223, %5, s47 offen lds\n"
      "s_add_u32 m0, s40, 33792\n"
      "s_nop 0\n"
      "buffer_load_dwordx4 v224, %5, s47 offen lds\n"
      "s_add_u32 m0, s40, 34816\n"
      "s_nop 0\n"
      "buffer_load_dwordx4 v225, %5, s47 offen lds\n"
      "s_add_u32 m0, s40, 35840\n"
      "s_nop 0\n"
      "buffer_load_dwordx4 v226, %5, s47 offen lds\n"
      "s_add_u32 s47, s47, 128\n"
      "s_cmp_eq_u32 s47, 896\n"
      "s_cselect_b32 s47, 0, s47\n"
      "s_waitcnt vmcnt(16) lgkmcnt(0)\n"
      "s_barrier\n"
      "s_add_u32 m0, s41, 0\n"
      "s_nop 0\n"
      "buffer_load_dwordx4 v223, %5, s48 offen lds\n"
      "s_add_u32 m0, s41, 1024\n"
      "s_nop 0\n"
      "buffer_load_dwordx4 v224, %5, s48 offen lds\n"
      "s_add_u32 m0, s41, 2048\n"
      "s_nop 0\n"
      "buffer_load_dwordx4 v225, %5, s48 offen lds\n"
      "s_add_u32 m0, s41, 3072\n"
      "s_nop 0\n"
      "buffer_load_dwordx4 v226, %5, s48 offen lds\n"
      "s_add_u32 s48, s48, 128\n"
      "s_cmp_eq_u32 s48, 115584\n"
      "s_cselect_b32 s48, 114688, s48\n"
      "s_waitcnt vmcnt(12) lgkmcnt(0)\n"
      "s_barrier\n"
      "s_waitcnt vmcnt(4) lgkmcnt(0)\n"
      "s_barrier\n"
      "s_waitcnt vmcnt(0) lgkmcnt(0)\n"
      "s_barrier\n"
      "s_cmp_lg_u32 %14, 0\n"
      "s_cbranch_scc1 LdA_ef_%=\n"
      "LdA_ef_%=:\n"
      "s_branch Lend_%=\n"
      "Lend_%=:\n"
      "s_waitcnt vmcnt(0) lgkmcnt(0)\n"
      : "=&v"(eb)
      : "s"(sK), "s"(sV), "s"(sX), "s"(sWq), "s"(sWo), "s"(sP), "s"(mA), "s"(kv0), "s"(c0), "s"(j0), "s"(prog), "s"(gflag + 32),
        "s"(rfl(idx * 4 + w)), "s"(w), "s"(gflag + 64 + hd),
        "v"(vK), "v"(vV), "v"(vW), "v"(vX0), "v"(vO0), "v"(aK0), "v"(aX0), "v"(aV0), "v"(aW0), "v"(aO0), "v"(vSt)
      : "v0", "v1", "v2", "v3", "v4", "v5", "v6", "v7", "v8", "v9", "v10", "v11", "v12", "v13", "v14", "v15", "v16", "v17", "v18", "v19", "v20", "v21", "v22", "v23", "v24", "v25", "v26", "v27", "v28", "v29", "v30", "v31", "v32", "v33", "v34", "v35", "v36", "v37", "v38", "v39", "v40", "v41", "v42", "v43", "v44", "v45", "v46", "v47", "v48", "v49", "v50", "v51", "v52", "v53", "v54", "v55", "v56", "v57", "v58", "v59", "v60", "v61", "v62", "v63", "v64", "v65", "v66", "v67", "v68", "v69", "v70", "v71", "v72", "v73", "v74", "v75", "v76", "v77", "v78", "v79", "v80", "v81", "v82", "v83", "v84", "v85", "v86", "v87", "v88", "v89", "v90", "v91", "v92", "v93", "v94", "v95", "v96", "v97", "v98", "v99", "v100", "v101", "v102", "v103", "v104", "v105", "v106", "v107", "v108", "v109", "v110", "v111", "v112", "v113", "v114", "v115", "v116", "v117", "v118", "v119", "v120", "v121", "v122", "v123", "v124", "v125", "v126", "v127", "v128", "v129", "v130", "v131", "v132", "v133", "v134", "v135", "v136", "v137", "v138", "v139", "v140", "v141", "v142", "v143", "v144", "v145", "v146", "v147", "v148", "v149", "v150", "v151", "v152", "v153", "v154", "v155", "v156", "v157", "v158", "v159", "v160", "v161", "v162", "v163", "v164", "v165", "v166", "v167", "v168", "v169", "v170", "v171", "v172", "v173", "v174", "v175", "v176", "v177", "v178", "v179", "v180", "v181", "v182", "v183", "v184", "v185", "v186", "v187", "v188", "v189", "v190", "v191", "v192", "v193", "v194", "v195", "v196", "v197", "v198", "v199", "v200", "v201", "v202", "v203", "v204", "v205", "v206", "v207", "v208", "v209", "v210", "v211", "v212", "v213", "v214", "v215", "v216", "v217", "v218", "v219", "v220", "v221", "v222", "v223", "v224", "v225", "v226", "v227", "v228", "v229", "v230", "v231", "v232", "v233", "v234", "v235",
      "a0", "a1", "a2", "a3", "a4", "a5", "a6", "a7", "a8", "a9", "a10", "a11", "a12", "a13", "a14", "a15", "a16", "a17", "a18", "a19", "a20", "a21", "a22", "a23", "a24", "a25", "a26", "a27", "a28", "a29", "a30", "a31", "a32", "a33", "a34", "a35", "a36", "a37", "a38", "a39", "a40", "a41", "a42", "a43", "a44", "a45", "a46", "a47", "a48", "a49", "a50", "a51", "a52", "a53", "a54", "a55", "a56", "a57", "a58", "a59", "a60", "a61", "a62", "a63", "a64", "a65", "a66", "a67", "a68", "a69", "a70", "a71", "a72", "a73", "a74", "a75", "a76", "a77", "a78", "a79", "a80", "a81", "a82", "a83", "a84", "a85", "a86", "a87", "a88", "a89", "a90", "a91", "a92", "a93", "a94", "a95", "a96", "a97", "a98", "a99", "a100", "a101", "a102", "a103", "a104", "a105", "a106", "a107", "a108", "a109", "a110", "a111", "a112", "a113", "a114", "a115", "a116", "a117", "a118", "a119", "a120", "a121", "a122", "a123", "a124", "a125", "a126", "a127", "a128", "a129", "a130", "a131", "a132", "a133", "a134", "a135", "a136", "a137", "a138", "a139", "a140", "a141", "a142", "a143", "a144", "a145", "a146", "a147", "a148", "a149", "a150", "a151", "a152", "a153", "a154", "a155", "a156", "a157", "a158", "a159", "a160", "a161", "a162", "a163", "a164", "a165", "a166", "a167", "a168", "a169", "a170", "a171", "a172", "a173", "a174", "a175", "a176", "a177", "a178", "a179", "a180", "a181", "a182", "a183", "a184", "a185", "a186", "a187", "a188", "a189", "a190", "a191", "a192", "a193", "a194", "a195", "a196", "a197", "a198", "a199", "a200", "a201", "a202", "a203", "a204", "a205", "a206", "a207", "a208", "a209", "a210", "a211", "a212", "a213", "a214", "a215", "a216", "a217", "a218", "a219", "a220", "a221", "a222", "a223", "a224", "a225", "a226", "a227", "a228", "a229", "a230", "a231", "a232", "a233", "a234", "a235", "a236", "a237", "a238", "a239", "a240", "a241", "a242", "a243", "a244", "a245", "a246", "a247", "a248", "a249", "a250", "a251", "a252", "a253", "a254", "a255",
      "s40", "s41", "s42", "s43", "s44", "s45", "s46", "s47", "s48", "s49", "s50", "s51", "s52", "s53", "s54", "s55", "s56", "s57", "s58", "s59", "s60", "s61", "s62", "s63", "s64", "s65", "s66", "s67", "s68", "s69", "s70", "s71", "s72", "s73", "s74", "s75", "vcc", "scc", "memory");

  __builtin_amdgcn_s_barrier();
  const unsigned epoch = eb * 2654435761u + 0x9E3779B9u;
  auto wait_flags = [&](gu32* f) {
    for (unsigned spins = 0; spins < (1u << 17); ++spins) {
      const unsigned v = __hip_atomic_load(f + (l & 15), __ATOMIC_RELAXED, __HIP_MEMORY_SCOPE_AGENT);
      if (__all(v == epoch)) break;
    }
  };
  if (w == 0) {
    if (l == 0) __hip_atomic_store(gflag + hd, epoch, __ATOMIC_RELAXED, __HIP_MEMORY_SCOPE_AGENT);
    wait_flags(gflag + 64);
  }
  __builtin_amdgcn_fence(__ATOMIC_ACQUIRE, "wavefront");
  __builtin_amdgcn_s_barrier();
  __builtin_amdgcn_fence(__ATOMIC_ACQUIRE, "wavefront");

  constexpr unsigned NE = 4, PE = 4 * NE, PL = 28 - PE;
  const unsigned nq = half ? 3 : 4;
  const unsigned elo = (nq * PE * hd) >> 4, ehi = (nq * PE * (hd + 1)) >> 4, llo = (nq * PL * hd) >> 4, lhi = (nq * PL * (hd + 1)) >> 4;
  const unsigned pe = elo + w, pl = llo + w;
  const bool hasE = pe < ehi, hasL = pl < lhi;
  const unsigned qE = pe / PE, rE = pe % PE, qL = pl / PL, rL = PE + pl % PL;
  const unsigned wE = ((j0 + (rE >> 2)) % 7) * 4 + (rE & 3), wL = ((j0 + (rL >> 2)) % 7) * 4 + (rL & 3);
  const __amdgpu_buffer_rsrc_t sAll = __builtin_amdgcn_make_buffer_rsrc((void*)part, (short)0, 1024 * PART_WAVE, SRD3);
  const __amdgpu_buffer_rsrc_t sOut = __builtin_amdgcn_make_buffer_rsrc((void*)out, (short)0, NB * SQ * DM * 2, SRD3);
  u32x4 v[2][16];
  auto load16 = [&](u32x4 (&dst)[16], bool has, unsigned ql, unsigned wi) {
    const unsigned base = has ? ql * PART_WAVE + wi * 1024 : 0x7f000000u;
#pragma unroll
    for (int h2 = 0; h2 < 16; ++h2) {
      const unsigned bid2 = ((((b & 3) * 4 + (h2 & 3)) * 2 + half) * 8 + (b >> 2) * 4 + (h2 >> 2));
      dst[h2] = __builtin_amdgcn_raw_buffer_load_b128(sAll, l * 16, rfl(base + bid2 * 4 * PART_WAVE), 16);
    }
  };
  load16(v[0], hasE, qE, wE);
  if (w == 0) {
    wait_flags(gflag);
    if (hd == 0 && l == 0) __hip_atomic_store(gflag + 32, epoch, __ATOMIC_RELAXED, __HIP_MEMORY_SCOPE_AGENT);
  }
  __builtin_amdgcn_fence(__ATOMIC_ACQUIRE, "wavefront");
  __builtin_amdgcn_s_barrier();
  __builtin_amdgcn_fence(__ATOMIC_ACQUIRE, "wavefront");
  load16(v[1], hasL, qL, wL);
#pragma unroll
  for (int k = 0; k < 2; ++k) {
    float a[8] = {};
#pragma unroll
    for (int h2 = 0; h2 < 16; ++h2)
#pragma unroll
      for (int e = 0; e < 4; ++e) { a[2 * e] += __uint_as_float(v[k][h2][e] << 16); a[2 * e + 1] += __uint_as_float(v[k][h2][e] & 0xffff0000u); }
    u32x4 o;
#pragma unroll
    for (int e = 0; e < 4; ++e) o[e] = cvtpk(a[2 * e], a[2 * e + 1]);
    const unsigned ql = k ? qL : qE, wi = k ? wL : wE;
    const unsigned row = b * SQ + 32 * (4 * half + ql) + (l & 31);
    const unsigned n0 = 64 * (wi >> 2) + 32 * ((wi >> 1) & 1) + 16 * (wi & 1) + 8 * (l >> 5);
    __builtin_amdgcn_raw_buffer_store_b128(o, sOut, (k ? hasL : hasE) ? (row * DM + n0) * 2 : 0x7f000000u, 0, 0);
  }
  asm volatile(".Lmha_end:");
}

extern "C" void kernel_launch(void* const* d_in, const int* in_sizes, int n_in, void* d_out, int out_size, void* d_ws, size_t ws_size, hipStream_t stream) {
  (void)hipFuncSetAttribute((const void*)mha_main, hipFuncAttributeMaxDynamicSharedMemorySize, LDS_BYTES);
  char* part = (char*)d_ws;
  unsigned* flags = (unsigned*)((char*)d_ws + (32u << 20));
  mha_main<<<256, 256, LDS_BYTES, stream>>>((const char*)d_in[0], (const char*)d_in[1], (const char*)d_in[2], (const char*)d_in[3], (const char*)d_in[4], part, flags, (char*)d_out);
}
```
